```python
import torch

def _softmax(x, axis=-1):
    return torch.softmax(x, dim=axis)

def _gelu(x):
    return torch.nn.functional.gelu(x, approximate='tanh')

def _silu(x):
    return torch.nn.functional.silu(x)

def _sigmoid(x):
    return torch.sigmoid(x)

def _layernorm(x, axis=-1, eps=1e-05):
    m = x.mean(dim=axis, keepdim=True)
    v = x.var(dim=axis, keepdim=True, unbiased=False)
    return (x - m) / torch.sqrt(v + eps)

def _scatter_add(tgt, idx, src, axis=0):
    return tgt.clone().index_add_(axis, idx, src)

def _segment_sum(src, seg, n):
    out = torch.zeros((n,) + src.shape[1:], dtype=src.dtype)
    return out.index_add_(0, seg, src)

def _pad(x, pw):
    flat = tuple((v for pair in reversed(pw) for v in pair))
    return torch.nn.functional.pad(x, flat)
kernel_name = 'adaLN_cond__softmax_full_mha__dense_activation_silu__prompt'

def setup_inputs():

    _w = lambda a: (lambda t: t.to(torch.bfloat16) if t.is_floating_point() else t)(torch.as_tensor(a))

    def _A_setup():
        B, S, D = (1, 2048, 448)
        return {'x': _w((lambda *s: torch.randn(*s, dtype=torch.bfloat16) * 0.05)(B, S, D)), 'wqkv': _w((lambda *s: torch.randn(*s, dtype=torch.bfloat16) * 0.05)(D, 3 * D)), 'wo': _w((lambda *s: torch.randn(*s, dtype=torch.bfloat16) * 0.05)(D, D))}
    _A_in = _A_setup()

    def _B_setup():
        B, S, D, F = (1, 2048, 448, 1792)
        return {'x': _w((lambda *s: torch.randn(*s, dtype=torch.bfloat16) * 0.05)(B, S, D)), 'w1': _w((lambda *s: torch.randn(*s, dtype=torch.bfloat16) * 3.2)(D, F) * D ** (-0.5)), 'w2': _w((lambda *s: torch.randn(*s, dtype=torch.bfloat16) * 0.2)(F, D) * F ** (-0.5))}
    _B_in = _B_setup()
    return {'A_x': _A_in['x'], 'A_wqkv': _A_in['wqkv'], 'A_wo': _A_in['wo'], 'B_x': _B_in['x'], 'B_w1': _B_in['w1'], 'B_w2': _B_in['w2'], 'c': _w((lambda *s: torch.randn(*s, dtype=torch.bfloat16) * 0.1)(1, 448) * 8.0), 'w_mod': _w((lambda *s: torch.randn(*s, dtype=torch.bfloat16) * 0.1)(448, 6 * 448) * 448 ** (-0.5) * 8.0)}

def reference(A_x, A_wqkv, A_wo, B_x, B_w1, B_w2, c, w_mod):

    def _A_ref(x, wqkv, wo):
        B, S, D = x.shape
        H = 16
        Dh = D // H
        qkv = (lambda x, *d: x.permute(*d))((x @ wqkv).reshape(B, S, 3, H, Dh), 2, 0, 3, 1, 4)
        q, k, v = (qkv[0], qkv[1], qkv[2])
        scale = Dh ** (-0.5)
        scores = torch.einsum('bhqd,bhkd->bhqk', q, k) * scale
        p = _softmax(scores, axis=-1)
        o = torch.einsum('bhqk,bhkd->bhqd', p, v)
        return (lambda x, *d: x.permute(*d))(o, 0, 2, 1, 3).reshape(B, S, D) @ wo

    def _B_ref(x, w1, w2):
        h = torch.einsum('bsd,df->bsf', x, w1)
        h = _silu(h)
        out = torch.einsum('bsf,fd->bsd', h, w2)
        return out
    x = A_x
    mod = (_silu(c) @ w_mod).reshape(c.shape[0], 6, 1, -1)
    sa, ca, ga, sb, cb, gb = (mod[:, 0].clone(), mod[:, 1].clone(), mod[:, 2].clone(), mod[:, 3].clone(), mod[:, 4].clone(), mod[:, 5].clone())
    h = _layernorm(x, axis=-1) * (1 + ca) + sa
    x = x + ga * _A_ref(h, A_wqkv, A_wo)
    h = _layernorm(x, axis=-1) * (1 + cb) + sb
    x = x + gb * _B_ref(h, B_w1, B_w2)
    return x
```

```cpp
#include <hip/hip_runtime.h>
#include <hip/hip_bf16.h>
#include <stdint.h>

typedef __attribute__((ext_vector_type(8))) short bf16x8;
typedef __attribute__((ext_vector_type(16))) float f32x16;
typedef __attribute__((ext_vector_type(4))) unsigned u32x4;
typedef __attribute__((ext_vector_type(2))) unsigned u32x2;
typedef __attribute__((ext_vector_type(4))) float f32x4;
typedef __attribute__((ext_vector_type(2))) float f32x2;
typedef __attribute__((ext_vector_type(2))) __bf16 bf16x2_t;
typedef unsigned short u16;

#define DI __device__ __forceinline__
#define MFMA32(a, b, c) __builtin_amdgcn_mfma_f32_32x32x16_bf16((a), (b), (c), 0, 0, 0)

constexpr int SEQ = 2048, DM = 448, NH = 16, DH = 28, FF = 1792;
constexpr int NTT = SEQ / 32;
constexpr int KS_D = DM / 16;
constexpr int KS_O = NH * 2;
constexpr int KS_F = FF / 16;
constexpr int FT_D = DM / 32;
constexpr int FT_F = FF / 32;

constexpr size_t OFF_MOD = 0;
constexpr size_t OFF_V1 = 16384;
constexpr size_t OFF_U2 = OFF_V1 + 8192;
constexpr size_t OFF_V2 = OFF_U2 + 8192;
constexpr size_t OFF_MU1 = OFF_V2 + 8192;
constexpr size_t OFF_KMAX = OFF_MU1 + 8192;
constexpr size_t OFF_ST2 = OFF_KMAX + 8192;
constexpr size_t OFF_XH = 1 << 20;
constexpr size_t OFF_WQ = OFF_XH + (size_t)NTT * KS_D * 1024;
constexpr size_t OFF_WO = OFF_WQ + (size_t)48 * KS_D * 1024;
constexpr size_t OFF_W1 = OFF_WO + (size_t)FT_D * KS_O * 1024;
constexpr size_t OFF_W2 = OFF_W1 + (size_t)FT_F * KS_D * 1024;
constexpr size_t OFF_Q = OFF_W2 + (size_t)FT_D * KS_F * 1024;
constexpr size_t OFF_KV = OFF_Q + (size_t)NH * NTT * 2 * 1024;
constexpr size_t OFF_O = OFF_KV + (size_t)NH * NTT * 4 * 1024;
constexpr size_t OFF_Y = OFF_O + (size_t)NTT * KS_O * 1024;
constexpr size_t OFF_X1 = OFF_Y + (size_t)NTT * KS_D * 1024;
constexpr size_t OFF_H1 = OFF_X1 + (size_t)NTT * FT_D * 4096;

DI float bf2f(u16 v) { return __uint_as_float((unsigned)v << 16); }
DI float bflo(unsigned w) { return __uint_as_float(w << 16); }
DI float bfhi(unsigned w) { return __uint_as_float(w & 0xffff0000u); }
DI unsigned pack2(float lo, float hi) {
  f32x2 v = {lo, hi};
  bf16x2_t b = __builtin_convertvector(v, bf16x2_t);
  return __builtin_bit_cast(unsigned, b);
}
DI float rbf(float x) { return bflo(pack2(x, x)); }
DI bf16x8 pack8(const f32x16& x, int s) {
  u32x4 p;
  p[0] = pack2(x[8 * s + 0], x[8 * s + 1]);
  p[1] = pack2(x[8 * s + 2], x[8 * s + 3]);
  p[2] = pack2(x[8 * s + 4], x[8 * s + 5]);
  p[3] = pack2(x[8 * s + 6], x[8 * s + 7]);
  return __builtin_bit_cast(bf16x8, p);
}
DI int kperm(int s, int h, int j) { return 16 * s + 8 * (j >> 2) + 4 * h + (j & 3); }
DI float xhalf(float v) { return __shfl_xor(v, 32); }

__global__ void __launch_bounds__(256) k_mod(const u16* __restrict__ c, const u16* __restrict__ wmod, float* __restrict__ mod) {
  __shared__ float red[64][33];
  const int t = threadIdx.x, cq = t & 3, rg = t >> 2;
  const int col0 = blockIdx.x * 32 + cq * 8;
  u32x4 w[7];
#pragma unroll
  for (int i = 0; i < 7; ++i) w[i] = *(const u32x4*)(wmod + (size_t)(rg + 64 * i) * (6 * DM) + col0);
  float acc[8];
#pragma unroll
  for (int j = 0; j < 8; ++j) acc[j] = 0.f;
#pragma unroll
  for (int i = 0; i < 7; ++i) {
    float cv = bf2f(c[rg + 64 * i]);
    float sv = rbf(cv / (1.f + __expf(-cv)));
#pragma unroll
    for (int j = 0; j < 4; ++j) { acc[2 * j] += sv * bflo(w[i][j]); acc[2 * j + 1] += sv * bfhi(w[i][j]); }
  }
#pragma unroll
  for (int j = 0; j < 8; ++j) red[rg][cq * 8 + j] = acc[j];
  __syncthreads();
  if (t < 32) {
    float s = 0.f;
    for (int i = 0; i < 64; ++i) s += red[i][t];
    mod[blockIdx.x * 32 + t] = rbf(s);
  }
}

constexpr int NB_XH = 64;
constexpr int NFRAG_WQ = 48 * KS_D, NFRAG_WO = FT_D * KS_O, NFRAG_W1 = FT_F * KS_D, NFRAG_W2 = FT_D * KS_F;
constexpr int NFRAG = NFRAG_WQ + NFRAG_WO + NFRAG_W1 + NFRAG_W2;
constexpr int NB_PACK = NFRAG / 4;
constexpr int NB_GEMV = 48 + 56;

__global__ void __launch_bounds__(256) k_prep(const u16* __restrict__ x, const u16* __restrict__ wqkv, const u16* __restrict__ wo,
                                               const u16* __restrict__ w1, const u16* __restrict__ w2, char* __restrict__ ws) {
  __shared__ float red[8][64];
  const float* mod = (const float*)(ws + OFF_MOD);
  const int t = threadIdx.x, lane = t & 63, wv = t >> 6, r = lane & 31, h = lane >> 5;
  int b = blockIdx.x;
  if (b < NB_XH) {
    const int tt = b;
    u32x4 v[7];
    const u16* xr = x + (size_t)(tt * 32 + r) * DM + 8 * h;
#pragma unroll
    for (int i = 0; i < 7; ++i) v[i] = *(const u32x4*)(xr + 16 * (wv * 7 + i));
    float s = 0.f;
#pragma unroll
    for (int i = 0; i < 7; ++i)
#pragma unroll
      for (int j = 0; j < 4; ++j) s += bflo(v[i][j]) + bfhi(v[i][j]);
    red[wv][lane] = s;
    __syncthreads();
    float tot = 0.f;
#pragma unroll
    for (int w = 0; w < 4; ++w) tot += red[w][r] + red[w][r + 32];
    const float mu = tot * (1.f / DM);
    float q = 0.f;
#pragma unroll
    for (int i = 0; i < 7; ++i)
#pragma unroll
      for (int j = 0; j < 4; ++j) { float a = bflo(v[i][j]) - mu, bb = bfhi(v[i][j]) - mu; q += a * a + bb * bb; }
    red[4 + wv][lane] = q;
    __syncthreads();
    float qt = 0.f;
#pragma unroll
    for (int w = 0; w < 4; ++w) qt += red[4 + w][r] + red[4 + w][r + 32];
    const float rstd = rsqrtf(qt * (1.f / DM) + 1e-5f);
    u32x4* dst = (u32x4*)(ws + OFF_XH) + ((size_t)tt * KS_D + wv * 7) * 64 + lane;
#pragma unroll
    for (int i = 0; i < 7; ++i) {
      u32x4 o;
#pragma unroll
      for (int j = 0; j < 4; ++j) o[j] = pack2((bflo(v[i][j]) - mu) * rstd, (bfhi(v[i][j]) - mu) * rstd);
      dst[i * 64] = o;
    }
    if (wv == 0 && h == 0) ((float*)(ws + OFF_MU1))[tt * 32 + r] = mu;
    return;
  }
  b -= NB_XH;
  if (b < NB_PACK) {
    int f = b * 4 + wv;
    float e[8];
    u32x4* dst;
    if (f < NFRAG_WQ) {
      const int ct = f / KS_D, s = f % KS_D, head = ct / 3, which = ct % 3;
      const int n = which * DM + head * DH + r;
#pragma unroll
      for (int j = 0; j < 8; ++j) {
        const int k = 16 * s + 8 * h + j;
        const float g = rbf(1.f + mod[DM + k]);
        e[j] = (r < DH) ? g * bf2f(wqkv[(size_t)k * (3 * DM) + n]) : 0.f;
      }
      dst = (u32x4*)(ws + OFF_WQ) + (size_t)f * 64 + lane;
    } else if ((f -= NFRAG_WQ) < NFRAG_WO) {
      const int ft = f / KS_O, ks = f % KS_O, head = ks >> 1, s = ks & 1;
#pragma unroll
      for (int j = 0; j < 8; ++j) {
        const int d = kperm(s, h, j);
        e[j] = (d < DH) ? bf2f(wo[(size_t)(head * DH + d) * DM + ft * 32 + r]) : 0.f;
      }
      dst = (u32x4*)(ws + OFF_WO) + (size_t)f * 64 + lane;
    } else if ((f -= NFRAG_WO) < NFRAG_W1) {
      const int ft = f / KS_D, ks = f % KS_D;
#pragma unroll
      for (int j = 0; j < 8; ++j) {
        const int k = 32 * (ks >> 1) + kperm(ks & 1, h, j);
        e[j] = bf2f(w1[(size_t)k * FF + ft * 32 + r]);
      }
      dst = (u32x4*)(ws + OFF_W1) + (size_t)f * 64 + lane;
    } else {
      f -= NFRAG_W1;
      const int ft = f / KS_F, ks = f % KS_F;
#pragma unroll
      for (int j = 0; j < 8; ++j) {
        const int k = 32 * (ks >> 1) + kperm(ks & 1, h, j);
        e[j] = bf2f(w2[(size_t)k * DM + ft * 32 + r]);
      }
      dst = (u32x4*)(ws + OFF_W2) + (size_t)f * 64 + lane;
    }
    u32x4 o;
#pragma unroll
    for (int j = 0; j < 4; ++j) o[j] = pack2(e[2 * j], e[2 * j + 1]);
    *dst = o;
    return;
  }
  b -= NB_PACK;
  {
    const int kg = t >> 5, cc = t & 31;
    float a0 = 0.f, a1 = 0.f;
    if (b < 48) {
      const int head = b / 3, which = b % 3;
      const int n = which * DM + head * DH + (cc < DH ? cc : 0);
      for (int k = kg * 56; k < kg * 56 + 56; ++k) a0 += mod[k] * bf2f(wqkv[(size_t)k * (3 * DM) + n]);
      if (cc >= DH) a0 = 0.f;
    } else {
      const int n = (b - 48) * 32 + cc;
      for (int k = kg * 56; k < kg * 56 + 56; ++k) {
        const float wvv = bf2f(w1[(size_t)k * FF + n]);
        a0 += mod[3 * DM + k] * wvv;
        a1 += rbf(1.f + mod[4 * DM + k]) * wvv;
      }
    }
    red[kg][cc] = a0; red[kg][32 + cc] = a1;
    __syncthreads();
    if (t < 64) {
      float s = 0.f;
#pragma unroll
      for (int i = 0; i < 8; ++i) s += red[i][t];
      if (b < 48) { if (t < 32) ((float*)(ws + OFF_V1))[b * 32 + t] = s; }
      else if (t < 32) ((float*)(ws + OFF_V2))[(b - 48) * 32 + t] = s;
      else ((float*)(ws + OFF_U2))[(b - 48) * 32 + t - 32] = s;
    }
  }
}

__global__ void __launch_bounds__(256) k_qkv(char* __restrict__ ws) {
  __shared__ __attribute__((aligned(16))) char lds[3 * KS_D * 1024];
  const int t = threadIdx.x, lane = t & 63, wv = __builtin_amdgcn_readfirstlane(t >> 6), r = lane & 31, h = lane >> 5;
  const int head = blockIdx.x & 15, rt = blockIdx.x >> 4;
  const int tt = rt * 4 + wv;
  const char* wsrc = ws + OFF_WQ + (size_t)head * 3 * KS_D * 1024;
#pragma unroll
  for (int i = 0; i < 21; ++i) {
    const int blk = wv * 21 + i;
    __builtin_amdgcn_global_load_lds((const __attribute__((address_space(1))) void*)(wsrc + blk * 1024 + lane * 16),
                                     (__attribute__((address_space(3))) void*)(lds + blk * 1024), 16, 0, 0);
  }
  bf16x8 xf[KS_D];
  const bf16x8* xsrc = (const bf16x8*)(ws + OFF_XH) + (size_t)tt * KS_D * 64 + lane;
#pragma unroll
  for (int s = 0; s < KS_D; ++s) xf[s] = xsrc[s * 64];
  const float* v1 = (const float*)(ws + OFF_V1) + head * 96;
  f32x16 aq, ak, av;
#pragma unroll
  for (int q = 0; q < 4; ++q) {
    f32x4 bq = *(const f32x4*)(v1 + 8 * q + 4 * h);
    f32x4 bk = *(const f32x4*)(v1 + 32 + 8 * q + 4 * h);
#pragma unroll
    for (int i = 0; i < 4; ++i) { aq[4 * q + i] = bq[i]; ak[4 * q + i] = bk[i]; }
  }
  {
    const float bv = v1[64 + r];
#pragma unroll
    for (int i = 0; i < 16; ++i) av[i] = bv;
  }
  __syncthreads();
  const bf16x8* wl = (const bf16x8*)lds + lane;
#pragma unroll
  for (int s = 0; s < KS_D; ++s) {
    const bf16x8 wq = wl[(0 * KS_D + s) * 64], wk = wl[(1 * KS_D + s) * 64], wvv = wl[(2 * KS_D + s) * 64];
    aq = MFMA32(wq, xf[s], aq);
    ak = MFMA32(wk, xf[s], ak);
    av = MFMA32(xf[s], wvv, av);
  }
  const float qs = 0.18898223650461362f * 1.4426950408889634f;
#pragma unroll
  for (int i = 0; i < 16; ++i) aq[i] *= qs;
  float kn = 0.f;
#pragma unroll
  for (int i = 0; i < 16; ++i) { float v = rbf(ak[i]); kn += v * v; }
  kn += xhalf(kn);
  if (h == 1) { ak[12] = 1.f; ak[13] = 1.f; ak[14] = 1.f; }
  if (r == DH) {
#pragma unroll
    for (int i = 0; i < 16; ++i) av[i] = 1.f;
  }
#pragma unroll
  for (int m = 16; m >= 1; m >>= 1) kn = fmaxf(kn, __shfl_xor(kn, m));
  if (lane == 0) ((float*)(ws + OFF_KMAX))[head * NTT + tt] = kn;
  bf16x8* qd = (bf16x8*)(ws + OFF_Q) + ((size_t)(head * NTT + tt) * 2) * 64 + lane;
  qd[0] = pack8(aq, 0); qd[64] = pack8(aq, 1);
  bf16x8* kd = (bf16x8*)(ws + OFF_KV) + ((size_t)(head * NTT + tt) * 4) * 64 + lane;
  kd[0] = pack8(ak, 0); kd[64] = pack8(ak, 1); kd[128] = pack8(av, 0); kd[192] = pack8(av, 1);
}

DI u16 bf_up(float x) {
  unsigned u = __float_as_uint(x);
  return (u16)((u + 0xffffu) >> 16);
}

DI void attn_main(const bf16x8* __restrict__ kv, const bf16x8 (&qf)[4][2], f32x16 (&o)[4]) {
#pragma unroll
  for (int qb = 0; qb < 4; ++qb)
#pragma unroll
    for (int i = 0; i < 16; ++i) o[qb][i] = 0.f;
  f32x16 zero;
#pragma unroll
  for (int i = 0; i < 16; ++i) zero[i] = 0.f;
  bf16x8 k0 = kv[0], k1 = kv[64], v0 = kv[128], v1 = kv[192];
  bf16x8 nk0 = kv[256], nk1 = kv[256 + 64], nv0 = kv[256 + 128], nv1 = kv[256 + 192];
#pragma unroll 1
  for (int kt = 0; kt < 16; ++kt) {
    const int nx = (kt + 2 < 16) ? kt + 2 : 15;
    const bf16x8* p = kv + nx * 256;
    bf16x8 pk0 = p[0], pk1 = p[64], pv0 = p[128], pv1 = p[192];
#pragma unroll
    for (int qb = 0; qb < 4; ++qb) {
      f32x16 s = MFMA32(k0, qf[qb][0], zero);
      s = MFMA32(k1, qf[qb][1], s);
#pragma unroll
      for (int i = 0; i < 16; ++i) s[i] = __builtin_amdgcn_exp2f(s[i]);
      const bf16x8 p0 = pack8(s, 0), p1 = pack8(s, 1);
      o[qb] = MFMA32(v0, p0, o[qb]);
      o[qb] = MFMA32(v1, p1, o[qb]);
    }
    k0 = nk0; k1 = nk1; v0 = nv0; v1 = nv1;
    nk0 = pk0; nk1 = pk1; nv0 = pv0; nv1 = pv1;
  }
}

__global__ void __launch_bounds__(256) k_attn(char* __restrict__ ws) {
  __shared__ __attribute__((aligned(16))) float red[4][3][4][64][4];
  __shared__ float smax[4][128];
  __shared__ int sflag;
  const int t = threadIdx.x, lane = t & 63, wv = __builtin_amdgcn_readfirstlane(t >> 6), r = lane & 31, h = lane >> 5;
  const int head = blockIdx.x & 15, qt = blockIdx.x >> 4;
  const bf16x8* kv = (const bf16x8*)(ws + OFF_KV) + ((size_t)(head * NTT + wv * 16) * 4) * 64 + lane;
  bf16x8 qf[4][2];
  const bf16x8* qsrc = (const bf16x8*)(ws + OFF_Q) + ((size_t)(head * NTT + qt * 4) * 2) * 64 + lane;
#pragma unroll
  for (int qb = 0; qb < 4; ++qb) { qf[qb][0] = qsrc[(qb * 2) * 64]; qf[qb][1] = qsrc[(qb * 2 + 1) * 64]; }
  float km = ((const float*)(ws + OFF_KMAX))[head * NTT + lane];
#pragma unroll
  for (int m = 32; m >= 1; m >>= 1) km = fmaxf(km, __shfl_xor(km, m));
  if (t == 0) sflag = 0;
#pragma unroll
  for (int qb = 0; qb < 4; ++qb) {
    float qn = 0.f;
#pragma unroll
    for (int s = 0; s < 2; ++s)
#pragma unroll
      for (int j = 0; j < 8; ++j) { float v = bf2f((u16)qf[qb][s][j]); qn += v * v; }
    qn += xhalf(qn);
    const float bound = sqrtf(qn * km) * 1.001f + 1e-30f;
    if (h == 1) qf[qb][1][4] = (short)(bf_up(bound) | 0x8000);
  }
  f32x16 o[4];
  attn_main(kv, qf, o);
  f32x16 fin;
  float l;
#pragma unroll 1
  for (int pass = 0; pass < 2; ++pass) {
#pragma unroll
    for (int qb = 0; qb < 4; ++qb) {
      if (qb != wv) {
        const int slot = (wv - qb - 1) & 3;
#pragma unroll
        for (int q = 0; q < 4; ++q) {
          f32x4 v = {o[qb][4 * q], o[qb][4 * q + 1], o[qb][4 * q + 2], o[qb][4 * q + 3]};
          *(f32x4*)&red[qb][slot][q][lane][0] = v;
        }
      }
    }
    __syncthreads();
    fin = (wv == 0) ? o[0] : (wv == 1) ? o[1] : (wv == 2) ? o[2] : o[3];
#pragma unroll
    for (int sl = 0; sl < 3; ++sl)
#pragma unroll
      for (int q = 0; q < 4; ++q) {
        f32x4 v = *(const f32x4*)&red[wv][sl][q][lane][0];
#pragma unroll
        for (int i = 0; i < 4; ++i) fin[4 * q + i] += v[i];
      }
    l = __shfl(fin[12], r + 32);
    if (pass == 1) break;
    const bool bad = !(l >= 9.094947e-13f);
    if (__any(bad) && lane == 0) sflag = 1;
    __syncthreads();
    if (sflag == 0) break;
    {
      float mx[4];
      f32x16 zero;
#pragma unroll
      for (int i = 0; i < 16; ++i) zero[i] = 0.f;
#pragma unroll
      for (int qb = 0; qb < 4; ++qb) { mx[qb] = -INFINITY; if (h == 1) qf[qb][1][4] = 0; }
#pragma unroll 1
      for (int kt = 0; kt < 16; ++kt) {
        const bf16x8 k0 = kv[kt * 256], k1 = kv[kt * 256 + 64];
#pragma unroll
        for (int qb = 0; qb < 4; ++qb) {
          f32x16 s = MFMA32(k0, qf[qb][0], zero);
          s = MFMA32(k1, qf[qb][1], s);
#pragma unroll
          for (int i = 0; i < 16; ++i) mx[qb] = fmaxf(mx[qb], s[i]);
        }
      }
#pragma unroll
      for (int qb = 0; qb < 4; ++qb) {
        mx[qb] = fmaxf(mx[qb], xhalf(mx[qb]));
        if (h == 0) smax[wv][qb * 32 + r] = mx[qb];
      }
      __syncthreads();
#pragma unroll
      for (int qb = 0; qb < 4; ++qb) {
        float m = fmaxf(fmaxf(smax[0][qb * 32 + r], smax[1][qb * 32 + r]), fmaxf(smax[2][qb * 32 + r], smax[3][qb * 32 + r]));
        const float a = rbf(m), b2 = rbf(m - a), c2 = rbf(m - a - b2);
        if (h == 1) {
          qf[qb][1][4] = (short)(__float_as_uint(-a) >> 16);
          qf[qb][1][5] = (short)(__float_as_uint(-b2) >> 16);
          qf[qb][1][6] = (short)(__float_as_uint(-c2) >> 16);
        }
      }
      attn_main(kv, qf, o);
    }
  }
  const float inv = 1.f / l;
#pragma unroll
  for (int i = 0; i < 16; ++i) fin[i] *= inv;
  bf16x8* od = (bf16x8*)(ws + OFF_O) + ((size_t)(qt * 4 + wv) * KS_O + head * 2) * 64 + lane;
  od[0] = pack8(fin, 0); od[64] = pack8(fin, 1);
}

template <int KS>
DI void ksplit_gemm(const bf16x8* __restrict__ wA, const bf16x8* __restrict__ act, float (*red)[3][4][64][4], int wv, int lane, f32x16& fin) {
  constexpr int KW = KS / 4;
  f32x16 acc[4];
#pragma unroll
  for (int i = 0; i < 4; ++i)
#pragma unroll
    for (int j = 0; j < 16; ++j) acc[i][j] = 0.f;
  const bf16x8* pa = wA + (size_t)wv * KW * 64;
  const bf16x8* pb = act + (size_t)wv * KW * 64;
  constexpr int CH = (KW % 7 == 0) ? 7 : 8;
#pragma unroll 1
  for (int c = 0; c < KW; c += CH) {
    bf16x8 a0[CH], a1[CH], b0[CH], b1[CH];
#pragma unroll
    for (int i = 0; i < CH; ++i) {
      a0[i] = pa[(c + i) * 64]; a1[i] = pa[(KS + c + i) * 64];
      b0[i] = pb[(c + i) * 64]; b1[i] = pb[(KS + c + i) * 64];
    }
#pragma unroll
    for (int i = 0; i < CH; ++i) {
      acc[0] = MFMA32(a0[i], b0[i], acc[0]);
      acc[1] = MFMA32(a0[i], b1[i], acc[1]);
      acc[2] = MFMA32(a1[i], b0[i], acc[2]);
      acc[3] = MFMA32(a1[i], b1[i], acc[3]);
    }
  }
#pragma unroll
  for (int qb = 0; qb < 4; ++qb) {
    if (qb != wv) {
      const int slot = (wv - qb - 1) & 3;
#pragma unroll
      for (int q = 0; q < 4; ++q) {
        f32x4 v = {acc[qb][4 * q], acc[qb][4 * q + 1], acc[qb][4 * q + 2], acc[qb][4 * q + 3]};
        *(f32x4*)&red[qb][slot][q][lane][0] = v;
      }
    }
  }
  __syncthreads();
  fin = (wv == 0) ? acc[0] : (wv == 1) ? acc[1] : (wv == 2) ? acc[2] : acc[3];
#pragma unroll
  for (int sl = 0; sl < 3; ++sl)
#pragma unroll
    for (int q = 0; q < 4; ++q) {
      f32x4 v = *(const f32x4*)&red[wv][sl][q][lane][0];
#pragma unroll
      for (int i = 0; i < 4; ++i) fin[4 * q + i] += v[i];
    }
}

__global__ void __launch_bounds__(256) k_oproj(const u16* __restrict__ x, char* __restrict__ ws) {
  __shared__ __attribute__((aligned(16))) float red[4][3][4][64][4];
  __shared__ float sst[2][2][32][2];
  const int t = threadIdx.x, lane = t & 63, wv = __builtin_amdgcn_readfirstlane(t >> 6), r = lane & 31, h = lane >> 5;
  const int fg = blockIdx.x % 7, tg = blockIdx.x / 7;
  const int ft = fg * 2 + (wv >> 1), tt = tg * 2 + (wv & 1);
  const float* mod = (const float*)(ws + OFF_MOD);
  u32x2 xr[4]; f32x4 ga[4], g2[4];
#pragma unroll
  for (int q = 0; q < 4; ++q) {
    const int n = ft * 32 + 8 * q + 4 * h;
    xr[q] = *(const u32x2*)(x + (size_t)(tt * 32 + r) * DM + n);
    ga[q] = *(const f32x4*)(mod + 2 * DM + n);
    g2[q] = *(const f32x4*)(mod + 4 * DM + n);
  }
  const float mu1 = ((const float*)(ws + OFF_MU1))[tt * 32 + r];
  f32x16 fin;
  ksplit_gemm<KS_O>((const bf16x8*)(ws + OFF_WO) + (size_t)fg * 2 * KS_O * 64 + lane,
                    (const bf16x8*)(ws + OFF_O) + (size_t)tg * 2 * KS_O * 64 + lane, red, wv, lane, fin);
  f32x16 y;
  float s1 = 0.f, s2 = 0.f;
  f32x4* x1d = (f32x4*)(ws + OFF_X1) + ((size_t)(tt * FT_D + ft) * 4) * 64 + lane;
#pragma unroll
  for (int q = 0; q < 4; ++q) {
    f32x4 x1;
    x1[0] = bflo(xr[q][0]) + ga[q][0] * fin[4 * q + 0];
    x1[1] = bfhi(xr[q][0]) + ga[q][1] * fin[4 * q + 1];
    x1[2] = bflo(xr[q][1]) + ga[q][2] * fin[4 * q + 2];
    x1[3] = bfhi(xr[q][1]) + ga[q][3] * fin[4 * q + 3];
    x1d[q * 64] = x1;
#pragma unroll
    for (int i = 0; i < 4; ++i) {
      const float d = x1[i] - mu1;
      s1 += d; s2 += d * d;
      y[4 * q + i] = d * rbf(1.f + g2[q][i]);
    }
  }
  bf16x8* yd = (bf16x8*)(ws + OFF_Y) + ((size_t)tt * KS_D + ft * 2) * 64 + lane;
  yd[0] = pack8(y, 0); yd[64] = pack8(y, 1);
  s1 += xhalf(s1); s2 += xhalf(s2);
  if (h == 0) { sst[wv >> 1][wv & 1][r][0] = s1; sst[wv >> 1][wv & 1][r][1] = s2; }
  __syncthreads();
  if (t < 64) {
    const int tb = t >> 5, rr = t & 31;
    f32x2 v = {sst[0][tb][rr][0] + sst[1][tb][rr][0], sst[0][tb][rr][1] + sst[1][tb][rr][1]};
    ((f32x2*)(ws + OFF_ST2))[(size_t)((tg * 2 + tb) * 32 + rr) * 7 + fg] = v;
  }
}

__global__ void __launch_bounds__(448) k_ffn1(char* __restrict__ ws) {
  __shared__ __attribute__((aligned(16))) char lds[2 * KS_D * 1024];
  const int t = threadIdx.x, lane = t & 63, wv = __builtin_amdgcn_readfirstlane(t >> 6), r = lane & 31, h = lane >> 5;
  const int fg = blockIdx.x & 7, tg = blockIdx.x >> 3;
  const int ft = fg * 7 + wv;
  const char* ysrc = ws + OFF_Y + (size_t)tg * 2 * KS_D * 1024;
#pragma unroll
  for (int i = 0; i < 8; ++i) {
    const int blk = wv * 8 + i;
    __builtin_amdgcn_global_load_lds((const __attribute__((address_space(1))) void*)(ysrc + blk * 1024 + lane * 16),
                                     (__attribute__((address_space(3))) void*)(lds + blk * 1024), 16, 0, 0);
  }
  bf16x8 wf[KS_D];
  const bf16x8* wsrc = (const bf16x8*)(ws + OFF_W1) + (size_t)ft * KS_D * 64 + lane;
#pragma unroll
  for (int s = 0; s < KS_D; ++s) wf[s] = wsrc[s * 64];
  float rstd[2], dl[2];
#pragma unroll
  for (int b = 0; b < 2; ++b) {
    const f32x2* st = (const f32x2*)(ws + OFF_ST2) + (size_t)((tg * 2 + b) * 32 + r) * 7;
    float s1 = 0.f, s2 = 0.f;
#pragma unroll
    for (int i = 0; i < 7; ++i) { f32x2 v = st[i]; s1 += v[0]; s2 += v[1]; }
    const float m = s1 * (1.f / DM);
    const float var = fmaxf(s2 * (1.f / DM) - m * m, 0.f);
    rstd[b] = rsqrtf(var + 1e-5f); dl[b] = m;
  }
  f32x4 u2[4], v2[4];
#pragma unroll
  for (int q = 0; q < 4; ++q) {
    u2[q] = *(const f32x4*)((const float*)(ws + OFF_U2) + ft * 32 + 8 * q + 4 * h);
    v2[q] = *(const f32x4*)((const float*)(ws + OFF_V2) + ft * 32 + 8 * q + 4 * h);
  }
  f32x16 acc[2];
#pragma unroll
  for (int b = 0; b < 2; ++b)
#pragma unroll
    for (int q = 0; q < 4; ++q)
#pragma unroll
      for (int i = 0; i < 4; ++i) acc[b][4 * q + i] = -dl[b] * u2[q][i];
  __syncthreads();
  const bf16x8* yl = (const bf16x8*)lds + lane;
#pragma unroll
  for (int s = 0; s < KS_D; ++s) {
    acc[0] = MFMA32(wf[s], yl[s * 64], acc[0]);
    acc[1] = MFMA32(wf[s], yl[(KS_D + s) * 64], acc[1]);
  }
#pragma unroll
  for (int b = 0; b < 2; ++b) {
#pragma unroll
    for (int q = 0; q < 4; ++q)
#pragma unroll
      for (int i = 0; i < 4; ++i) {
        const float p = rstd[b] * acc[b][4 * q + i] + v2[q][i];
        acc[b][4 * q + i] = p * __builtin_amdgcn_rcpf(1.f + __builtin_amdgcn_exp2f(-1.4426950408889634f * p));
      }
    bf16x8* hd = (bf16x8*)(ws + OFF_H1) + ((size_t)(tg * 2 + b) * KS_F + ft * 2) * 64 + lane;
    hd[0] = pack8(acc[b], 0); hd[64] = pack8(acc[b], 1);
  }
}

__global__ void __launch_bounds__(256) k_ffn2(char* __restrict__ ws, u16* __restrict__ out) {
  __shared__ __attribute__((aligned(16))) float red[4][3][4][64][4];
  const int t = threadIdx.x, lane = t & 63, wv = __builtin_amdgcn_readfirstlane(t >> 6), r = lane & 31, h = lane >> 5;
  const int fg = blockIdx.x % 7, tg = blockIdx.x / 7;
  const int ft = fg * 2 + (wv >> 1), tt = tg * 2 + (wv & 1);
  const float* mod = (const float*)(ws + OFF_MOD);
  f32x4 gb[4], x1[4];
  const f32x4* x1s = (const f32x4*)(ws + OFF_X1) + ((size_t)(tt * FT_D + ft) * 4) * 64 + lane;
#pragma unroll
  for (int q = 0; q < 4; ++q) {
    gb[q] = *(const f32x4*)(mod + 5 * DM + ft * 32 + 8 * q + 4 * h);
    x1[q] = x1s[q * 64];
  }
  f32x16 fin;
  ksplit_gemm<KS_F>((const bf16x8*)(ws + OFF_W2) + (size_t)fg * 2 * KS_F * 64 + lane,
                    (const bf16x8*)(ws + OFF_H1) + (size_t)tg * 2 * KS_F * 64 + lane, red, wv, lane, fin);
#pragma unroll
  for (int q = 0; q < 4; ++q) {
    u32x2 o;
    o[0] = pack2(x1[q][0] + gb[q][0] * fin[4 * q + 0], x1[q][1] + gb[q][1] * fin[4 * q + 1]);
    o[1] = pack2(x1[q][2] + gb[q][2] * fin[4 * q + 2], x1[q][3] + gb[q][3] * fin[4 * q + 3]);
    *(u32x2*)(out + (size_t)(tt * 32 + r) * DM + ft * 32 + 8 * q + 4 * h) = o;
  }
}

extern "C" void kernel_launch(void* const* d_in, const int* in_sizes, int n_in, void* d_out, int out_size, void* d_ws,
                              size_t ws_size, hipStream_t stream) {
  const u16* x = (const u16*)d_in[0];
  const u16* wqkv = (const u16*)d_in[1];
  const u16* wo = (const u16*)d_in[2];
  const u16* w1 = (const u16*)d_in[4];
  const u16* w2 = (const u16*)d_in[5];
  const u16* c = (const u16*)d_in[6];
  const u16* wmod = (const u16*)d_in[7];
  char* ws = (char*)d_ws;
  k_mod<<<84, 256, 0, stream>>>(c, wmod, (float*)(ws + OFF_MOD));
  k_prep<<<NB_XH + NB_PACK + NB_GEMV, 256, 0, stream>>>(x, wqkv, wo, w1, w2, ws);
  k_qkv<<<256, 256, 0, stream>>>(ws);
  k_attn<<<256, 256, 0, stream>>>(ws);
  k_oproj<<<224, 256, 0, stream>>>(x, ws);
  k_ffn1<<<256, 448, 0, stream>>>(ws);
  k_ffn2<<<224, 256, 0, stream>>>(ws, (u16*)d_out);
}
```

```cpp
#include <hip/hip_runtime.h>
#include <hip/hip_bf16.h>
#include <stdint.h>

typedef __attribute__((ext_vector_type(8))) short bf16x8;
typedef __attribute__((ext_vector_type(4))) short s16x4;
typedef __attribute__((ext_vector_type(16))) float f32x16;
typedef __attribute__((ext_vector_type(4))) unsigned u32x4;
typedef __attribute__((ext_vector_type(2))) unsigned u32x2;
typedef __attribute__((ext_vector_type(4))) float f32x4;
typedef __attribute__((ext_vector_type(2))) float f32x2;
typedef __attribute__((ext_vector_type(2))) __bf16 bf16x2_t;
typedef __attribute__((ext_vector_type(2))) _Float16 h16x2;
typedef unsigned short u16;
typedef unsigned long long u64;
#define GAS __attribute__((address_space(1)))
#define LAS __attribute__((address_space(3)))
typedef GAS u64 gu64;
typedef LAS const char* lds_cptr;
typedef LAS char* lds_ptr;

#define DI __device__ __forceinline__
#define MFMA32(a, b, c) __builtin_amdgcn_mfma_f32_32x32x16_bf16((a), (b), (c), 0, 0, 0)
#define MFMA16(a, b, c) __builtin_amdgcn_mfma_f32_16x16x32_bf16((a), (b), (c), 0, 0, 0)
#define SBAR() __builtin_amdgcn_sched_barrier(0)
#define PIN(x) asm volatile("" : "+v"(x))
#define RLX_AGENT __ATOMIC_RELAXED, __HIP_MEMORY_SCOPE_AGENT
#define BC8(x) __builtin_bit_cast(bf16x8, x)
#define EX2(v) __builtin_amdgcn_exp2f(v)
#define BAR_LGKM() asm volatile("s_waitcnt lgkmcnt(0)\n\ts_barrier" ::: "memory")
#define BAR_ALL() asm volatile("s_waitcnt vmcnt(0) lgkmcnt(0)\n\ts_barrier" ::: "memory")
#define BAR_VM(n) asm volatile("s_waitcnt vmcnt(" #n ")\n\ts_barrier" ::: "memory")

constexpr int DM = 448, DH = 28, FF = 1792;
constexpr int NTT = 64;

constexpr unsigned OFF_MODG = 4096;
constexpr unsigned OFF_FKV = 32768;
constexpr unsigned OFF_FO = 36864;
constexpr unsigned OFF_FY = 40960;
constexpr unsigned OFF_FH = 49152;
constexpr unsigned OFF_ST = 65536;
constexpr unsigned OFF_Q = 1u << 20;
constexpr unsigned OFF_KV = 3u << 20;
constexpr unsigned OFF_O = 7u << 20;
constexpr unsigned OFF_Y = 9u << 20;
constexpr unsigned OFF_H = 11u << 20;
constexpr unsigned WS_USED = 19u << 20;

#define STAMP(i) do {} while (0)

DI float bf2f(u16 v) { return __uint_as_float((unsigned)v << 16); }
DI float bflo(unsigned w) { return __uint_as_float(w << 16); }
DI float bfhi(unsigned w) { return __uint_as_float(w & 0xffff0000u); }
DI unsigned pack2(float lo, float hi) {
  f32x2 v = {lo, hi};
  bf16x2_t b = __builtin_convertvector(v, bf16x2_t);
  return __builtin_bit_cast(unsigned, b);
}
DI unsigned packh2(float lo, float hi) {
  f32x2 v = {lo, hi};
  h16x2 b = __builtin_convertvector(v, h16x2);
  return __builtin_bit_cast(unsigned, b);
}
DI float hlo(unsigned w) { return (float)__builtin_bit_cast(h16x2, w)[0]; }
DI float hhi(unsigned w) { return (float)__builtin_bit_cast(h16x2, w)[1]; }
DI float rbf(float x) { return bflo(pack2(x, x)); }
DI u32x4 pack8(const f32x16& x, int s) {
  u32x4 p;
  p[0] = pack2(x[8 * s + 0], x[8 * s + 1]);
  p[1] = pack2(x[8 * s + 2], x[8 * s + 3]);
  p[2] = pack2(x[8 * s + 4], x[8 * s + 5]);
  p[3] = pack2(x[8 * s + 6], x[8 * s + 7]);
  return p;
}
DI float xhalf_add(float v) {
  auto rr = __builtin_amdgcn_permlane32_swap(__float_as_uint(v), __float_as_uint(v), false, false);
  return __uint_as_float(rr[0]) + __uint_as_float(rr[1]);
}
DI float xhalf_max(float v) {
  auto rr = __builtin_amdgcn_permlane32_swap(__float_as_uint(v), __float_as_uint(v), false, false);
  return fmaxf(__uint_as_float(rr[0]), __uint_as_float(rr[1]));
}
DI void dma1(const void* sbase, unsigned voff, unsigned lds_base) {
  unsigned sv;
  asm volatile("s_mov_b32 %0, m0\n\ts_mov_b32 m0, %2\n\ts_nop 0\n\tglobal_load_lds_dwordx4 %1, %3\n\ts_mov_b32 m0, %0"
               : "=&s"(sv) : "v"(voff), "s"(lds_base), "s"(sbase) : "memory");
}
DI void dma1c(const void* sbase, unsigned voff, unsigned lds_base) {
  unsigned sv;
  asm volatile("s_mov_b32 %0, m0\n\ts_mov_b32 m0, %2\n\ts_nop 0\n\tglobal_load_lds_dwordx4 %1, %3 sc1\n\ts_mov_b32 m0, %0"
               : "=&s"(sv) : "v"(voff), "s"(lds_base), "s"(sbase) : "memory");
}
DI void dma4c(const void* sbase, unsigned voff, unsigned lds_base) {
  unsigned sv;
  asm volatile("s_mov_b32 %0, m0\n\ts_mov_b32 m0, %2\n\ts_nop 0\n\t"
               "global_load_lds_dwordx4 %1, %3 sc1\n\tglobal_load_lds_dwordx4 %1, %3 offset:1024 sc1\n\t"
               "global_load_lds_dwordx4 %1, %3 offset:2048 sc1\n\tglobal_load_lds_dwordx4 %1, %3 offset:3072 sc1\n\t"
               "s_mov_b32 m0, %0" : "=&s"(sv) : "v"(voff), "s"(lds_base), "s"(sbase) : "memory");
}
DI void touch1(const void* sbase, unsigned voff, unsigned lds_base) {
  unsigned sv;
  asm volatile("s_mov_b32 %0, m0\n\ts_mov_b32 m0, %2\n\ts_nop 0\n\tglobal_load_lds_dword %1, %3\n\ts_mov_b32 m0, %0"
               : "=&s"(sv) : "v"(voff), "s"(lds_base), "s"(sbase) : "memory");
}
DI bf16x8 ldsfrag(lds_cptr p) { return *(const LAS bf16x8*)p; }
DI s16x4 trread(lds_cptr p) { return __builtin_amdgcn_ds_read_tr16_b64_v4i16((LAS s16x4*)p); }
DI bf16x8 trfrag(lds_cptr p0, lds_cptr p1) {
  s16x4 lo = trread(p0), hi = trread(p1);
  return __builtin_shufflevector(lo, hi, 0, 1, 2, 3, 4, 5, 6, 7);
}
DI float dot2bf(unsigned a, unsigned b, float c) {
  return __builtin_amdgcn_fdot2_f32_bf16(__builtin_bit_cast(bf16x2_t, a), __builtin_bit_cast(bf16x2_t, b), c, false);
}
DI unsigned wait_words(const char* p, int n, unsigned tag, int lane) {
  const gu64* f = (const gu64*)p + (lane < n ? lane : n - 1);
  const u64 t0 = __builtin_amdgcn_s_memrealtime();
  u64 v;
  for (;;) {
    v = __hip_atomic_load(f, RLX_AGENT);
    if (__all((unsigned)(v >> 32) == tag)) break;
    if (__builtin_amdgcn_s_memrealtime() - t0 > 2000000ull) break;
    __builtin_amdgcn_s_sleep(1);
  }
  return (unsigned)v;
}
DI void put_word(char* p, unsigned tag, unsigned val) { __hip_atomic_store((gu64*)p, ((u64)tag << 32) | val, RLX_AGENT); }

constexpr int L_WQ = 0;
constexpr int L_XS = 86016;
constexpr int L_MODW = 143360;
constexpr int L_SILU = 157696;
constexpr int L_STAT = 143360;
constexpr int L_GB = 145408;
constexpr int L_KMX = 158592;
constexpr int L_LOW = 0;
constexpr int L_M2 = 57344;
constexpr int L_GA = L_M2, L_G2 = L_M2 + 1792, L_GBV = L_M2 + 3584, L_PK = L_M2 + 5376;
constexpr int L_SM = L_M2 + 7168;
constexpr int L_HI = 65536;
constexpr int LDS_BYTES = 160 * 1024;

#define ASTEP(SC, SN, KADDR, V1ADDR, V0ADDR) do { \
    SN = MFMA32(kf0, qf0, zero16); SBAR(); \
    pw1[2] = pack2(e12, e13); pw1[3] = pack2(e14, e15); \
    kf0 = ldsfrag(KADDR); SBAR(); \
    SC[0] = EX2(SC[0]); SC[1] = EX2(SC[1]); SC[2] = EX2(SC[2]); SC[3] = EX2(SC[3]); PIN(SC); PIN(pw1); SBAR(); \
    o = MFMA32(vf1, BC8(pw1), o); SBAR(); \
    vf1 = ldsfrag(V1ADDR); \
    SC[4] = EX2(SC[4]); SC[5] = EX2(SC[5]); SBAR(); pw0[0] = pack2(SC[0], SC[1]); pw0[1] = pack2(SC[2], SC[3]); SBAR(); SC[6] = EX2(SC[6]); SC[7] = EX2(SC[7]); PIN(SC); PIN(pw0); SBAR(); \
    SN = MFMA32(kf1, qf1, SN); SBAR(); \
    kf1 = ldsfrag((KADDR) + 1024); \
    SC[8] = EX2(SC[8]); SC[9] = EX2(SC[9]); SBAR(); pw0[2] = pack2(SC[4], SC[5]); pw0[3] = pack2(SC[6], SC[7]); SBAR(); SC[10] = EX2(SC[10]); SC[11] = EX2(SC[11]); PIN(SC); PIN(pw0); SBAR(); \
    o = MFMA32(vf0, BC8(pw0), o); SBAR(); \
    vf0 = ldsfrag(V0ADDR); \
    e12 = EX2(SC[12]); e13 = EX2(SC[13]); SBAR(); pw1[0] = pack2(SC[8], SC[9]); pw1[1] = pack2(SC[10], SC[11]); SBAR(); e14 = EX2(SC[14]); e15 = EX2(SC[15]); PIN(e12); PIN(e13); PIN(e14); PIN(e15); PIN(pw1); SBAR(); \
  } while (0)

#define ATTN_PROLOGUE() \
    kf0 = ldsfrag(abase); kf1 = ldsfrag(abase + 1024); \
    vf0 = ldsfrag(abase + 2048); vf1 = vf0; \
    e12 = 0.f; e13 = 0.f; e14 = 0.f; e15 = 0.f; pw1[0] = 0; pw1[1] = 0; o = zero16; \
    sA = MFMA32(kf0, qf0, zero16); sA = MFMA32(kf1, qf1, sA); \
    kf0 = ldsfrag(abase + 4096); kf1 = ldsfrag(abase + 4096 + 1024);

__global__ void __launch_bounds__(512) fused_block(const u16* __restrict__ x, const u16* __restrict__ wqkv, const u16* __restrict__ wo,
                                                    const u16* __restrict__ w1, const u16* __restrict__ w2, const u16* __restrict__ cvec,
                                                    const u16* __restrict__ wmod, u16* __restrict__ out, char* __restrict__ ws) {
  __shared__ __attribute__((aligned(16))) char lds_arr[LDS_BYTES];
  const lds_ptr L = (lds_ptr)lds_arr;
  const unsigned lds0 = (unsigned)(uintptr_t)lds_arr;
  const int t = threadIdx.x, lane = t & 63, wv = __builtin_amdgcn_readfirstlane(t >> 6), r = lane & 31, h = lane >> 5;
  __builtin_assume(wv < 8);
  const int g16 = (lane >> 4) & 1, tq = (lane & 15) >> 2, tp = lane & 3;
  const int b = blockIdx.x, xg = b & 7, rk = b >> 3;
  const __amdgpu_buffer_rsrc_t rs = __builtin_amdgcn_make_buffer_rsrc(ws, 0, WS_USED, 0x00020000);
  STAMP(0);

  const int rt = xg * 2 + (rk >> 4), hq = rk & 15;
  const bool modrole = b < 168;
  unsigned craw = 0;
  if (modrole && wv < 7) {
    asm volatile("global_load_ushort %0, %1, %2" : "=v"(craw) : "v"(t * 2), "s"(cvec) : "memory");
    const unsigned vo = (unsigned)(wv * 64 + (lane >> 1)) * (6 * DM * 2) + (lane & 1) * 16;
    dma1(wmod + b * 16, vo, lds0 + L_MODW + wv * 2048);
    dma1(wmod + b * 16 + 32 * 6 * DM, vo, lds0 + L_MODW + wv * 2048 + 1024);
  }
  const int a_tt = wv & 3, a_kh = wv >> 2;
  const int first_half = hq & 1;
  unsigned xvo[7];
#pragma unroll
  for (int i = 0; i < 7; ++i) {
    const int q = i * 64 + lane, rl = q / 56, c = q - rl * 56, row = wv * 8 + rl;
    xvo[i] = row * (DM * 2) + ((c ^ ((row >> 1) & 7)) * 16);
  }
  {
    const u16* xb = x + (size_t)(rt * 128 + first_half * 64) * DM;
#pragma unroll
    for (int i = 0; i < 7; ++i) dma1(xb, xvo[i], lds0 + L_XS + (wv * 7 + i) * 1024);
  }
  u64 epoch;
  asm volatile("s_load_dwordx2 %0, %1, 0x0" : "=s"(epoch) : "s"(ws) : "memory");
  {
    const unsigned vo = (lane >> 2) * (3 * DM * 2) + ((tp == 3) ? 40 : tp * 16);
#pragma unroll
    for (int i = 0; i < 11; ++i) {
      const int blk = wv + 8 * i;
      if (blk < 84) {
        const int seg = blk / 28, s = blk - seg * 28;
        dma1(wqkv + hq * DH + seg * DM + s * 16 * 3 * DM, vo, lds0 + L_WQ + blk * 1024);
      }
    }
  }
  if (t == 0) *(LAS unsigned*)(L + L_KMX) = 0;
  asm volatile("s_waitcnt lgkmcnt(0)" : "+s"(epoch) :: "memory");
  const unsigned tag = (unsigned)epoch + 1u;
  if (modrole) {
    asm volatile("s_waitcnt vmcnt(17)" : "+v"(craw) :: "memory");
    if (wv < 7) {
      const float cval = __uint_as_float(craw << 16);
      const float sv = cval * __builtin_amdgcn_rcpf(1.f + EX2(-1.4426950408889634f * cval));
      *(LAS u16*)(L + L_SILU + t * 2) = (u16)(pack2(sv, sv) & 0xffff);
    }
    BAR_LGKM();
    if (wv == 0) {
      const int kg = lane >> 4;
      const lds_cptr bp = L + L_MODW + (8 * kg + tq) * 32 + tp * 8;
      const lds_cptr ap = L + L_SILU + kg * 16;
      bf16x8 af[14], bfr[14];
#pragma unroll
      for (int s = 0; s < 14; ++s) { af[s] = ldsfrag(ap + s * 64); bfr[s] = trfrag(bp + s * 1024, bp + s * 1024 + 128); }
      f32x4 acc0 = {0.f, 0.f, 0.f, 0.f}, acc1 = acc0;
#pragma unroll
      for (int s = 0; s < 14; s += 2) { acc0 = MFMA16(af[s], bfr[s], acc0); acc1 = MFMA16(af[s + 1], bfr[s + 1], acc1); }
      if (lane < 16) put_word(ws + OFF_MODG + (b * 16 + lane) * 8, tag, __float_as_uint(rbf(acc0[0] + acc1[0])));
    }
  }
  STAMP(1);
  u32x4 xr[14];
  lds_cptr xa[4];
  {
    const int row = (a_tt & 1) * 32 + r, gsw = (row >> 1) & 7, c0 = a_kh * 28 + h;
#pragma unroll
    for (int i = 0; i < 4; ++i) { const int c = c0 + 2 * i; xa[i] = L + L_XS + row * (DM * 2) + (c & ~7) * 16 + (((c & 7) ^ gsw) * 16); }
  }
  const bool in_first = (a_tt >> 1) == first_half;
  BAR_VM(10);
  if (in_first) {
#pragma unroll
    for (int s = 0; s < 14; ++s) xr[s] = *(const LAS u32x4*)(xa[s & 3] + (s >> 2) * 128);
  }
  BAR_LGKM();
  {
    const u16* xb = x + (size_t)(rt * 128 + (first_half ^ 1) * 64) * DM;
#pragma unroll
    for (int i = 0; i < 7; ++i) dma1(xb, xvo[i], lds0 + L_XS + (wv * 7 + i) * 1024);
  }
  BAR_ALL();
  STAMP(2);
  if (!in_first) {
#pragma unroll
    for (int s = 0; s < 14; ++s) xr[s] = *(const LAS u32x4*)(xa[s & 3] + (s >> 2) * 128);
  }
  {
    float s1a = 0.f, s2a = 0.f, s1b = 0.f, s2b = 0.f;
#pragma unroll
    for (int s = 0; s < 14; ++s) {
      s1a = dot2bf(xr[s][0], 0x3f803f80u, s1a); s2a = dot2bf(xr[s][0], xr[s][0], s2a);
      s1b = dot2bf(xr[s][1], 0x3f803f80u, s1b); s2b = dot2bf(xr[s][1], xr[s][1], s2b);
      s1a = dot2bf(xr[s][2], 0x3f803f80u, s1a); s2a = dot2bf(xr[s][2], xr[s][2], s2a);
      s1b = dot2bf(xr[s][3], 0x3f803f80u, s1b); s2b = dot2bf(xr[s][3], xr[s][3], s2b);
    }
    const float s1 = xhalf_add(s1a + s1b), s2 = xhalf_add(s2a + s2b);
    if (h == 0) { f32x2 v = {s1, s2}; *(LAS f32x2*)(L + L_STAT + (wv * 32 + r) * 8) = v; }
    BAR_LGKM();
    const f32x2 o2 = *(const LAS f32x2*)(L + L_STAT + ((wv ^ 4) * 32 + r) * 8);
    const float mu = (s1 + o2[0]) * (1.f / DM);
    const float var = fmaxf((s2 + o2[1]) * (1.f / DM) - mu * mu, 0.f);
    const float rstd = __builtin_amdgcn_rsqf(var + 1e-5f);
    const float nb = -mu * rstd;
#pragma unroll
    for (int s = 0; s < 14; ++s) {
#pragma unroll
      for (int j = 0; j < 4; ++j) xr[s][j] = packh2(bflo(xr[s][j]) * rstd + nb, bfhi(xr[s][j]) * rstd + nb);
      PIN(xr[s]);
    }
  }
  const lds_cptr wp = L + L_WQ + a_kh * 14 * 1024 + (8 * h + tq) * 64 + g16 * 32 + tp * 8;
  bf16x8 wqf[14], wkf[14];
#pragma unroll
  for (int s = 0; s < 14; ++s) {
    wqf[s] = trfrag(wp + s * 1024, wp + s * 1024 + 256);
    wkf[s] = trfrag(wp + (28 + s) * 1024, wp + (28 + s) * 1024 + 256);
    PIN(wqf[s]); PIN(wkf[s]);
  }
  STAMP(3);
  if (wv < 7) {
    const u64 t0 = __builtin_amdgcn_s_memrealtime();
    u32x4 v;
    for (;;) {
      v = __builtin_amdgcn_raw_buffer_load_b128(rs, OFF_MODG + t * 16, 0, 16);
      if (__all(v[1] == tag && v[3] == tag)) break;
      if (__builtin_amdgcn_s_memrealtime() - t0 > 2000000ull) break;
      __builtin_amdgcn_s_sleep(1);
    }
    f32x2 o2 = {__uint_as_float(v[0]), __uint_as_float(v[2])};
    const bool isg = t >= 224;
    if (isg) { o2[0] = rbf(1.f + o2[0]); o2[1] = rbf(1.f + o2[1]); }
    *(LAS f32x2*)(L + L_GB + (isg ? 2 * t - DM : DM + 2 * t) * 4) = o2;
  }
  BAR_LGKM();
  STAMP(4);
  f32x16 zero16;
#pragma unroll
  for (int i = 0; i < 16; ++i) zero16[i] = 0.f;
  f32x16 aq = zero16, ak = zero16, av = zero16;
  {
    const lds_cptr gp = L + L_GB + (a_kh * 224 + 8 * h) * 4;
    f32x4 g0, g1, b0, b1;
    bf16x8 wvf;
    u32x4 hf;
#define LOADGB(s) do { g0 = *(const LAS f32x4*)(gp + (s) * 64); g1 = *(const LAS f32x4*)(gp + (s) * 64 + 16); \
      b0 = *(const LAS f32x4*)(gp + DM * 4 + (s) * 64); b1 = *(const LAS f32x4*)(gp + DM * 4 + (s) * 64 + 16); } while (0)
#define MAKEH(dst, s) do { \
      dst[0] = pack2(hlo(xr[s][0]) * g0[0] + b0[0], hhi(xr[s][0]) * g0[1] + b0[1]); \
      dst[1] = pack2(hlo(xr[s][1]) * g0[2] + b0[2], hhi(xr[s][1]) * g0[3] + b0[3]); \
      dst[2] = pack2(hlo(xr[s][2]) * g1[0] + b1[0], hhi(xr[s][2]) * g1[1] + b1[1]); \
      dst[3] = pack2(hlo(xr[s][3]) * g1[2] + b1[2], hhi(xr[s][3]) * g1[3] + b1[3]); } while (0)
    LOADGB(0);
    wvf = trfrag(wp + 56 * 1024, wp + 56 * 1024 + 256);
    MAKEH(hf, 0);
    LOADGB(1);
    SBAR();
#pragma unroll
    for (int s = 0; s < 14; ++s) {
      u32x4 hn;
      bf16x8 wvn;
      aq = MFMA32(wqf[s], BC8(hf), aq); SBAR();
      if (s + 1 < 14) { wvn = trfrag(wp + (57 + s) * 1024, wp + (57 + s) * 1024 + 256); MAKEH(hn, s + 1); PIN(hn); }
      SBAR();
      ak = MFMA32(wkf[s], BC8(hf), ak); SBAR();
      if (s + 2 < 14) LOADGB(s + 2);
      SBAR();
      av = MFMA32(BC8(hf), wvf, av); SBAR();
      if (s + 1 < 14) { hf = hn; wvf = wvn; }
    }
  }
  STAMP(5);
  {
    const lds_ptr xw = L + L_XS + wv * 6144 + lane * 16;
    const f32x16 snd = a_kh ? aq : ak;
#pragma unroll
    for (int q = 0; q < 4; ++q) { f32x4 v = {snd[4 * q], snd[4 * q + 1], snd[4 * q + 2], snd[4 * q + 3]}; *(LAS f32x4*)(xw + q * 1024) = v; }
#pragma unroll
    for (int q = 0; q < 2; ++q) {
      f32x4 v;
#pragma unroll
      for (int i = 0; i < 4; ++i) v[i] = a_kh ? av[4 * q + i] : av[8 + 4 * q + i];
      *(LAS f32x4*)(xw + (4 + q) * 1024) = v;
    }
    BAR_LGKM();
    const lds_cptr xrd = L + L_XS + (wv ^ 4) * 6144 + lane * 16;
    f32x16 mine = a_kh ? ak : aq;
    float vv[8];
#pragma unroll
    for (int q = 0; q < 4; ++q) {
      const f32x4 v = *(const LAS f32x4*)(xrd + q * 1024);
#pragma unroll
      for (int i = 0; i < 4; ++i) mine[4 * q + i] += v[i];
    }
#pragma unroll
    for (int q = 0; q < 2; ++q) {
      const f32x4 v = *(const LAS f32x4*)(xrd + (4 + q) * 1024);
#pragma unroll
      for (int i = 0; i < 4; ++i) vv[4 * q + i] = v[i] + (a_kh ? av[8 + 4 * q + i] : av[4 * q + i]);
    }
    const int tt = rt * 4 + a_tt;
    const float msc = a_kh ? 1.f : 0.18898223650461362f * 1.4426950408889634f;
#pragma unroll
    for (int i = 0; i < 16; ++i) mine[i] *= msc;
    u32x4 f0 = pack8(mine, 0), f1 = pack8(mine, 1);
    if (h == 0) { f1[2] = 0; f1[3] = 0; }
    float kn = 0.f;
#pragma unroll
    for (int i = 0; i < 4; ++i) { kn = dot2bf(f0[i], f0[i], kn); kn = dot2bf(f1[i], f1[i], kn); }
    if (a_kh && h == 0) { f1[2] = 0x3f803f80u; f1[3] = 0x00003f80u; }
    const unsigned mo = (a_kh ? OFF_KV + (hq * NTT + tt) * 4096 : OFF_Q + (hq * NTT + tt) * 2048) + lane * 16;
    __builtin_amdgcn_raw_buffer_store_b128(f0, rs, mo, 0, 16);
    __builtin_amdgcn_raw_buffer_store_b128(f1, rs, mo + 1024, 0, 16);
    const bool vpad = (r >= 24 && r < 28);
    const unsigned vconst = (r == 24) ? 0x3f803f80u : 0u;
    u32x4 vf;
#pragma unroll
    for (int i = 0; i < 4; ++i) vf[i] = vpad ? vconst : pack2(vv[2 * i], vv[2 * i + 1]);
    __builtin_amdgcn_raw_buffer_store_b128(vf, rs, OFF_KV + ((hq * NTT + tt) * 4 + 2 + a_kh) * 1024 + lane * 16, 0, 16);
    if (a_kh) {
      kn = xhalf_add(kn);
      if (h == 0) __hip_atomic_fetch_max((LAS unsigned*)(L + L_KMX), __float_as_uint(kn), __ATOMIC_RELAXED, __HIP_MEMORY_SCOPE_WORKGROUP);
    }
    BAR_ALL();
    if (t == 0) put_word(ws + OFF_FKV + (hq * 16 + rt) * 8, tag, *(const LAS unsigned*)(L + L_KMX));
  }
  STAMP(6);
  const int head = xg * 2 + (rk >> 4), qt = rk & 15;
  const int tg = xg * 4 + (rk >> 3), sl = rk & 7;
  const int qb = wv & 3, kh = wv >> 2;
  if (wv == 7) {
    float km = __uint_as_float(wait_words(ws + OFF_FKV + head * 128, 16, tag, lane));
#pragma unroll
    for (int m = 8; m >= 1; m >>= 1) km = fmaxf(km, __shfl_xor(km, m));
    if (lane == 0) *(LAS float*)(L + L_SM) = km;
  } else {
    const u64 t0 = __builtin_amdgcn_s_memrealtime();
    u32x4 v0, v1;
    for (;;) {
      v0 = __builtin_amdgcn_raw_buffer_load_b128(rs, OFF_MODG + 7168 + t * 16, 0, 16);
      v1 = __builtin_amdgcn_raw_buffer_load_b128(rs, OFF_MODG + 14336 + t * 16, 0, 16);
      if (__all(v0[1] == tag && v0[3] == tag && v1[1] == tag && v1[3] == tag)) break;
      if (__builtin_amdgcn_s_memrealtime() - t0 > 2000000ull) break;
      __builtin_amdgcn_s_sleep(1);
    }
    const int e = (t < 224) ? 2 * t : 2 * t - DM;
    const int pk = (e >> 4) * 32 + ((e >> 2) & 1) * 16 + ((e >> 3) & 1) * 8 + (e & 3) * 2;
    if (t < 224) {
      f32x2 a = {__uint_as_float(v0[0]), __uint_as_float(v0[2])};
      f32x2 g = {rbf(1.f + __uint_as_float(v1[0])), rbf(1.f + __uint_as_float(v1[2]))};
      *(LAS f32x2*)(L + L_GA + e * 4) = a;
      *(LAS f32x2*)(L + L_G2 + e * 4) = g;
      *(LAS unsigned*)(L + L_PK + pk) = pack2(g[0], g[1]);
    } else {
      f32x2 g = {__uint_as_float(v1[0]), __uint_as_float(v1[2])};
      *(LAS f32x2*)(L + L_GBV + e * 4) = g;
      *(LAS unsigned*)(L + L_PK + 896 + pk) = pack2(__uint_as_float(v0[0]), __uint_as_float(v0[2]));
    }
  }
  BAR_LGKM();
  STAMP(7);
  const char* kvsrc = ws + OFF_KV + (size_t)(head * NTT + kh * 32 + qb) * 4096;
  const unsigned kvdst = lds0 + L_HI + kh * 16384 + qb * 4096;
  const unsigned l16 = lane * 16;
#define DMA_KV(i, buf) dma4c(kvsrc + (size_t)(i) * 16384, l16, kvdst + (buf))
  bf16x8 qf0, qf1;
  {
    const unsigned qo = OFF_Q + (head * NTT + qt * 4 + qb) * 2048 + l16;
    u32x4 q0 = __builtin_amdgcn_raw_buffer_load_b128(rs, qo, 0, 16), q1 = __builtin_amdgcn_raw_buffer_load_b128(rs, qo + 1024, 0, 16);
    DMA_KV(0, 0);
    const float km = *(const LAS float*)(L + L_SM);
    float qn = 0.f;
#pragma unroll
    for (int i = 0; i < 4; ++i) { qn = dot2bf(q0[i], q0[i], qn); qn = dot2bf(q1[i], q1[i], qn); }
    qn = xhalf_add(qn);
    const float bound = __builtin_amdgcn_sqrtf(qn * km) * 1.004f + 1e-30f;
    if (h == 0) q1[2] = ((__float_as_uint(bound) + 0xffffu) >> 16) | 0x8000u;
    qf0 = BC8(q0); qf1 = BC8(q1);
  }
  DMA_KV(1, 32768);
  unsigned w1vo[4];
#pragma unroll
  for (int i = 0; i < 4; ++i) { const int q = (wv + 8 * i) * 64 + lane, row = q / 28, c = q - row * 28; w1vo[i] = row * (FF * 2) + c * 16; }
  const u16* w1b = w1 + sl * 224;
  const lds_cptr w1rd = L + L_LOW + (4 * h + tq) * 448 + wv * 64 + g16 * 32 + tp * 8;
  bf16x8 wf[28];
  f32x16 o, sA, sB;
  u32x4 pw0, pw1;
  bf16x8 kf0, kf1, vf0, vf1;
  float e12, e13, e14, e15;
  const lds_cptr abase = L + L_HI + kh * 16384 + l16;
  BAR_VM(4);
  STAMP(8);
  ATTN_PROLOGUE();
#pragma unroll
  for (int st = 0; st < 8; ++st) {
    const int cur = (st % 3) * 32768, nxt = ((st + 1) % 3) * 32768, nn = ((st + 2) % 3) * 32768;
    const lds_cptr bc = abase + cur, bn = abase + nxt;
    ASTEP(sA, sB, bc + 2 * 4096, bc + 0 * 4096 + 3072, bc + 1 * 4096 + 2048);
    ASTEP(sB, sA, bc + 3 * 4096, bc + 1 * 4096 + 3072, bc + 2 * 4096 + 2048);
    BAR_VM(0);
    if (st + 2 < 8) DMA_KV(st + 2, nn);
    if (st < 7) {
      const u16* src = w1b + (size_t)st * 64 * FF;
      const unsigned dst = lds0 + L_LOW + (st & 1) * 28672 + wv * 1024;
      dma1(src, w1vo[0], dst); dma1(src, w1vo[1], dst + 8192); dma1(src, w1vo[2], dst + 16384);
      if (wv < 4) dma1(src, w1vo[3], dst + 24576);
    }
    if (st == 1) {
      if (wv < 2) { const int li = rk * 98 + wv * 64 + lane; touch1(wo, (li < rk * 98 + 98 ? li : rk * 98) * 128, lds0 + L_SM + 256); }
      else { const int li = (wv - 2) * 64 + lane; touch1(w2, (rk * 392 + li) * 128, lds0 + L_SM + 256); if (wv == 7 && lane < 8) {} }
    }
    if (st == 2 && wv == 0) { const int li = 384 + (lane & 7); touch1(w2, (rk * 392 + li) * 128, lds0 + L_SM + 256); }
    if (st >= 1 && wv < 7) {
      const lds_cptr p = w1rd + ((st - 1) & 1) * 28672;
#pragma unroll
      for (int i = 0; i < 4; ++i) { wf[4 * (st - 1) + i] = trfrag(p + i * 16 * 448, p + (i * 16 + 8) * 448); PIN(wf[4 * (st - 1) + i]); }
    }
    SBAR();
    ASTEP(sA, sB, bn + 0 * 4096, bc + 2 * 4096 + 3072, bc + 3 * 4096 + 2048);
    ASTEP(sB, sA, bn + 1 * 4096, bc + 3 * 4096 + 3072, bn + 0 * 4096 + 2048);
  }
  STAMP(9);
  float fin[8];
  float lsum;
#pragma unroll 1
  for (int pass = 0; pass < 2; ++pass) {
    pw1[2] = pack2(e12, e13); pw1[3] = pack2(e14, e15);
    o = MFMA32(vf1, BC8(pw1), o);
    constexpr int L_X = L_HI + 65536;
    {
      f32x4 a, b2;
#pragma unroll
      for (int i = 0; i < 4; ++i) { a[i] = kh ? o[i] : o[8 + i]; b2[i] = kh ? o[4 + i] : o[12 + i]; }
      *(LAS f32x4*)(L + L_X + 8192 + (wv * 2 + 0) * 1024 + l16) = a;
      *(LAS f32x4*)(L + L_X + 8192 + (wv * 2 + 1) * 1024 + l16) = b2;
      if (h == 0) *(LAS float*)(L + L_X + 24576 + (wv * 32 + r) * 4) = o[12];
    }
    BAR_LGKM();
    {
      const int pw = wv ^ 4;
      const f32x4 a = *(const LAS f32x4*)(L + L_X + 8192 + (pw * 2 + 0) * 1024 + l16);
      const f32x4 b2 = *(const LAS f32x4*)(L + L_X + 8192 + (pw * 2 + 1) * 1024 + l16);
      const float c0 = *(const LAS float*)(L + L_X + 24576 + lane * 4) + *(const LAS float*)(L + L_X + 24576 + (128 + lane) * 4);
      const float c1 = *(const LAS float*)(L + L_X + 24576 + (64 + lane) * 4) + *(const LAS float*)(L + L_X + 24576 + (192 + lane) * 4);
#pragma unroll
      for (int i = 0; i < 4; ++i) { fin[i] = a[i] + (kh ? o[8 + i] : o[i]); fin[4 + i] = b2[i] + (kh ? o[12 + i] : o[4 + i]); }
      lsum = *(const LAS float*)(L + L_X + 24576 + (qb * 32 + r) * 4) + *(const LAS float*)(L + L_X + 24576 + ((qb + 4) * 32 + r) * 4);
      const bool ok = (c0 >= 9.094947e-13f) && (c1 >= 9.094947e-13f);
      if (pass == 1 || __all(ok)) break;
    }
    {
      u32x4 q1 = __builtin_bit_cast(u32x4, qf1);
      if (h == 0) { q1[2] = 0; q1[3] = 0; }
      qf1 = BC8(q1);
      float mx = -INFINITY;
      const unsigned ko = OFF_KV + (head * NTT + kh * 32) * 4096 + l16;
#pragma unroll 1
      for (int kt = 0; kt < 32; ++kt) {
        const u32x4 k0 = __builtin_amdgcn_raw_buffer_load_b128(rs, ko + kt * 4096, 0, 16);
        const u32x4 k1 = __builtin_amdgcn_raw_buffer_load_b128(rs, ko + kt * 4096 + 1024, 0, 16);
        f32x16 s = MFMA32(BC8(k0), qf0, zero16);
        s = MFMA32(BC8(k1), qf1, s);
#pragma unroll
        for (int i = 0; i < 16; ++i) mx = fmaxf(mx, s[i]);
      }
      mx = xhalf_max(mx);
      if (h == 0) *(LAS float*)(L + L_X + 28672 + (wv * 32 + r) * 4) = mx;
      BAR_LGKM();
      mx = fmaxf(*(const LAS float*)(L + L_X + 28672 + (qb * 32 + r) * 4), *(const LAS float*)(L + L_X + 28672 + ((qb + 4) * 32 + r) * 4));
      const float m0 = rbf(mx), m1 = rbf(mx - m0), m2 = rbf(mx - m0 - m1);
      if (h == 0) { q1[2] = pack2(-m0, -m1); q1[3] = pack2(-m2, 0.f); }
      qf1 = BC8(q1);
      DMA_KV(0, 0);
      DMA_KV(1, 32768);
      asm volatile("s_waitcnt vmcnt(4) lgkmcnt(0)\n\ts_barrier" ::: "memory");
      ATTN_PROLOGUE();
      int cur = 0, nxt = 32768, nn = 65536;
#pragma unroll 1
      for (int st = 0; st < 8; ++st) {
        const lds_cptr bc = abase + cur, bn = abase + nxt;
        ASTEP(sA, sB, bc + 2 * 4096, bc + 0 * 4096 + 3072, bc + 1 * 4096 + 2048);
        ASTEP(sB, sA, bc + 3 * 4096, bc + 1 * 4096 + 3072, bc + 2 * 4096 + 2048);
        BAR_VM(0);
        if (st + 2 < 8) DMA_KV(st + 2, nn);
        ASTEP(sA, sB, bn + 0 * 4096, bc + 2 * 4096 + 3072, bc + 3 * 4096 + 2048);
        ASTEP(sB, sA, bn + 1 * 4096, bc + 3 * 4096 + 3072, bn + 0 * 4096 + 2048);
        const int tmp = cur; cur = nxt; nxt = nn; nn = tmp;
      }
    }
  }
  {
    const float inv = __builtin_amdgcn_rcpf(lsum);
    u32x4 ov;
#pragma unroll
    for (int i = 0; i < 4; ++i) ov[i] = pack2(fin[2 * i] * inv, fin[2 * i + 1] * inv);
    if (kh == 1 && h == 0) { ov[2] = 0; ov[3] = 0; }
    __builtin_amdgcn_raw_buffer_store_b128(ov, rs, OFF_O + ((qt * 4 + qb) * 32 + head * 2 + kh) * 1024 + l16, 0, 16);
    BAR_ALL();
    if (t == 0) put_word(ws + OFF_FO + (qt * 16 + head) * 8, tag, 0);
  }
  STAMP(10);
  const int tb = wv & 1, kq = wv >> 1;
  const int tt_own = tg * 2 + tb;
  const int n0 = (sl * 2 + (kq >> 1)) * 32 + (kq & 1) * 16 + 4 * h;
  const unsigned rvo = (lane >> 3) * (DM * 2) + (((lane & 7) ^ (((lane >> 4) & 1) << 2)) * 16);
  const int rsw = ((tq >> 1) & 1) * 64;
  u32x2 xa2, xb2;
  unsigned xc0;
  if (sl < 7) {
#pragma unroll
    for (int i = 0; i < 7; ++i) { const int blk = wv * 7 + i; dma1(wo + sl * 64 + blk * 8 * DM, rvo, lds0 + L_LOW + blk * 1024); }
    const u16* xrow = x + (size_t)(tt_own * 32 + r) * DM;
    xa2 = *(const u32x2*)(xrow + n0); xb2 = *(const u32x2*)(xrow + n0 + 8);
    xc0 = xrow[0];
  }
  f32x16 u2, v2;
  if (wv < 7) {
    f32x16 uv = zero16;
    const lds_cptr pp = L + L_PK + g16 * 896 + h * 16;
#pragma unroll
    for (int ks = 0; ks < 28; ++ks) uv = MFMA32(wf[ks], ldsfrag(pp + ks * 32), uv);
#pragma unroll
    for (int i = 0; i < 16; ++i) {
      auto rr = __builtin_amdgcn_permlane16_swap(__float_as_uint(uv[i]), __float_as_uint(uv[i]), false, false);
      u2[i] = __uint_as_float(rr[0]); v2[i] = __uint_as_float(rr[1]);
    }
  }
  STAMP(11);
  float x1[8];
  if (sl < 7) {
    if (wv == 7) wait_words(ws + OFF_FO + (tg >> 1) * 128, 16, tag, lane);
    BAR_ALL();
    STAMP(12);
    u32x4 ob[8];
#pragma unroll
    for (int i = 0; i < 8; ++i) ob[i] = __builtin_amdgcn_raw_buffer_load_b128(rs, OFF_O + (tt_own * 32 + kq * 8 + i) * 1024 + l16, 0, 16);
    SBAR();
    f32x16 acc0 = zero16, acc1 = zero16;
    {
      const lds_cptr wop = L + L_LOW + (kq * 4 * DH + 4 * h + tq) * 128 + g16 * 32 + tp * 8;
      const lds_cptr p0 = wop + rsw, p1 = wop + (64 ^ rsw);
      bf16x8 wa0[8], wa1[8];
#pragma unroll
      for (int i = 0; i < 8; ++i) {
        const int row0 = (i >> 1) * DH + 16 * (i & 1), row1 = (i >> 1) * DH + ((i & 1) ? 20 : 8);
        wa0[i] = trfrag(p0 + row0 * 128, p0 + row1 * 128);
        wa1[i] = trfrag(p1 + row0 * 128, p1 + row1 * 128);
      }
#pragma unroll
      for (int i = 0; i < 8; ++i) { acc0 = MFMA32(wa0[i], BC8(ob[i]), acc0); acc1 = MFMA32(wa1[i], BC8(ob[i]), acc1); }
    }
    STAMP(13);
#define RED_WRITE(A0, A1) do { _Pragma("unroll") for (int p = 0; p < 4; ++p) { if (p != kq) { \
        const int o8 = (p & 1) * 8; f32x4 v0, v1; \
        _Pragma("unroll") for (int i = 0; i < 4; ++i) { v0[i] = (p >> 1) ? A1[o8 + i] : A0[o8 + i]; v1[i] = (p >> 1) ? A1[o8 + 4 + i] : A0[o8 + 4 + i]; } \
        *(LAS f32x4*)(L + L_HI + (((tb * 4 + p) * 4 + kq) * 2) * 1024 + l16) = v0; \
        *(LAS f32x4*)(L + L_HI + (((tb * 4 + p) * 4 + kq) * 2 + 1) * 1024 + l16) = v1; } } } while (0)
#define RED_READ(AT, A0, A1) do { _Pragma("unroll") for (int i = 0; i < 8; ++i) { \
        const float v00 = A0[i], v01 = A0[8 + i], v10 = A1[i], v11 = A1[8 + i]; \
        AT[i] = (kq == 0) ? v00 : (kq == 1) ? v01 : (kq == 2) ? v10 : v11; } \
      _Pragma("unroll") for (int d = 1; d < 4; ++d) { const int src = (kq + d) & 3; \
        const f32x4 v0 = *(const LAS f32x4*)(L + L_HI + (((tb * 4 + kq) * 4 + src) * 2) * 1024 + l16); \
        const f32x4 v1 = *(const LAS f32x4*)(L + L_HI + (((tb * 4 + kq) * 4 + src) * 2 + 1) * 1024 + l16); \
        _Pragma("unroll") for (int i = 0; i < 4; ++i) { AT[i] += v0[i]; AT[4 + i] += v1[i]; } } } while (0)
    RED_WRITE(acc0, acc1);
    const f32x4 ga0 = *(const LAS f32x4*)(L + L_GA + n0 * 4), ga1 = *(const LAS f32x4*)(L + L_GA + (n0 + 8) * 4);
    const f32x4 g20 = *(const LAS f32x4*)(L + L_G2 + n0 * 4), g21 = *(const LAS f32x4*)(L + L_G2 + (n0 + 8) * 4);
    BAR_LGKM();
    float at[8];
    RED_READ(at, acc0, acc1);
    x1[0] = bflo(xa2[0]) + ga0[0] * at[0]; x1[1] = bfhi(xa2[0]) + ga0[1] * at[1];
    x1[2] = bflo(xa2[1]) + ga0[2] * at[2]; x1[3] = bfhi(xa2[1]) + ga0[3] * at[3];
    x1[4] = bflo(xb2[0]) + ga1[0] * at[4]; x1[5] = bfhi(xb2[0]) + ga1[1] * at[5];
    x1[6] = bflo(xb2[1]) + ga1[2] * at[6]; x1[7] = bfhi(xb2[1]) + ga1[3] * at[7];
    const float cshift = __uint_as_float(xc0 << 16);
    float p1 = 0.f, p2 = 0.f, y[8];
#pragma unroll
    for (int i = 0; i < 8; ++i) {
      const float d = x1[i] - cshift;
      p1 += d; p2 += d * d;
      y[i] = d * (i < 4 ? g20[i & 3] : g21[i & 3]);
    }
    u32x4 yv;
#pragma unroll
    for (int i = 0; i < 4; ++i) yv[i] = pack2(y[2 * i], y[2 * i + 1]);
    __builtin_amdgcn_raw_buffer_store_b128(yv, rs, OFF_Y + (tt_own * 28 + sl * 4 + kq) * 1024 + l16, 0, 16);
    p1 = xhalf_add(p1); p2 = xhalf_add(p2);
    if (h == 0) {
      u32x2 sv = {__float_as_uint(p1), __float_as_uint(p2)};
      __builtin_amdgcn_raw_buffer_store_b64(sv, rs, OFF_ST + ((tt_own * 28 + sl * 4 + kq) * 32 + r) * 8, 0, 16);
    }
    BAR_ALL();
    if (t == 0) put_word(ws + OFF_FY + tg * 128 + sl * 8, tag, 0);
    if (b == 0 && t == 64) __hip_atomic_store((gu64*)ws, epoch + 1ull, RLX_AGENT);
#pragma unroll
    for (int i = 0; i < 12; ++i) {
      const int pc = wv * 12 + i, q4 = pc / 24, rr8 = pc - q4 * 24;
      dma1(w2 + sl * 64 + (size_t)(q4 * 448 + rr8 * 8) * DM, rvo, lds0 + L_HI + pc * 1024);
    }
  }
  STAMP(14);
  if (wv == 7) wait_words(ws + OFF_FY + tg * 128, 7, tag, lane);
  BAR_LGKM();
  STAMP(15);
  {
    const char* ysrc = ws + OFF_Y + (size_t)tg * 57344;
    if (wv < 7) { dma4c(ysrc + wv * 8192, l16, lds0 + L_LOW + wv * 8192); dma4c(ysrc + wv * 8192 + 4096, l16, lds0 + L_LOW + wv * 8192 + 4096); }
    else {
      float a1 = 0.f, a2 = 0.f;
      u32x2 sv[28];
#pragma unroll
      for (int i = 0; i < 28; ++i) sv[i] = __builtin_amdgcn_raw_buffer_load_b64(rs, OFF_ST + (((tg * 2 + h) * 28 + i) * 32 + r) * 8, 0, 16);
#pragma unroll
      for (int i = 0; i < 28; ++i) { a1 += __uint_as_float(sv[i][0]); a2 += __uint_as_float(sv[i][1]); }
      const float m = a1 * (1.f / DM);
      const float var = fmaxf(a2 * (1.f / DM) - m * m, 0.f);
      f32x2 o2 = {__builtin_amdgcn_rsqf(var + 1e-5f), m};
      *(LAS f32x2*)(L + L_SM + 512 + lane * 8) = o2;
    }
  }
  BAR_ALL();
  STAMP(16);
  f32x16 c0 = zero16, c1 = zero16;
  f32x2 st0, st1;
  f32x4 gb0, gb1;
  if (wv < 7) {
    const lds_cptr yp = L + L_LOW + l16;
#pragma unroll
    for (int ks = 0; ks < 28; ++ks) {
      c0 = MFMA32(wf[ks], ldsfrag(yp + ks * 1024), c0);
      c1 = MFMA32(wf[ks], ldsfrag(yp + (28 + ks) * 1024), c1);
    }
    st0 = *(const LAS f32x2*)(L + L_SM + 512 + r * 8); st1 = *(const LAS f32x2*)(L + L_SM + 512 + (32 + r) * 8);
  }
  gb0 = *(const LAS f32x4*)(L + L_GBV + (sl < 7 ? n0 : 0) * 4); gb1 = *(const LAS f32x4*)(L + L_GBV + ((sl < 7 ? n0 : 0) + 8) * 4);
  BAR_LGKM();
  STAMP(17);
  if (sl < 7) {
#pragma unroll
    for (int i = 0; i < 8; ++i) {
      const int pc = wv * 8 + i, q4 = pc >> 4, rr8 = pc & 15;
      dma1(w2 + sl * 64 + (size_t)(q4 * 448 + 192 + rr8 * 8) * DM, rvo, lds0 + pc * 1024);
    }
  }
  if (wv < 7) {
#pragma unroll
    for (int i = 0; i < 16; ++i) {
      const float p0 = st0[0] * (c0[i] - st0[1] * u2[i]) + v2[i];
      c0[i] = p0 * __builtin_amdgcn_rcpf(1.f + EX2(-1.4426950408889634f * p0));
    }
    const unsigned ho = OFF_H + ((tg * 2) * 112 + (sl * 7 + wv) * 2) * 1024 + l16;
    __builtin_amdgcn_raw_buffer_store_b128(pack8(c0, 0), rs, ho, 0, 16);
    __builtin_amdgcn_raw_buffer_store_b128(pack8(c0, 1), rs, ho + 1024, 0, 16);
#pragma unroll
    for (int i = 0; i < 16; ++i) {
      const float p1 = st1[0] * (c1[i] - st1[1] * u2[i]) + v2[i];
      c1[i] = p1 * __builtin_amdgcn_rcpf(1.f + EX2(-1.4426950408889634f * p1));
    }
    __builtin_amdgcn_raw_buffer_store_b128(pack8(c1, 0), rs, ho + 112 * 1024, 0, 16);
    __builtin_amdgcn_raw_buffer_store_b128(pack8(c1, 1), rs, ho + 113 * 1024, 0, 16);
  }
  STAMP(18);
  BAR_ALL();
  if (t == 0) put_word(ws + OFF_FH + tg * 128 + sl * 8, tag, 0);
  STAMP(19);
  if (sl == 7) return;
  {
    const int rowoff = (4 * h + tq) * 128 + g16 * 32 + tp * 8;
    const lds_cptr lo0 = L + kq * 16384 + rowoff + rsw, lo1 = L + kq * 16384 + rowoff + (64 ^ rsw);
    bf16x8 r0[8], r1[8];
#pragma unroll
    for (int i = 0; i < 8; ++i) {
      r0[i] = trfrag(lo0 + i * 2048, lo0 + i * 2048 + 1024);
      r1[i] = trfrag(lo1 + i * 2048, lo1 + i * 2048 + 1024);
      PIN(r0[i]); PIN(r1[i]);
    }
    BAR_LGKM();
#pragma unroll
    for (int i = 0; i < 8; ++i) {
      const int pc = wv * 8 + i, q4 = pc >> 4, rr8 = pc & 15;
      dma1(w2 + sl * 64 + (size_t)(q4 * 448 + 320 + rr8 * 8) * DM, rvo, lds0 + pc * 1024);
    }
    if (wv == 7) wait_words(ws + OFF_FH + tg * 128, 8, tag, lane);
    BAR_ALL();
    STAMP(20);
    u32x4 hb[28];
    const unsigned ho = OFF_H + (tt_own * 112 + kq * 28) * 1024 + l16;
#pragma unroll
    for (int i = 0; i < 28; ++i) {
      const int ks = (i < 8) ? 12 + i : (i < 20) ? i - 8 : i;
      hb[i] = __builtin_amdgcn_raw_buffer_load_b128(rs, ho + ks * 1024, 0, 16);
    }
    SBAR();
    f32x16 d0 = zero16, d1 = zero16;
#pragma unroll
    for (int i = 0; i < 8; ++i) { d0 = MFMA32(r0[i], BC8(hb[i]), d0); d1 = MFMA32(r1[i], BC8(hb[i]), d1); }
    const lds_cptr hi0 = L + L_HI + kq * 24576 + rowoff + rsw, hi1 = L + L_HI + kq * 24576 + rowoff + (64 ^ rsw);
#pragma unroll
    for (int i = 0; i < 12; ++i) {
      d0 = MFMA32(trfrag(hi0 + i * 2048, hi0 + i * 2048 + 1024), BC8(hb[8 + i]), d0);
      d1 = MFMA32(trfrag(hi1 + i * 2048, hi1 + i * 2048 + 1024), BC8(hb[8 + i]), d1);
    }
#pragma unroll
    for (int i = 0; i < 8; ++i) {
      d0 = MFMA32(trfrag(lo0 + i * 2048, lo0 + i * 2048 + 1024), BC8(hb[20 + i]), d0);
      d1 = MFMA32(trfrag(lo1 + i * 2048, lo1 + i * 2048 + 1024), BC8(hb[20 + i]), d1);
    }
    STAMP(21);
    BAR_LGKM();
    RED_WRITE(d0, d1);
    BAR_LGKM();
    float f[8];
    RED_READ(f, d0, d1);
    u32x2 oa, ob2;
    oa[0] = pack2(x1[0] + gb0[0] * f[0], x1[1] + gb0[1] * f[1]);
    oa[1] = pack2(x1[2] + gb0[2] * f[2], x1[3] + gb0[3] * f[3]);
    ob2[0] = pack2(x1[4] + gb1[0] * f[4], x1[5] + gb1[1] * f[5]);
    ob2[1] = pack2(x1[6] + gb1[2] * f[6], x1[7] + gb1[3] * f[7]);
    u16* orow = out + (size_t)(tt_own * 32 + r) * DM;
    *(u32x2*)(orow + n0) = oa;
    *(u32x2*)(orow + n0 + 8) = ob2;
  }
  STAMP(22);
}

extern "C" void kernel_launch(void* const* d_in, const int* in_sizes, int n_in, void* d_out, int out_size, void* d_ws,
                              size_t ws_size, hipStream_t stream) {
  fused_block<<<256, 512, 0, stream>>>((const u16*)d_in[0], (const u16*)d_in[1], (const u16*)d_in[2], (const u16*)d_in[4],
                                       (const u16*)d_in[5], (const u16*)d_in[6], (const u16*)d_in[7], (u16*)d_out, (char*)d_ws);
}
```

```cpp
#include <hip/hip_runtime.h>
#include <hip/hip_bf16.h>
#include <stdint.h>

typedef __attribute__((ext_vector_type(8))) short bf16x8;
typedef __attribute__((ext_vector_type(4))) short s16x4;
typedef __attribute__((ext_vector_type(16))) float f32x16;
typedef __attribute__((ext_vector_type(4))) unsigned u32x4;
typedef __attribute__((ext_vector_type(2))) unsigned u32x2;
typedef __attribute__((ext_vector_type(4))) float f32x4;
typedef __attribute__((ext_vector_type(2))) float f32x2;
typedef __attribute__((ext_vector_type(2))) __bf16 bf16x2_t;
typedef __attribute__((ext_vector_type(2))) _Float16 h16x2;
typedef unsigned short u16;
typedef unsigned long long u64;
#define GAS __attribute__((address_space(1)))
#define LAS __attribute__((address_space(3)))
typedef GAS u64 gu64;
typedef LAS const char* lds_cptr;
typedef LAS char* lds_ptr;

#define DI __device__ __forceinline__
#define MFMA32(a, b, c) __builtin_amdgcn_mfma_f32_32x32x16_bf16((a), (b), (c), 0, 0, 0)
#define MFMA16(a, b, c) __builtin_amdgcn_mfma_f32_16x16x32_bf16((a), (b), (c), 0, 0, 0)
#define SBAR() __builtin_amdgcn_sched_barrier(0)
#define PIN(x) asm volatile("" : "+v"(x))
#define RLX_AGENT __ATOMIC_RELAXED, __HIP_MEMORY_SCOPE_AGENT
#define BC8(x) __builtin_bit_cast(bf16x8, x)
#define EX2(v) __builtin_amdgcn_exp2f(v)
#define BAR_LGKM() asm volatile("s_waitcnt lgkmcnt(0)\n\ts_barrier" ::: "memory")
#define BAR_ALL() asm volatile("s_waitcnt vmcnt(0) lgkmcnt(0)\n\ts_barrier" ::: "memory")
#define BAR_VM(n) asm volatile("s_waitcnt vmcnt(" #n ")\n\ts_barrier" ::: "memory")

constexpr int DM = 448, DH = 28, FF = 1792;
constexpr int NTT = 64;

constexpr unsigned OFF_MODG = 4096;
constexpr unsigned OFF_FKV = 32768;
constexpr unsigned OFF_FO = 36864;
constexpr unsigned OFF_FY = 40960;
constexpr unsigned OFF_FH = 49152;
constexpr unsigned OFF_ST = 65536;
constexpr unsigned OFF_Q = 1u << 20;
constexpr unsigned OFF_KV = 3u << 20;
constexpr unsigned OFF_O = 7u << 20;
constexpr unsigned OFF_Y = 9u << 20;
constexpr unsigned OFF_H = 11u << 20;
constexpr unsigned WS_USED = 19u << 20;

#define STAMP(i) do {} while (0)
#define STAMP_DECL() do {} while (0)
#define STAMP_DUMP() do {} while (0)

DI float bf2f(u16 v) { return __uint_as_float((unsigned)v << 16); }
DI float bflo(unsigned w) { return __uint_as_float(w << 16); }
DI float bfhi(unsigned w) { return __uint_as_float(w & 0xffff0000u); }
DI unsigned pack2(float lo, float hi) {
  f32x2 v = {lo, hi};
  bf16x2_t b = __builtin_convertvector(v, bf16x2_t);
  return __builtin_bit_cast(unsigned, b);
}
DI unsigned packh2(float lo, float hi) {
  f32x2 v = {lo, hi};
  h16x2 b = __builtin_convertvector(v, h16x2);
  return __builtin_bit_cast(unsigned, b);
}
DI float hlo(unsigned w) { return (float)__builtin_bit_cast(h16x2, w)[0]; }
DI float hhi(unsigned w) { return (float)__builtin_bit_cast(h16x2, w)[1]; }
DI float fmix_lo(unsigned w, float g, float b) { float d; asm("v_fma_mix_f32 %0, %1, %2, %3 op_sel_hi:[1,0,0]" : "=v"(d) : "v"(w), "v"(g), "v"(b)); return d; }
DI float fmix_hi(unsigned w, float g, float b) { float d; asm("v_fma_mix_f32 %0, %1, %2, %3 op_sel:[1,0,0] op_sel_hi:[1,0,0]" : "=v"(d) : "v"(w), "v"(g), "v"(b)); return d; }
DI float rbf(float x) { return bflo(pack2(x, x)); }
DI u32x4 pack8(const f32x16& x, int s) {
  u32x4 p;
  p[0] = pack2(x[8 * s + 0], x[8 * s + 1]);
  p[1] = pack2(x[8 * s + 2], x[8 * s + 3]);
  p[2] = pack2(x[8 * s + 4], x[8 * s + 5]);
  p[3] = pack2(x[8 * s + 6], x[8 * s + 7]);
  return p;
}
DI float xhalf_add(float v) {
  auto rr = __builtin_amdgcn_permlane32_swap(__float_as_uint(v), __float_as_uint(v), false, false);
  return __uint_as_float(rr[0]) + __uint_as_float(rr[1]);
}
DI float xhalf_max(float v) {
  auto rr = __builtin_amdgcn_permlane32_swap(__float_as_uint(v), __float_as_uint(v), false, false);
  return fmaxf(__uint_as_float(rr[0]), __uint_as_float(rr[1]));
}
DI void dma1(const void* sbase, unsigned voff, unsigned lds_base) {
  unsigned sv;
  asm volatile("s_mov_b32 %0, m0\n\ts_mov_b32 m0, %2\n\ts_nop 0\n\tglobal_load_lds_dwordx4 %1, %3\n\ts_mov_b32 m0, %0"
               : "=&s"(sv) : "v"(voff), "s"(lds_base), "s"(sbase) : "memory");
}
DI void dma1c(const void* sbase, unsigned voff, unsigned lds_base) {
  unsigned sv;
  asm volatile("s_mov_b32 %0, m0\n\ts_mov_b32 m0, %2\n\ts_nop 0\n\tglobal_load_lds_dwordx4 %1, %3 sc1\n\ts_mov_b32 m0, %0"
               : "=&s"(sv) : "v"(voff), "s"(lds_base), "s"(sbase) : "memory");
}
DI void dma4c(const void* sbase, unsigned voff, unsigned lds_base) {
  unsigned sv;
  asm volatile("s_mov_b32 %0, m0\n\ts_mov_b32 m0, %2\n\ts_nop 0\n\t"
               "global_load_lds_dwordx4 %1, %3 sc1\n\tglobal_load_lds_dwordx4 %1, %3 offset:1024 sc1\n\t"
               "global_load_lds_dwordx4 %1, %3 offset:2048 sc1\n\tglobal_load_lds_dwordx4 %1, %3 offset:3072 sc1\n\t"
               "s_mov_b32 m0, %0" : "=&s"(sv) : "v"(voff), "s"(lds_base), "s"(sbase) : "memory");
}
DI void touch1(const void* sbase, unsigned voff, unsigned lds_base) {
  unsigned sv;
  asm volatile("s_mov_b32 %0, m0\n\ts_mov_b32 m0, %2\n\ts_nop 0\n\tglobal_load_lds_dword %1, %3\n\ts_mov_b32 m0, %0"
               : "=&s"(sv) : "v"(voff), "s"(lds_base), "s"(sbase) : "memory");
}
DI bf16x8 ldsfrag(lds_cptr p) { return *(const LAS bf16x8*)p; }
DI s16x4 trread(lds_cptr p) { return __builtin_amdgcn_ds_read_tr16_b64_v4i16((LAS s16x4*)p); }
DI bf16x8 trfrag(lds_cptr p0, lds_cptr p1) {
  s16x4 lo = trread(p0), hi = trread(p1);
  return __builtin_shufflevector(lo, hi, 0, 1, 2, 3, 4, 5, 6, 7);
}
DI float dot2bf(unsigned a, unsigned b, float c) {
  return __builtin_amdgcn_fdot2_f32_bf16(__builtin_bit_cast(bf16x2_t, a), __builtin_bit_cast(bf16x2_t, b), c, false);
}
DI unsigned wait_words(const char* p, int n, unsigned tag, int lane) {
  const gu64* f = (const gu64*)p + (lane < n ? lane : n - 1);
  const u64 t0 = __builtin_amdgcn_s_memrealtime();
  u64 v;
  for (;;) {
    v = __hip_atomic_load(f, RLX_AGENT);
    if (__all((unsigned)(v >> 32) == tag)) break;
    if (__builtin_amdgcn_s_memrealtime() - t0 > 2000000ull) break;
    __builtin_amdgcn_s_sleep(1);
  }
  return (unsigned)v;
}
DI void put_word(char* p, unsigned tag, unsigned val) { __hip_atomic_store((gu64*)p, ((u64)tag << 32) | val, RLX_AGENT); }

constexpr int L_XS = 0;
constexpr int L_WKV = 0;
constexpr int L_XCH = 65536;
constexpr int L_WQ = 114688;
constexpr int L_MODW = 143360;
constexpr int L_SILU = 157696;
constexpr int L_STAT = 143360;
constexpr int L_GB = 145408;
constexpr int L_KMX = 158592;
constexpr int L_LOW = 0;
constexpr int L_M2 = 57344;
constexpr int L_GA = L_M2, L_G2 = L_M2 + 1792, L_GBV = L_M2 + 3584, L_PK = L_M2 + 5376;
constexpr int L_SM = L_M2 + 7168;
constexpr int L_HI = 65536;
constexpr int LDS_BYTES = 160 * 1024;

#define ASTEPH(SC, SN, KADDR, V1ADDR, V0ADDR, H1, H2, H3, H4) do { \
    SN = MFMA32(kf0, qf0, zero16); SBAR(); \
    pw1[2] = pack2(e12, e13); pw1[3] = pack2(e14, e15); \
    kf0 = ldsfrag(KADDR); SBAR(); \
    SC[0] = EX2(SC[0]); SC[1] = EX2(SC[1]); SBAR(); H1; SBAR(); SC[2] = EX2(SC[2]); SC[3] = EX2(SC[3]); PIN(SC); PIN(pw1); SBAR(); \
    o = MFMA32(vf1, BC8(pw1), o); SBAR(); \
    vf1 = ldsfrag(V1ADDR); \
    SC[4] = EX2(SC[4]); SC[5] = EX2(SC[5]); SBAR(); pw0[0] = pack2(SC[0], SC[1]); pw0[1] = pack2(SC[2], SC[3]); SBAR(); H2; SBAR(); SC[6] = EX2(SC[6]); SC[7] = EX2(SC[7]); PIN(SC); PIN(pw0); SBAR(); \
    SN = MFMA32(kf1, qf1, SN); SBAR(); \
    kf1 = ldsfrag((KADDR) + 1024); \
    SC[8] = EX2(SC[8]); SC[9] = EX2(SC[9]); SBAR(); pw0[2] = pack2(SC[4], SC[5]); pw0[3] = pack2(SC[6], SC[7]); SBAR(); H3; SBAR(); SC[10] = EX2(SC[10]); SC[11] = EX2(SC[11]); PIN(SC); PIN(pw0); SBAR(); \
    o = MFMA32(vf0, BC8(pw0), o); SBAR(); \
    vf0 = ldsfrag(V0ADDR); \
    e12 = EX2(SC[12]); e13 = EX2(SC[13]); SBAR(); pw1[0] = pack2(SC[8], SC[9]); pw1[1] = pack2(SC[10], SC[11]); SBAR(); H4; SBAR(); e14 = EX2(SC[14]); e15 = EX2(SC[15]); PIN(e12); PIN(e13); PIN(e14); PIN(e15); PIN(pw1); SBAR(); \
  } while (0)
#define NOH ((void)0)
#define ASTEP(SC, SN, KADDR, V1ADDR, V0ADDR) ASTEPH(SC, SN, KADDR, V1ADDR, V0ADDR, NOH, NOH, NOH, NOH)

#define ATTN_PROLOGUE() \
    kf0 = ldsfrag(abase); kf1 = ldsfrag(abase + 1024); \
    vf0 = ldsfrag(abase + 2048); vf1 = vf0; \
    e12 = 0.f; e13 = 0.f; e14 = 0.f; e15 = 0.f; pw1[0] = 0; pw1[1] = 0; o = zero16; \
    sA = MFMA32(kf0, qf0, zero16); sA = MFMA32(kf1, qf1, sA); \
    kf0 = ldsfrag(abase + 4096); kf1 = ldsfrag(abase + 4096 + 1024);

__global__ void __launch_bounds__(512) fused_block(const u16* __restrict__ x, const u16* __restrict__ wqkv, const u16* __restrict__ wo,
                                                    const u16* __restrict__ w1, const u16* __restrict__ w2, const u16* __restrict__ cvec,
                                                    const u16* __restrict__ wmod, u16* __restrict__ out, char* __restrict__ ws) {
  __shared__ __attribute__((aligned(16))) char lds_arr[LDS_BYTES];
  const lds_ptr L = (lds_ptr)lds_arr;
  const unsigned lds0 = (unsigned)(uintptr_t)lds_arr;
  const int t = threadIdx.x, lane = t & 63, wv = __builtin_amdgcn_readfirstlane(t >> 6), r = lane & 31, h = lane >> 5;
  __builtin_assume(wv < 8);
  const int g16 = (lane >> 4) & 1, tq = (lane & 15) >> 2, tp = lane & 3;
  const int b = blockIdx.x, xg = b & 7, rk = b >> 3;
  const __amdgpu_buffer_rsrc_t rs = __builtin_amdgcn_make_buffer_rsrc(ws, 0, WS_USED, 0x00020000);
  asm volatile("" :: "s"(x), "s"(wqkv), "s"(wo), "s"(w1), "s"(w2), "s"(cvec), "s"(wmod), "s"(out), "s"(ws));
  STAMP_DECL();
  STAMP(0);

  const int rt = xg * 2 + (rk >> 4), hq = rk & 15;
  const bool modwave = (b < 168) && (wv == 0);
  u32x4 craw = {0, 0, 0, 0};
  if (modwave) {
    asm volatile("global_load_dwordx4 %0, %1, %2" : "=v"(craw) : "v"((lane < 56 ? lane : 55) * 16), "s"(cvec) : "memory");
    const unsigned vo = (unsigned)(lane >> 1) * (6 * DM * 2) + (lane & 1) * 16;
#pragma unroll
    for (int i = 0; i < 14; ++i) dma1(wmod + b * 16 + i * 32 * 6 * DM, vo, lds0 + L_MODW + i * 1024);
  }
  const int a_tt = wv & 3, a_kh = wv >> 2;
  if (wv > 0) {
    const u16* xb = x + (size_t)(rt * 128) * DM;
    const u16* xw = xb + (wv - 1) * 16 * DM;
    const unsigned dw = lds0 + L_XS + (wv - 1) * 14336;
#pragma unroll
    for (int i = 0; i < 7; ++i) {
      const int q = i * 64 + lane, rl = (q * 1171) >> 16, c = q - rl * 56;
      const unsigned vo = rl * (DM * 2) + ((c ^ (rl >> 1)) * 16);
      dma1(xw, vo, dw + i * 1024);
      dma1(xw + 8 * DM, vo ^ 64, dw + 7168 + i * 1024);
    }
    const int q = (wv - 1) * 64 + lane, rl = (q * 1171) >> 16, c = q - rl * 56;
    const unsigned vo = rl * (DM * 2) + ((c ^ (rl >> 1)) * 16);
    dma1(xb + 112 * DM, vo, lds0 + L_XS + 14 * 7168 + (wv - 1) * 1024);
    dma1(xb + 120 * DM, vo ^ 64, lds0 + L_XS + 15 * 7168 + (wv - 1) * 1024);
  }
  u64 epoch;
  asm volatile("s_load_dwordx2 %0, %1, 0x0" : "=s"(epoch) : "s"(ws) : "memory");
  const unsigned wvo = (lane >> 2) * (3 * DM * 2) + ((tp == 3) ? 40 : tp * 16);
  if (t == 0) *(LAS unsigned*)(L + L_KMX) = 0;
  asm volatile("s_waitcnt lgkmcnt(0)" : "+s"(epoch) :: "memory");
  const unsigned tag = (unsigned)epoch + 1u;
  if (wv > 0) asm volatile("s_waitcnt vmcnt(0)" ::: "memory");
  if (modwave) {
    asm volatile("s_waitcnt vmcnt(14)" : "+v"(craw) :: "memory");
    u32x4 sv;
#pragma unroll
    for (int j = 0; j < 4; ++j) {
      const float c0_ = bflo(craw[j]), c1_ = bfhi(craw[j]);
      sv[j] = pack2(c0_ * __builtin_amdgcn_rcpf(1.f + EX2(-1.4426950408889634f * c0_)), c1_ * __builtin_amdgcn_rcpf(1.f + EX2(-1.4426950408889634f * c1_)));
    }
    if (lane < 56) *(LAS u32x4*)(L + L_SILU + lane * 16) = sv;
    const int kg = lane >> 4;
    const lds_cptr bp = L + L_MODW + (8 * kg + tq) * 32 + tp * 8;
    const lds_cptr ap = L + L_SILU + kg * 16;
    bf16x8 af[14], bfr[14];
    asm volatile("s_waitcnt lgkmcnt(0)" ::: "memory");
#pragma unroll
    for (int s = 0; s < 14; ++s) af[s] = ldsfrag(ap + s * 64);
    asm volatile("s_waitcnt vmcnt(0)" ::: "memory");
#pragma unroll
    for (int s = 0; s < 14; ++s) bfr[s] = trfrag(bp + s * 1024, bp + s * 1024 + 128);
    f32x4 acc0 = {0.f, 0.f, 0.f, 0.f}, acc1 = acc0;
#pragma unroll
    for (int s = 0; s < 14; s += 2) { acc0 = MFMA16(af[s], bfr[s], acc0); acc1 = MFMA16(af[s + 1], bfr[s + 1], acc1); }
    if (lane < 16) put_word(ws + OFF_MODG + (b * 16 + lane) * 8, tag, __float_as_uint(rbf(acc0[0] + acc1[0])));
  }
  STAMP(1);
  BAR_VM(1);
  u32x4 xr[14];
  {
    const int row = a_tt * 32 + r, gsw = (row >> 1) & 7, c0 = a_kh * 28 + h;
#pragma unroll
    for (int i = 0; i < 4; ++i) {
      const int c = c0 + 2 * i;
      const lds_cptr xa = L + L_XS + row * (DM * 2) + (c & ~7) * 16 + (((c & 7) ^ gsw) * 16);
#pragma unroll
      for (int s = i; s < 14; s += 4) xr[s] = *(const LAS u32x4*)(xa + (s >> 2) * 128);
    }
  }
  u32x4 mg;
  asm volatile("global_load_dwordx4 %0, %1, %2 offset:0 sc1" : "=v"(mg) : "v"((t < DM ? t : DM - 1) * 16), "s"(ws + OFF_MODG) : "memory");
  {
    const int w7 = wv < 7 ? wv : 6;
#pragma unroll
    for (int i = 0; i < 4; ++i) dma1(wqkv + hq * DH + (w7 * 4 + i) * 16 * 3 * DM, wvo, lds0 + L_WQ + (w7 * 4 + i) * 1024);
  }
  BAR_LGKM();
  STAMP(2);
  {
    const u16* wsrc = wqkv + hq * DH + (1 + (wv >> 2)) * DM + (wv & 3) * 7 * 16 * 3 * DM;
    float s1a = 0.f, s2a = 0.f, s1b = 0.f, s2b = 0.f;
#pragma unroll
    for (int s = 0; s < 14; ++s) {
      if (s == 3 || s == 8 || s == 13) dma1(wsrc + (s / 5) * 16 * 3 * DM, wvo, lds0 + L_WKV + (wv * 7 + (s / 5)) * 1024);
      s1a = dot2bf(xr[s][0], 0x3f803f80u, s1a); s2a = dot2bf(xr[s][0], xr[s][0], s2a);
      s1b = dot2bf(xr[s][1], 0x3f803f80u, s1b); s2b = dot2bf(xr[s][1], xr[s][1], s2b);
      s1a = dot2bf(xr[s][2], 0x3f803f80u, s1a); s2a = dot2bf(xr[s][2], xr[s][2], s2a);
      s1b = dot2bf(xr[s][3], 0x3f803f80u, s1b); s2b = dot2bf(xr[s][3], xr[s][3], s2b);
      PIN(s1a); PIN(s2a); PIN(s1b); PIN(s2b); SBAR();
    }
    const float s1 = xhalf_add(s1a + s1b), s2 = xhalf_add(s2a + s2b);
    if (h == 0) { f32x2 v = {s1, s2}; *(LAS f32x2*)(L + L_STAT + (wv * 32 + r) * 8) = v; }
    asm volatile("s_waitcnt vmcnt(7)" : "+v"(mg) :: "memory");
    if (wv < 7) {
      if (!__all(mg[1] == tag && mg[3] == tag)) {
        const u64 t0 = __builtin_amdgcn_s_memrealtime();
        for (;;) {
          mg = __builtin_amdgcn_raw_buffer_load_b128(rs, OFF_MODG + t * 16, 0, 16);
          if (__all(mg[1] == tag && mg[3] == tag)) break;
          if (__builtin_amdgcn_s_memrealtime() - t0 > 2000000ull) break;
          __builtin_amdgcn_s_sleep(1);
        }
      }
      f32x2 o2 = {__uint_as_float(mg[0]), __uint_as_float(mg[2])};
      const bool isg = t >= 224;
      if (isg) { o2[0] = rbf(1.f + o2[0]); o2[1] = rbf(1.f + o2[1]); }
      *(LAS f32x2*)(L + L_GB + (isg ? 2 * t - DM : DM + 2 * t) * 4) = o2;
    }
    BAR_LGKM();
    STAMP(3);
    const lds_cptr gp = L + L_GB + (a_kh * 224 + 8 * h) * 4;
    f32x4 gq[3][4];
#define LOADGB(slot, s) do { gq[slot][0] = *(const LAS f32x4*)(gp + (s) * 64); gq[slot][1] = *(const LAS f32x4*)(gp + (s) * 64 + 16); \
      gq[slot][2] = *(const LAS f32x4*)(gp + DM * 4 + (s) * 64); gq[slot][3] = *(const LAS f32x4*)(gp + DM * 4 + (s) * 64 + 16); } while (0)
    const f32x2 o2 = *(const LAS f32x2*)(L + L_STAT + ((wv ^ 4) * 32 + r) * 8);
    LOADGB(0, 0); LOADGB(1, 1);
    const float mu = (s1 + o2[0]) * (1.f / DM);
    const float var = fmaxf((s2 + o2[1]) * (1.f / DM) - mu * mu, 0.f);
    const float rstd = __builtin_amdgcn_rsqf(var + 1e-5f);
    const float nb = -mu * rstd;
    SBAR();
#pragma unroll
    for (int s = 0; s < 14; ++s) {
      if (s + 2 < 14) LOADGB((s + 2) % 3, s + 2);
      if (s == 1 || s == 4 || s == 7 || s == 10) dma1(wsrc + (3 + s / 3) * 16 * 3 * DM, wvo, lds0 + L_WKV + (wv * 7 + 3 + s / 3) * 1024);
      SBAR();
      const f32x4 g0 = gq[s % 3][0], g1 = gq[s % 3][1], b0 = gq[s % 3][2], b1 = gq[s % 3][3];
      xr[s][0] = pack2((bflo(xr[s][0]) * rstd + nb) * g0[0] + b0[0], (bfhi(xr[s][0]) * rstd + nb) * g0[1] + b0[1]);
      xr[s][1] = pack2((bflo(xr[s][1]) * rstd + nb) * g0[2] + b0[2], (bfhi(xr[s][1]) * rstd + nb) * g0[3] + b0[3]);
      xr[s][2] = pack2((bflo(xr[s][2]) * rstd + nb) * g1[0] + b1[0], (bfhi(xr[s][2]) * rstd + nb) * g1[1] + b1[1]);
      xr[s][3] = pack2((bflo(xr[s][3]) * rstd + nb) * g1[2] + b1[2], (bfhi(xr[s][3]) * rstd + nb) * g1[3] + b1[3]);
      PIN(xr[s]); SBAR();
    }
  }
  f32x16 zero16;
#pragma unroll
  for (int i = 0; i < 16; ++i) zero16[i] = 0.f;
  f32x16 aq = zero16, ak = zero16, av = zero16;
  {
    const int wro = a_kh * 14 * 1024 + (8 * h + tq) * 64 + g16 * 32 + tp * 8;
    const lds_cptr wqp = L + L_WQ + wro, wkp = L + L_WKV + wro, wvp = L + L_WKV + 28672 + wro;
    bf16x8 wa[14], wb[14];
    BAR_VM(7);
#pragma unroll
    for (int s = 0; s < 14; ++s) wa[s] = trfrag(wqp + s * 1024, wqp + s * 1024 + 256);
    SBAR();
#pragma unroll
    for (int s = 0; s < 7; ++s) { aq = MFMA32(wa[s], BC8(xr[s]), aq); PIN(aq); SBAR(); }
    BAR_VM(0);
    STAMP(4);
#pragma unroll
    for (int s = 7; s < 14; ++s) {
      aq = MFMA32(wa[s], BC8(xr[s]), aq); PIN(aq); SBAR();
      wb[2 * (s - 7)] = trfrag(wkp + 2 * (s - 7) * 1024, wkp + 2 * (s - 7) * 1024 + 256);
      wb[2 * (s - 7) + 1] = trfrag(wkp + (2 * (s - 7) + 1) * 1024, wkp + (2 * (s - 7) + 1) * 1024 + 256); SBAR();
    }
#pragma unroll
    for (int s = 0; s < 14; ++s) {
      ak = MFMA32(wb[s], BC8(xr[s]), ak); PIN(ak); SBAR();
      wa[s] = trfrag(wvp + s * 1024, wvp + s * 1024 + 256); SBAR();
    }
#pragma unroll
    for (int s = 0; s < 14; ++s) { av = MFMA32(BC8(xr[s]), wa[s], av); PIN(av); SBAR(); }
  }
  STAMP(5);
  {
    const lds_ptr xw = L + L_XCH + wv * 6144 + lane * 16;
    const lds_cptr xrd = L + L_XCH + (wv ^ 4) * 6144 + lane * 16;
    const int tt = rt * 4 + a_tt;
    const bool vpad = (r >= 24 && r < 28);
    const unsigned vconst = (r == 24) ? 0x3f803f80u : 0u;
#define W4(off, A, i0) do { f32x4 v_ = {A[i0], A[(i0) + 1], A[(i0) + 2], A[(i0) + 3]}; *(LAS f32x4*)(xw + (off)) = v_; } while (0)
#define R4(off, A, i0) do { const f32x4 v_ = *(const LAS f32x4*)(xrd + (off)); A[i0] += v_[0]; A[(i0) + 1] += v_[1]; A[(i0) + 2] += v_[2]; A[(i0) + 3] += v_[3]; } while (0)
    if (a_kh == 0) {
      W4(0, ak, 0); W4(1024, ak, 4); W4(2048, ak, 8); W4(3072, ak, 12); W4(4096, av, 8); W4(5120, av, 12);
      BAR_LGKM();
      R4(0, aq, 0); R4(1024, aq, 4); R4(2048, aq, 8); R4(3072, aq, 12); R4(4096, av, 0); R4(5120, av, 4);
      const float qs = 0.18898223650461362f * 1.4426950408889634f;
#pragma unroll
      for (int i = 0; i < 16; ++i) aq[i] *= qs;
      u32x4 f0 = pack8(aq, 0), f1 = pack8(aq, 1), vf = pack8(av, 0);
      if (h == 0) { f1[2] = 0; f1[3] = 0; }
#pragma unroll
      for (int i = 0; i < 4; ++i) vf[i] = vpad ? vconst : vf[i];
      const unsigned mo = OFF_Q + (hq * NTT + tt) * 2048 + lane * 16;
      __builtin_amdgcn_raw_buffer_store_b128(f0, rs, mo, 0, 16);
      __builtin_amdgcn_raw_buffer_store_b128(f1, rs, mo + 1024, 0, 16);
      __builtin_amdgcn_raw_buffer_store_b128(vf, rs, OFF_KV + ((hq * NTT + tt) * 4 + 2) * 1024 + lane * 16, 0, 16);
    } else {
      W4(0, aq, 0); W4(1024, aq, 4); W4(2048, aq, 8); W4(3072, aq, 12); W4(4096, av, 0); W4(5120, av, 4);
      BAR_LGKM();
      R4(0, ak, 0); R4(1024, ak, 4); R4(2048, ak, 8); R4(3072, ak, 12); R4(4096, av, 8); R4(5120, av, 12);
      u32x4 f0 = pack8(ak, 0), f1 = pack8(ak, 1), vf = pack8(av, 1);
      u32x4 f1z = f1;
      if (h == 0) { f1z[2] = 0; f1z[3] = 0; f1[2] = 0x3f803f80u; f1[3] = 0x00003f80u; }
#pragma unroll
      for (int i = 0; i < 4; ++i) vf[i] = vpad ? vconst : vf[i];
      const unsigned mo = OFF_KV + (hq * NTT + tt) * 4096 + lane * 16;
      __builtin_amdgcn_raw_buffer_store_b128(f0, rs, mo, 0, 16);
      __builtin_amdgcn_raw_buffer_store_b128(f1, rs, mo + 1024, 0, 16);
      __builtin_amdgcn_raw_buffer_store_b128(vf, rs, mo + 3072, 0, 16);
      float kn = 0.f;
#pragma unroll
      for (int i = 0; i < 4; ++i) { kn = dot2bf(f0[i], f0[i], kn); kn = dot2bf(f1z[i], f1z[i], kn); }
      kn = xhalf_add(kn);
      asm volatile("ds_max_u32 %0, %1" :: "v"(lds0 + L_KMX), "v"(__float_as_uint(kn)) : "memory");
    }
    BAR_ALL();
    if (t == 0) put_word(ws + OFF_FKV + (hq * 16 + rt) * 8, tag, *(const LAS unsigned*)(L + L_KMX));
  }
  STAMP(6);
  const int head = xg * 2 + (rk >> 4), qt = rk & 15;
  const int tg = xg * 4 + (rk >> 3), sl = rk & 7;
  const int qb = wv & 3, kh = wv >> 2;
  unsigned w1vo[4];
#pragma unroll
  for (int i = 0; i < 4; ++i) { const int pc = (wv + 8 * i < 28) ? wv + 8 * i : wv; const int q = pc * 64 + lane, row = q / 28, c = q - row * 28; w1vo[i] = row * (FF * 2) + c * 16; }
  const u16* w1b = w1 + sl * 224;
  const lds_cptr w1rd = L + L_LOW + (4 * h + tq) * 448 + wv * 64 + g16 * 32 + tp * 8;
  const unsigned w1d3 = (wv < 4) ? 24576 : 0;
#define W1_DMA(c, buf) do { const u16* src_ = w1b + (size_t)(c) * 64 * FF; const unsigned dst_ = lds0 + L_LOW + (buf) * 28672 + wv * 1024; \
    dma1(src_, w1vo[0], dst_); dma1(src_, w1vo[1], dst_ + 8192); dma1(src_, w1vo[2], dst_ + 16384); dma1(src_, w1vo[3], dst_ + w1d3); } while (0)
  if (wv == 7) {
    float km = __uint_as_float(wait_words(ws + OFF_FKV + head * 128, 16, tag, lane));
#pragma unroll
    for (int m = 8; m >= 1; m >>= 1) km = fmaxf(km, __shfl_xor(km, m));
    if (lane == 0) *(LAS float*)(L + L_SM) = km;
  } else {
    const u64 t0 = __builtin_amdgcn_s_memrealtime();
    u32x4 v0, v1;
    for (;;) {
      v0 = __builtin_amdgcn_raw_buffer_load_b128(rs, OFF_MODG + 7168 + t * 16, 0, 16);
      v1 = __builtin_amdgcn_raw_buffer_load_b128(rs, OFF_MODG + 14336 + t * 16, 0, 16);
      if (__all(v0[1] == tag && v0[3] == tag && v1[1] == tag && v1[3] == tag)) break;
      if (__builtin_amdgcn_s_memrealtime() - t0 > 2000000ull) break;
      __builtin_amdgcn_s_sleep(1);
    }
    const int e = (t < 224) ? 2 * t : 2 * t - DM;
    const int pk = (e >> 4) * 32 + ((e >> 2) & 1) * 16 + ((e >> 3) & 1) * 8 + (e & 3) * 2;
    if (t < 224) {
      f32x2 a = {__uint_as_float(v0[0]), __uint_as_float(v0[2])};
      f32x2 g = {rbf(1.f + __uint_as_float(v1[0])), rbf(1.f + __uint_as_float(v1[2]))};
      *(LAS f32x2*)(L + L_GA + e * 4) = a;
      *(LAS f32x2*)(L + L_G2 + e * 4) = g;
      *(LAS unsigned*)(L + L_PK + pk) = pack2(g[0], g[1]);
    } else {
      f32x2 g = {__uint_as_float(v1[0]), __uint_as_float(v1[2])};
      *(LAS f32x2*)(L + L_GBV + e * 4) = g;
      *(LAS unsigned*)(L + L_PK + 896 + pk) = pack2(__uint_as_float(v0[0]), __uint_as_float(v0[2]));
    }
  }
  BAR_LGKM();
  STAMP(7);
  const char* kvsrc = ws + OFF_KV + (size_t)(head * NTT + kh * 32 + qb) * 4096;
  const unsigned kvdst = lds0 + L_HI + kh * 16384 + qb * 4096;
  const unsigned l16 = lane * 16;
#define DMA_KV(i, buf) dma4c(kvsrc + (size_t)(i) * 16384, l16, kvdst + (buf))
  const unsigned w1to = (unsigned)(((rk >> 3) * 16 + wv * 2 + ((lane >> 2) & 1)) * (FF * 2) + ((sl * 448) & ~127) + (lane & 3) * 128);
#define W1_TOUCH(c) touch1(w1 + (size_t)(c) * 64 * FF, w1to, lds0 + L_SM + 256)
  bf16x8 qf0, qf1;
  {
    u32x4 q0, q1;
    asm volatile("global_load_dwordx4 %0, %2, %3 sc1\n\tglobal_load_dwordx4 %1, %2, %3 offset:1024 sc1" : "=&v"(q0), "=&v"(q1)
                 : "v"(l16), "s"(ws + OFF_Q + (head * NTT + qt * 4 + qb) * 2048) : "memory");
    DMA_KV(0, 0);
    DMA_KV(1, 32768);
    W1_DMA(0, 0);
    const float km = *(const LAS float*)(L + L_SM);
    asm volatile("s_waitcnt vmcnt(12)" : "+v"(q0), "+v"(q1) :: "memory");
    float qn = 0.f;
#pragma unroll
    for (int i = 0; i < 4; ++i) { qn = dot2bf(q0[i], q0[i], qn); qn = dot2bf(q1[i], q1[i], qn); }
    qn = xhalf_add(qn);
    const float bound = __builtin_amdgcn_sqrtf(qn * km) * 1.004f + 1e-30f;
    if (h == 0) q1[2] = ((__float_as_uint(bound) + 0xffffu) >> 16) | 0x8000u;
    qf0 = BC8(q0); qf1 = BC8(q1);
  }
  bf16x8 wf[28];
  f32x16 o, sA, sB;
  u32x4 pw0, pw1;
  bf16x8 kf0, kf1, vf0, vf1;
  float e12, e13, e14, e15;
  const lds_cptr abase = L + L_HI + kh * 16384 + l16;
  BAR_VM(8);
  STAMP(8);
  ATTN_PROLOGUE();
#define W1_RD1(c, i_) (wf[4 * (c) + (i_)] = trfrag(w1rd + ((c) & 1) * 28672 + (i_) * 16 * 448, w1rd + ((c) & 1) * 28672 + ((i_) * 16 + 8) * 448))
#define W1_READ(c) do { W1_RD1(c, 0); W1_RD1(c, 1); W1_RD1(c, 2); W1_RD1(c, 3); } while (0)
#define KV1(st, j) do { if ((st) < 8) dma1c(kvsrc + (size_t)(st) * 16384 + (j) * 1024, l16, kvdst + ((st) % 3) * 32768 + (j) * 1024); } while (0)
#define W1D1(c, j) do { if ((c) >= 0) dma1(w1b + (size_t)(c) * 64 * FF, w1vo[j], lds0 + L_LOW + ((c) & 1) * 28672 + wv * 1024 + ((j) < 3 ? (j) * 8192 : w1d3)); } while (0)
#define W1R1(c, j) do { if ((c) >= 0) W1_RD1(((c) >= 0 ? (c) : 0), j); } while (0)
#define STAGE(st, NW, RC, DC, TCH) do { \
    constexpr int cur = ((st) % 3) * 32768, nxt = (((st) + 1) % 3) * 32768; \
    const lds_cptr bc = abase + cur, bn = abase + nxt; \
    ASTEP(sA, sB, bc + 2 * 4096, bc + 0 * 4096 + 3072, bc + 1 * 4096 + 2048); \
    ASTEP(sB, sA, bc + 3 * 4096, bc + 1 * 4096 + 3072, bc + 2 * 4096 + 2048); \
    BAR_VM(NW); \
    ASTEPH(sA, sB, bn + 0 * 4096, bc + 2 * 4096 + 3072, bc + 3 * 4096 + 2048, KV1((st) + 2, 0); W1R1(RC, 0), KV1((st) + 2, 1); W1R1(RC, 1), KV1((st) + 2, 2); W1R1(RC, 2), KV1((st) + 2, 3); W1R1(RC, 3)); \
    ASTEPH(sB, sA, bn + 1 * 4096, bc + 3 * 4096 + 3072, bn + 0 * 4096 + 2048, W1D1(DC, 0), W1D1(DC, 1), W1D1(DC, 2), W1D1(DC, 3); TCH); \
  } while (0)
  const int tl = wv * 9 + (lane < 9 ? lane : 8);
#define TOUCH_WO(i) touch1(wo, (rk * 98 + ((i) * 72 + tl < 98 ? (i) * 72 + tl : 97)) * 128, lds0 + L_SM + 256)
#define TOUCH_W2(i) touch1(w2, (rk * 392 + ((i) * 72 + tl < 392 ? (i) * 72 + tl : 391)) * 128, lds0 + L_SM + 256)
  STAGE(0, 4, -1, 1, TOUCH_WO(0));
  STAGE(1, 5, 0, -1, TOUCH_WO(1); TOUCH_W2(0));
  STAGE(2, 2, 1, 2, TOUCH_W2(1));
  STAGE(3, 5, -1, 3, TOUCH_W2(2));
  STAGE(4, 5, 2, -1, TOUCH_W2(3); W1_TOUCH(6));
  STAGE(5, 2, 3, 4, TOUCH_W2(4));
  STAGE(6, 5, -1, 5, TOUCH_W2(5));
  STAGE(7, 5, 4, -1, NOH);
  STAMP(9);
  float fin[8];
  float lsum;
#pragma unroll 1
  for (int pass = 0; pass < 2; ++pass) {
    pw1[2] = pack2(e12, e13); pw1[3] = pack2(e14, e15);
    o = MFMA32(vf1, BC8(pw1), o);
    constexpr int L_X = L_HI + 65536;
    {
      f32x4 a, b2;
      if (kh) {
#pragma unroll
        for (int i = 0; i < 4; ++i) { a[i] = o[i]; b2[i] = o[4 + i]; fin[i] = o[8 + i]; fin[4 + i] = o[12 + i]; }
      } else {
#pragma unroll
        for (int i = 0; i < 4; ++i) { a[i] = o[8 + i]; b2[i] = o[12 + i]; fin[i] = o[i]; fin[4 + i] = o[4 + i]; }
      }
      *(LAS f32x4*)(L + L_X + 8192 + (wv * 2 + 0) * 1024 + l16) = a;
      *(LAS f32x4*)(L + L_X + 8192 + (wv * 2 + 1) * 1024 + l16) = b2;
      if (h == 0) *(LAS float*)(L + L_X + 24576 + (wv * 32 + r) * 4) = o[12];
    }
    BAR_LGKM();
    {
      const int pw = wv ^ 4;
      const f32x4 a = *(const LAS f32x4*)(L + L_X + 8192 + (pw * 2 + 0) * 1024 + l16);
      const f32x4 b2 = *(const LAS f32x4*)(L + L_X + 8192 + (pw * 2 + 1) * 1024 + l16);
      const float c0 = *(const LAS float*)(L + L_X + 24576 + lane * 4) + *(const LAS float*)(L + L_X + 24576 + (128 + lane) * 4);
      const float c1 = *(const LAS float*)(L + L_X + 24576 + (64 + lane) * 4) + *(const LAS float*)(L + L_X + 24576 + (192 + lane) * 4);
#pragma unroll
      for (int i = 0; i < 4; ++i) { fin[i] += a[i]; fin[4 + i] += b2[i]; }
      lsum = *(const LAS float*)(L + L_X + 24576 + (qb * 32 + r) * 4) + *(const LAS float*)(L + L_X + 24576 + ((qb + 4) * 32 + r) * 4);
      const bool ok = (c0 >= 9.094947e-13f) && (c1 >= 9.094947e-13f);
      if (pass == 1 || __all(ok)) break;
    }
    {
      u32x4 q1 = __builtin_bit_cast(u32x4, qf1);
      if (h == 0) { q1[2] = 0; q1[3] = 0; }
      qf1 = BC8(q1);
      float mx = -INFINITY;
      const unsigned ko = OFF_KV + (head * NTT + kh * 32) * 4096 + l16;
#pragma unroll 1
      for (int kt = 0; kt < 32; ++kt) {
        const u32x4 k0 = __builtin_amdgcn_raw_buffer_load_b128(rs, ko + kt * 4096, 0, 16);
        const u32x4 k1 = __builtin_amdgcn_raw_buffer_load_b128(rs, ko + kt * 4096 + 1024, 0, 16);
        f32x16 s = MFMA32(BC8(k0), qf0, zero16);
        s = MFMA32(BC8(k1), qf1, s);
#pragma unroll
        for (int i = 0; i < 16; ++i) mx = fmaxf(mx, s[i]);
      }
      mx = xhalf_max(mx);
      if (h == 0) *(LAS float*)(L + L_X + 28672 + (wv * 32 + r) * 4) = mx;
      BAR_LGKM();
      mx = fmaxf(*(const LAS float*)(L + L_X + 28672 + (qb * 32 + r) * 4), *(const LAS float*)(L + L_X + 28672 + ((qb + 4) * 32 + r) * 4));
      const float m0 = rbf(mx), m1 = rbf(mx - m0), m2 = rbf(mx - m0 - m1);
      if (h == 0) { q1[2] = pack2(-m0, -m1); q1[3] = pack2(-m2, 0.f); }
      qf1 = BC8(q1);
      DMA_KV(0, 0);
      DMA_KV(1, 32768);
      asm volatile("s_waitcnt vmcnt(4) lgkmcnt(0)\n\ts_barrier" ::: "memory");
      ATTN_PROLOGUE();
      int cur = 0, nxt = 32768, nn = 65536;
#pragma unroll 1
      for (int st = 0; st < 8; ++st) {
        const lds_cptr bc = abase + cur, bn = abase + nxt;
        ASTEP(sA, sB, bc + 2 * 4096, bc + 0 * 4096 + 3072, bc + 1 * 4096 + 2048);
        ASTEP(sB, sA, bc + 3 * 4096, bc + 1 * 4096 + 3072, bc + 2 * 4096 + 2048);
        BAR_VM(0);
        if (st + 2 < 8) DMA_KV(st + 2, nn);
        ASTEP(sA, sB, bn + 0 * 4096, bc + 2 * 4096 + 3072, bc + 3 * 4096 + 2048);
        ASTEP(sB, sA, bn + 1 * 4096, bc + 3 * 4096 + 3072, bn + 0 * 4096 + 2048);
        const int tmp = cur; cur = nxt; nxt = nn; nn = tmp;
      }
    }
  }
  {
    const float inv = __builtin_amdgcn_rcpf(lsum);
    u32x4 ov;
#pragma unroll
    for (int i = 0; i < 4; ++i) ov[i] = pack2(fin[2 * i] * inv, fin[2 * i + 1] * inv);
    if (kh == 1 && h == 0) { ov[2] = 0; ov[3] = 0; }
    __builtin_amdgcn_raw_buffer_store_b128(ov, rs, OFF_O + ((qt * 4 + qb) * 32 + head * 2 + kh) * 1024 + l16, 0, 16);
    BAR_ALL();
    if (t == 0) put_word(ws + OFF_FO + (qt * 16 + head) * 8, tag, 0);
  }
  STAMP(10);
  const int tb = wv & 1, kq = wv >> 1;
  const int tt_own = tg * 2 + tb;
  const int n0 = (sl * 2 + (kq >> 1)) * 32 + (kq & 1) * 16 + 4 * h;
  const unsigned rvo = (lane >> 3) * (DM * 2) + (((lane & 7) ^ (((lane >> 4) & 1) << 2)) * 16);
  const int rsw = ((tq >> 1) & 1) * 64;
  const lds_ptr redb = L + L_HI + tb * 32768 + l16;
  u32x2 xa2, xb2;
  unsigned xc0;
  W1_READ(5);
  {
    const u16* src_ = w1b + (size_t)6 * 64 * FF; const unsigned dst_ = lds0 + L_HI + 65536 + wv * 1024;
    dma1(src_, w1vo[0], dst_); dma1(src_, w1vo[1], dst_ + 8192); dma1(src_, w1vo[2], dst_ + 16384); dma1(src_, w1vo[3], dst_ + w1d3);
  }
  BAR_LGKM();
  if (sl < 7) {
#pragma unroll
    for (int i = 0; i < 7; ++i) { const int blk = wv * 7 + i; dma1(wo + sl * 64 + blk * 8 * DM, rvo, lds0 + L_LOW + blk * 1024); }
    asm volatile("global_load_dwordx2 %0, %3, %5\n\tglobal_load_dwordx2 %1, %3, %5 offset:16\n\tglobal_load_ushort %2, %4, %5"
                 : "=&v"(xa2), "=&v"(xb2), "=&v"(xc0) : "v"((r * DM + n0) * 2), "v"(r * DM * 2), "s"(x + (size_t)tt_own * 32 * DM) : "memory");
  }
  f32x16 uv = zero16;
  const lds_cptr pp = L + L_PK + g16 * 896 + h * 16;
  if (wv < 7) {
    bf16x8 pf[12];
#pragma unroll
    for (int hf2 = 0; hf2 < 2; ++hf2) {
#pragma unroll
      for (int i = 0; i < 12; ++i) pf[i] = ldsfrag(pp + (hf2 * 12 + i) * 32);
      SBAR();
#pragma unroll
      for (int i = 0; i < 12; ++i) uv = MFMA32(wf[hf2 * 12 + i], pf[i], uv);
      PIN(uv); SBAR();
    }
  }
#define W1_READ6() do { const lds_cptr p_ = w1rd + (L_HI + 65536 - L_LOW); \
    _Pragma("unroll") for (int i_ = 0; i_ < 4; ++i_) wf[24 + i_] = trfrag(p_ + i_ * 16 * 448, p_ + (i_ * 16 + 8) * 448); } while (0)
  STAMP(11);
  float x1[8];
  if (sl < 7) {
    if (wv == 7) wait_words(ws + OFF_FO + (tg >> 1) * 128, 16, tag, lane);
    asm volatile("s_waitcnt vmcnt(0) lgkmcnt(0)\n\ts_barrier" : "+v"(xa2), "+v"(xb2), "+v"(xc0) :: "memory");
    STAMP(12);
    u32x4 ob[8];
#pragma unroll
    for (int i = 0; i < 8; ++i) ob[i] = __builtin_amdgcn_raw_buffer_load_b128(rs, OFF_O + (tt_own * 32 + kq * 8 + i) * 1024 + l16, 0, 16);
    SBAR();
    W1_READ6();
    f32x16 acc0 = zero16, acc1 = zero16;
    {
      const lds_cptr wop = L + L_LOW + (kq * 4 * DH + 4 * h + tq) * 128 + g16 * 32 + tp * 8;
      const lds_cptr p0 = wop + rsw, p1 = wop + (64 ^ rsw);
      bf16x8 wa0[4], wa1[4];
#define WOROW0(i) (((i) >> 1) * DH + 16 * ((i) & 1))
#define WOROW1(i) (((i) >> 1) * DH + (((i) & 1) ? 20 : 8))
#pragma unroll
      for (int i = 0; i < 4; ++i) {
        wa0[i] = trfrag(p0 + WOROW0(i) * 128, p0 + WOROW1(i) * 128);
        wa1[i] = trfrag(p1 + WOROW0(i) * 128, p1 + WOROW1(i) * 128);
      }
      SBAR();
#pragma unroll
      for (int i = 0; i < 8; ++i) {
        acc0 = MFMA32(wa0[i & 3], BC8(ob[i]), acc0); acc1 = MFMA32(wa1[i & 3], BC8(ob[i]), acc1); PIN(acc0); PIN(acc1); SBAR();
        if (i < 4) {
          wa0[i] = trfrag(p0 + WOROW0(i + 4) * 128, p0 + WOROW1(i + 4) * 128);
          wa1[i] = trfrag(p1 + WOROW0(i + 4) * 128, p1 + WOROW1(i + 4) * 128);
        }
        SBAR();
      }
    }
    STAMP(13);
#define RW1(A, o8, p, KQ) do { f32x4 v0 = {A[o8], A[(o8) + 1], A[(o8) + 2], A[(o8) + 3]}, v1 = {A[(o8) + 4], A[(o8) + 5], A[(o8) + 6], A[(o8) + 7]}; \
        *(LAS f32x4*)(redb + (((p) * 4 + (KQ)) * 2) * 1024) = v0; *(LAS f32x4*)(redb + (((p) * 4 + (KQ)) * 2 + 1) * 1024) = v1; } while (0)
#define RKEEP(AT, A, o8) do { _Pragma("unroll") for (int i = 0; i < 8; ++i) AT[i] = A[(o8) + i]; } while (0)
#define RED_WRITE(AT, A0, A1) do { \
      if (kq == 0)      { RW1(A0, 8, 1, 0); RW1(A1, 0, 2, 0); RW1(A1, 8, 3, 0); RKEEP(AT, A0, 0); } \
      else if (kq == 1) { RW1(A0, 0, 0, 1); RW1(A1, 0, 2, 1); RW1(A1, 8, 3, 1); RKEEP(AT, A0, 8); } \
      else if (kq == 2) { RW1(A0, 0, 0, 2); RW1(A0, 8, 1, 2); RW1(A1, 8, 3, 2); RKEEP(AT, A1, 0); } \
      else              { RW1(A0, 0, 0, 3); RW1(A0, 8, 1, 3); RW1(A1, 0, 2, 3); RKEEP(AT, A1, 8); } } while (0)
#define RED_READ(AT) do { \
      _Pragma("unroll") for (int d = 1; d < 4; ++d) { const int src = (kq + d) & 3; \
        const f32x4 v0 = *(const LAS f32x4*)(redb + ((kq * 4 + src) * 2) * 1024); \
        const f32x4 v1 = *(const LAS f32x4*)(redb + ((kq * 4 + src) * 2 + 1) * 1024); \
        _Pragma("unroll") for (int i = 0; i < 4; ++i) { AT[i] += v0[i]; AT[4 + i] += v1[i]; } } } while (0)
    float at[8];
    RED_WRITE(at, acc0, acc1);
    const f32x4 ga0 = *(const LAS f32x4*)(L + L_GA + n0 * 4), ga1 = *(const LAS f32x4*)(L + L_GA + (n0 + 8) * 4);
    const f32x4 g20 = *(const LAS f32x4*)(L + L_G2 + n0 * 4), g21 = *(const LAS f32x4*)(L + L_G2 + (n0 + 8) * 4);
    BAR_LGKM();
    RED_READ(at);
    x1[0] = bflo(xa2[0]) + ga0[0] * at[0]; x1[1] = bfhi(xa2[0]) + ga0[1] * at[1];
    x1[2] = bflo(xa2[1]) + ga0[2] * at[2]; x1[3] = bfhi(xa2[1]) + ga0[3] * at[3];
    x1[4] = bflo(xb2[0]) + ga1[0] * at[4]; x1[5] = bfhi(xb2[0]) + ga1[1] * at[5];
    x1[6] = bflo(xb2[1]) + ga1[2] * at[6]; x1[7] = bfhi(xb2[1]) + ga1[3] * at[7];
    const float cshift = __uint_as_float(xc0 << 16);
    float p1 = 0.f, p2 = 0.f, y[8];
#pragma unroll
    for (int i = 0; i < 8; ++i) {
      const float d = x1[i] - cshift;
      p1 += d; p2 += d * d;
      y[i] = d * (i < 4 ? g20[i & 3] : g21[i & 3]);
    }
    u32x4 yv;
#pragma unroll
    for (int i = 0; i < 4; ++i) yv[i] = pack2(y[2 * i], y[2 * i + 1]);
    __builtin_amdgcn_raw_buffer_store_b128(yv, rs, OFF_Y + (tt_own * 28 + sl * 4 + kq) * 1024 + l16, 0, 16);
    p1 = xhalf_add(p1); p2 = xhalf_add(p2);
    if (h == 0) {
      u32x2 sv = {__float_as_uint(p1), __float_as_uint(p2)};
      __builtin_amdgcn_raw_buffer_store_b64(sv, rs, OFF_ST + ((tt_own * 28 + sl * 4 + kq) * 32 + r) * 8, 0, 16);
    }
    BAR_ALL();
    if (t == 0) put_word(ws + OFF_FY + tg * 128 + sl * 8, tag, 0);
    if (b == 0 && t == 64) __hip_atomic_store((gu64*)ws, epoch + 1ull, RLX_AGENT);
#pragma unroll
    for (int i = 0; i < 12; ++i) {
      const int pc = wv * 12 + i, q4 = pc / 24, rr8 = pc - q4 * 24;
      dma1(w2 + sl * 64 + (size_t)(q4 * 448 + rr8 * 8) * DM, rvo, lds0 + L_HI + pc * 1024);
    }
  } else {
    BAR_ALL();
    W1_READ6();
  }
  STAMP(14);
#pragma unroll
  for (int i = 24; i < 28; ++i) uv = MFMA32(wf[i], ldsfrag(pp + i * 32), uv);
  f32x16 u2, v2;
#pragma unroll
  for (int i = 0; i < 16; ++i) {
    auto rr = __builtin_amdgcn_permlane16_swap(__float_as_uint(uv[i]), __float_as_uint(uv[i]), false, false);
    u2[i] = __uint_as_float(rr[0]); v2[i] = __uint_as_float(rr[1]) * -1.4426950408889634f;
  }
  PIN(u2); PIN(v2);
  if (wv == 7) wait_words(ws + OFF_FY + tg * 128, 7, tag, lane);
  BAR_LGKM();
  STAMP(15);
  {
    const char* ysrc = ws + OFF_Y + (size_t)tg * 57344;
    if (wv < 7) { dma4c(ysrc + wv * 8192, l16, lds0 + L_LOW + wv * 8192); dma4c(ysrc + wv * 8192 + 4096, l16, lds0 + L_LOW + wv * 8192 + 4096); }
    else {
      float a1 = 0.f, a2 = 0.f;
      u32x2 sv[28];
#pragma unroll
      for (int i = 0; i < 28; ++i) sv[i] = __builtin_amdgcn_raw_buffer_load_b64(rs, OFF_ST + (((tg * 2 + h) * 28 + i) * 32 + r) * 8, 0, 16);
#pragma unroll
      for (int i = 0; i < 28; ++i) { a1 += __uint_as_float(sv[i][0]); a2 += __uint_as_float(sv[i][1]); }
      const float m = a1 * (1.f / DM);
      const float var = fmaxf(a2 * (1.f / DM) - m * m, 0.f);
      f32x2 o2 = {__builtin_amdgcn_rsqf(var + 1e-5f), m};
      *(LAS f32x2*)(L + L_SM + 512 + lane * 8) = o2;
    }
  }
  BAR_ALL();
  STAMP(16);
  f32x16 c0 = zero16, c1 = zero16;
  f32x2 st0, st1;
  f32x4 gb0, gb1;
  const unsigned ho1 = OFF_H + ((tg * 2) * 112 + (sl * 7 + wv) * 2) * 1024 + l16;
#define SILU1(C, ST, i) do { const float t_ = __builtin_fmaf(-ST[1], u2[i], C[i]); const float q_ = __builtin_fmaf(ST[0], t_, v2[i]); \
      C[i] = q_ * __builtin_amdgcn_rcpf(1.f + EX2(q_)); } while (0)
  gb0 = *(const LAS f32x4*)(L + L_GBV + (sl < 7 ? n0 : 0) * 4); gb1 = *(const LAS f32x4*)(L + L_GBV + ((sl < 7 ? n0 : 0) + 8) * 4);
  if (wv < 7) {
    const lds_cptr yp = L + L_LOW + l16;
    st0 = *(const LAS f32x2*)(L + L_SM + 512 + r * 8); st1 = *(const LAS f32x2*)(L + L_SM + 512 + (32 + r) * 8);
    st0[0] *= -1.4426950408889634f; st1[0] *= -1.4426950408889634f;
    bf16x8 yb[4];
#pragma unroll
    for (int i = 0; i < 4; ++i) yb[i] = ldsfrag(yp + i * 1024);
    SBAR();
#pragma unroll
    for (int ks = 0; ks < 28; ++ks) {
      c0 = MFMA32(wf[ks], yb[ks & 3], c0); SBAR();
      yb[ks & 3] = ldsfrag(yp + (ks + 4) * 1024); SBAR();
    }
#pragma unroll
    for (int ks = 0; ks < 28; ++ks) {
      c1 = MFMA32(wf[ks], yb[ks & 3], c1); SBAR();
      if (ks + 4 < 28) yb[ks & 3] = ldsfrag(yp + (28 + ks + 4) * 1024);
      if (ks >= 2 && ks < 18) { SILU1(c0, st0, ks - 2); PIN(c0); }
      if (ks == 19) __builtin_amdgcn_raw_buffer_store_b128(pack8(c0, 0), rs, ho1, 0, 16);
      if (ks == 21) __builtin_amdgcn_raw_buffer_store_b128(pack8(c0, 1), rs, ho1 + 1024, 0, 16);
      SBAR();
    }
  }
  BAR_LGKM();
  STAMP(17);
  if (wv < 7) {
#pragma unroll
    for (int i = 0; i < 16; ++i) SILU1(c1, st1, i);
    __builtin_amdgcn_raw_buffer_store_b128(pack8(c1, 0), rs, ho1 + 112 * 1024, 0, 16);
    __builtin_amdgcn_raw_buffer_store_b128(pack8(c1, 1), rs, ho1 + 113 * 1024, 0, 16);
  }
  STAMP(18);
  BAR_ALL();
  if (t == 0) put_word(ws + OFF_FH + tg * 128 + sl * 8, tag, 0);
  STAMP(19);
  if (sl == 7) { STAMP_DUMP(); return; }
#pragma unroll
  for (int i = 0; i < 8; ++i) {
    const int pc = wv * 8 + i, q4 = pc >> 4, rr8 = pc & 15;
    dma1(w2 + sl * 64 + (size_t)(q4 * 448 + 192 + rr8 * 8) * DM, rvo, lds0 + pc * 1024);
  }
  BAR_VM(0);
  {
    const int rowoff = (4 * h + tq) * 128 + g16 * 32 + tp * 8;
    const lds_cptr lo0 = L + kq * 16384 + rowoff + rsw, lo1 = L + kq * 16384 + rowoff + (64 ^ rsw);
    bf16x8 r0[8], r1[8];
#pragma unroll
    for (int i = 0; i < 8; ++i) {
      r0[i] = trfrag(lo0 + i * 2048, lo0 + i * 2048 + 1024);
      r1[i] = trfrag(lo1 + i * 2048, lo1 + i * 2048 + 1024);
    }
    BAR_LGKM();
#pragma unroll
    for (int i = 0; i < 8; ++i) {
      const int pc = wv * 8 + i, q4 = pc >> 4, rr8 = pc & 15;
      dma1(w2 + sl * 64 + (size_t)(q4 * 448 + 320 + rr8 * 8) * DM, rvo, lds0 + pc * 1024);
    }
    if (wv == 7) wait_words(ws + OFF_FH + tg * 128, 8, tag, lane);
    BAR_ALL();
    STAMP(20);
    u32x4 hb[28];
    const unsigned ho = OFF_H + (tt_own * 112 + kq * 28) * 1024 + l16;
#pragma unroll
    for (int i = 0; i < 28; ++i) {
      const int ks = (i < 8) ? 12 + i : (i < 20) ? i - 8 : i;
      hb[i] = __builtin_amdgcn_raw_buffer_load_b128(rs, ho + ks * 1024, 0, 16);
    }
    SBAR();
    f32x16 d0 = zero16, d1 = zero16;
    const lds_cptr hi0 = L + L_HI + kq * 24576 + rowoff + rsw, hi1 = L + L_HI + kq * 24576 + rowoff + (64 ^ rsw);
#pragma unroll
    for (int i = 0; i < 28; ++i) {
      d0 = MFMA32(r0[i & 7], BC8(hb[i]), d0); PIN(d0); SBAR();
      if (i + 8 < 20) r0[i & 7] = trfrag(hi0 + i * 2048, hi0 + i * 2048 + 1024);
      else if (i + 8 < 28) r0[i & 7] = trfrag(lo0 + (i - 12) * 2048, lo0 + (i - 12) * 2048 + 1024);
      SBAR();
      d1 = MFMA32(r1[i & 7], BC8(hb[i]), d1); PIN(d1); SBAR();
      if (i + 8 < 20) r1[i & 7] = trfrag(hi1 + i * 2048, hi1 + i * 2048 + 1024);
      else if (i + 8 < 28) r1[i & 7] = trfrag(lo1 + (i - 12) * 2048, lo1 + (i - 12) * 2048 + 1024);
      SBAR();
    }
    STAMP(21);
    BAR_LGKM();
    float f[8];
    RED_WRITE(f, d0, d1);
    BAR_LGKM();
    RED_READ(f);
#pragma unroll
    for (int i = 0; i < 4; ++i) { gb0[i] *= -0.6931471805599453f; gb1[i] *= -0.6931471805599453f; }
    u32x2 oa, ob2;
    oa[0] = pack2(x1[0] + gb0[0] * f[0], x1[1] + gb0[1] * f[1]);
    oa[1] = pack2(x1[2] + gb0[2] * f[2], x1[3] + gb0[3] * f[3]);
    ob2[0] = pack2(x1[4] + gb1[0] * f[4], x1[5] + gb1[1] * f[5]);
    ob2[1] = pack2(x1[6] + gb1[2] * f[6], x1[7] + gb1[3] * f[7]);
    u16* orow = out + (size_t)(tt_own * 32 + r) * DM;
    *(u32x2*)(orow + n0) = oa;
    *(u32x2*)(orow + n0 + 8) = ob2;
  }
  STAMP(22);
  STAMP_DUMP();
}

extern "C" void kernel_launch(void* const* d_in, const int* in_sizes, int n_in, void* d_out, int out_size, void* d_ws,
                              size_t ws_size, hipStream_t stream) {
  fused_block<<<256, 512, 0, stream>>>((const u16*)d_in[0], (const u16*)d_in[1], (const u16*)d_in[2], (const u16*)d_in[4],
                                       (const u16*)d_in[5], (const u16*)d_in[6], (const u16*)d_in[7], (u16*)d_out, (char*)d_ws);
}
```

```cpp
#include <hip/hip_runtime.h>
#include <hip/hip_bf16.h>
#include <stdint.h>

typedef __attribute__((ext_vector_type(8))) short bf16x8;
typedef __attribute__((ext_vector_type(4))) short s16x4;
typedef __attribute__((ext_vector_type(16))) float f32x16;
typedef __attribute__((ext_vector_type(4))) unsigned u32x4;
typedef __attribute__((ext_vector_type(2))) unsigned u32x2;
typedef __attribute__((ext_vector_type(4))) float f32x4;
typedef __attribute__((ext_vector_type(2))) float f32x2;
typedef __attribute__((ext_vector_type(2))) __bf16 bf16x2_t;
typedef __attribute__((ext_vector_type(2))) _Float16 h16x2;
typedef unsigned short u16;
typedef unsigned long long u64;
#define GAS __attribute__((address_space(1)))
#define LAS __attribute__((address_space(3)))
typedef GAS u64 gu64;
typedef LAS const char* lds_cptr;
typedef LAS char* lds_ptr;

#define DI __device__ __forceinline__
#define MFMA32(a, b, c) __builtin_amdgcn_mfma_f32_32x32x16_bf16((a), (b), (c), 0, 0, 0)
#define MFMA16(a, b, c) __builtin_amdgcn_mfma_f32_16x16x32_bf16((a), (b), (c), 0, 0, 0)
#define SBAR() __builtin_amdgcn_sched_barrier(0)
#define PIN(x) asm volatile("" : "+v"(x))
#define RLX_AGENT __ATOMIC_RELAXED, __HIP_MEMORY_SCOPE_AGENT
#define BC8(x) __builtin_bit_cast(bf16x8, x)
#define EX2(v) __builtin_amdgcn_exp2f(v)
#define BAR_LGKM() asm volatile("s_waitcnt lgkmcnt(0)\n\ts_barrier" ::: "memory")
#define BAR_ALL() asm volatile("s_waitcnt vmcnt(0) lgkmcnt(0)\n\ts_barrier" ::: "memory")
#define BAR_VM(n) asm volatile("s_waitcnt vmcnt(" #n ")\n\ts_barrier" ::: "memory")

constexpr int DM = 448, DH = 28, FF = 1792;
constexpr int NTT = 64;

constexpr unsigned OFF_MODG = 4096;
constexpr unsigned OFF_FKV = 32768;
constexpr unsigned OFF_FO = 36864;
constexpr unsigned OFF_FY = 40960;
constexpr unsigned OFF_FH = 49152;
constexpr unsigned OFF_ST = 65536;
constexpr unsigned OFF_Q = 1u << 20;
constexpr unsigned OFF_KV = 3u << 20;
constexpr unsigned OFF_O = 7u << 20;
constexpr unsigned OFF_Y = 9u << 20;
constexpr unsigned OFF_H = 11u << 20;
constexpr unsigned WS_USED = 19u << 20;

#define STAMP(i) do {} while (0)
#define STAMP_DECL() do {} while (0)
#define STAMP_DUMP() do {} while (0)

DI float bf2f(u16 v) { return __uint_as_float((unsigned)v << 16); }
DI float bflo(unsigned w) { return __uint_as_float(w << 16); }
DI float bfhi(unsigned w) { return __uint_as_float(w & 0xffff0000u); }
DI unsigned pack2(float lo, float hi) {
  f32x2 v = {lo, hi};
  bf16x2_t b = __builtin_convertvector(v, bf16x2_t);
  return __builtin_bit_cast(unsigned, b);
}
DI unsigned packh2(float lo, float hi) {
  f32x2 v = {lo, hi};
  h16x2 b = __builtin_convertvector(v, h16x2);
  return __builtin_bit_cast(unsigned, b);
}
DI float hlo(unsigned w) { return (float)__builtin_bit_cast(h16x2, w)[0]; }
DI float hhi(unsigned w) { return (float)__builtin_bit_cast(h16x2, w)[1]; }
DI float fmix_lo(unsigned w, float g, float b) { float d; asm("v_fma_mix_f32 %0, %1, %2, %3 op_sel_hi:[1,0,0]" : "=v"(d) : "v"(w), "v"(g), "v"(b)); return d; }
DI float fmix_hi(unsigned w, float g, float b) { float d; asm("v_fma_mix_f32 %0, %1, %2, %3 op_sel:[1,0,0] op_sel_hi:[1,0,0]" : "=v"(d) : "v"(w), "v"(g), "v"(b)); return d; }
DI float rbf(float x) { return bflo(pack2(x, x)); }
DI u32x4 pack8(const f32x16& x, int s) {
  u32x4 p;
  p[0] = pack2(x[8 * s + 0], x[8 * s + 1]);
  p[1] = pack2(x[8 * s + 2], x[8 * s + 3]);
  p[2] = pack2(x[8 * s + 4], x[8 * s + 5]);
  p[3] = pack2(x[8 * s + 6], x[8 * s + 7]);
  return p;
}
DI float xhalf_add(float v) {
  auto rr = __builtin_amdgcn_permlane32_swap(__float_as_uint(v), __float_as_uint(v), false, false);
  return __uint_as_float(rr[0]) + __uint_as_float(rr[1]);
}
DI float xhalf_max(float v) {
  auto rr = __builtin_amdgcn_permlane32_swap(__float_as_uint(v), __float_as_uint(v), false, false);
  return fmaxf(__uint_as_float(rr[0]), __uint_as_float(rr[1]));
}
DI void dma1(const void* sbase, unsigned voff, unsigned lds_base) {
  unsigned sv;
  asm volatile("s_mov_b32 %0, m0\n\ts_mov_b32 m0, %2\n\ts_nop 0\n\tglobal_load_lds_dwordx4 %1, %3\n\ts_mov_b32 m0, %0"
               : "=&s"(sv) : "v"(voff), "s"(lds_base), "s"(sbase) : "memory");
}
DI void dma1c(const void* sbase, unsigned voff, unsigned lds_base) {
  unsigned sv;
  asm volatile("s_mov_b32 %0, m0\n\ts_mov_b32 m0, %2\n\ts_nop 0\n\tglobal_load_lds_dwordx4 %1, %3 sc1\n\ts_mov_b32 m0, %0"
               : "=&s"(sv) : "v"(voff), "s"(lds_base), "s"(sbase) : "memory");
}
DI void dma4c(const void* sbase, unsigned voff, unsigned lds_base) {
  unsigned sv;
  asm volatile("s_mov_b32 %0, m0\n\ts_mov_b32 m0, %2\n\ts_nop 0\n\t"
               "global_load_lds_dwordx4 %1, %3 sc1\n\tglobal_load_lds_dwordx4 %1, %3 offset:1024 sc1\n\t"
               "global_load_lds_dwordx4 %1, %3 offset:2048 sc1\n\tglobal_load_lds_dwordx4 %1, %3 offset:3072 sc1\n\t"
               "s_mov_b32 m0, %0" : "=&s"(sv) : "v"(voff), "s"(lds_base), "s"(sbase) : "memory");
}
DI void touch1(const void* sbase, unsigned voff, unsigned lds_base) {
  unsigned sv;
  asm volatile("s_mov_b32 %0, m0\n\ts_mov_b32 m0, %2\n\ts_nop 0\n\tglobal_load_lds_dword %1, %3\n\ts_mov_b32 m0, %0"
               : "=&s"(sv) : "v"(voff), "s"(lds_base), "s"(sbase) : "memory");
}
DI bf16x8 ldsfrag(lds_cptr p) { return *(const LAS bf16x8*)p; }
DI s16x4 trread(lds_cptr p) { return __builtin_amdgcn_ds_read_tr16_b64_v4i16((LAS s16x4*)p); }
DI bf16x8 trfrag(lds_cptr p0, lds_cptr p1) {
  s16x4 lo = trread(p0), hi = trread(p1);
  return __builtin_shufflevector(lo, hi, 0, 1, 2, 3, 4, 5, 6, 7);
}
DI float dot2bf(unsigned a, unsigned b, float c) {
  return __builtin_amdgcn_fdot2_f32_bf16(__builtin_bit_cast(bf16x2_t, a), __builtin_bit_cast(bf16x2_t, b), c, false);
}
DI unsigned wait_words(const char* p, int n, unsigned tag, int lane) {
  const gu64* f = (const gu64*)p + (lane < n ? lane : n - 1);
  const u64 t0 = __builtin_amdgcn_s_memrealtime();
  u64 v;
  for (;;) {
    v = __hip_atomic_load(f, RLX_AGENT);
    if (__all((unsigned)(v >> 32) == tag)) break;
    if (__builtin_amdgcn_s_memrealtime() - t0 > 2000000ull) break;
    __builtin_amdgcn_s_sleep(1);
  }
  return (unsigned)v;
}
DI void put_word(char* p, unsigned tag, unsigned val) { __hip_atomic_store((gu64*)p, ((u64)tag << 32) | val, RLX_AGENT); }

constexpr int L_XS = 0;
constexpr int L_WKV = 0;
constexpr int L_XCH = 65536;
constexpr int L_WQ = 114688;
constexpr int L_MODW = 143360;
constexpr int L_SILU = 157696;
constexpr int L_STAT = 143360;
constexpr int L_GB = 145408;
constexpr int L_KMX = 158592;
constexpr int L_LOW = 0;
constexpr int L_M2 = 57344;
constexpr int L_GA = L_M2, L_G2 = L_M2 + 1792, L_GBV = L_M2 + 3584, L_PK = L_M2 + 5376;
constexpr int L_SM = L_M2 + 7168;
constexpr int L_HI = 65536;
constexpr int LDS_BYTES = 160 * 1024;

#define ASTEPH(SC, SN, KADDR, V1ADDR, V0ADDR, H1, H2, H3, H4) do { \
    SN = MFMA32(kf0, qf0, zero16); SBAR(); \
    pw1[2] = pack2(e12, e13); pw1[3] = pack2(e14, e15); \
    kf0 = ldsfrag(KADDR); SBAR(); \
    SC[0] = EX2(SC[0]); SC[1] = EX2(SC[1]); SBAR(); H1; SBAR(); SC[2] = EX2(SC[2]); SC[3] = EX2(SC[3]); PIN(SC); PIN(pw1); SBAR(); \
    o = MFMA32(vf1, BC8(pw1), o); SBAR(); \
    vf1 = ldsfrag(V1ADDR); \
    SC[4] = EX2(SC[4]); SC[5] = EX2(SC[5]); SBAR(); pw0[0] = pack2(SC[0], SC[1]); pw0[1] = pack2(SC[2], SC[3]); SBAR(); H2; SBAR(); SC[6] = EX2(SC[6]); SC[7] = EX2(SC[7]); PIN(SC); PIN(pw0); SBAR(); \
    SN = MFMA32(kf1, qf1, SN); SBAR(); \
    kf1 = ldsfrag((KADDR) + 1024); \
    SC[8] = EX2(SC[8]); SC[9] = EX2(SC[9]); SBAR(); pw0[2] = pack2(SC[4], SC[5]); pw0[3] = pack2(SC[6], SC[7]); SBAR(); H3; SBAR(); SC[10] = EX2(SC[10]); SC[11] = EX2(SC[11]); PIN(SC); PIN(pw0); SBAR(); \
    o = MFMA32(vf0, BC8(pw0), o); SBAR(); \
    vf0 = ldsfrag(V0ADDR); \
    e12 = EX2(SC[12]); e13 = EX2(SC[13]); SBAR(); pw1[0] = pack2(SC[8], SC[9]); pw1[1] = pack2(SC[10], SC[11]); SBAR(); H4; SBAR(); e14 = EX2(SC[14]); e15 = EX2(SC[15]); PIN(e12); PIN(e13); PIN(e14); PIN(e15); PIN(pw1); SBAR(); \
  } while (0)
#define NOH ((void)0)
#define ASTEP(SC, SN, KADDR, V1ADDR, V0ADDR) ASTEPH(SC, SN, KADDR, V1ADDR, V0ADDR, NOH, NOH, NOH, NOH)

#define ATTN_PROLOGUE() \
    kf0 = ldsfrag(abase); kf1 = ldsfrag(abase + 1024); \
    vf0 = ldsfrag(abase + 2048); vf1 = vf0; \
    e12 = 0.f; e13 = 0.f; e14 = 0.f; e15 = 0.f; pw1[0] = 0; pw1[1] = 0; o = zero16; \
    sA = MFMA32(kf0, qf0, zero16); sA = MFMA32(kf1, qf1, sA); \
    kf0 = ldsfrag(abase + 4096); kf1 = ldsfrag(abase + 4096 + 1024);

__global__ void __launch_bounds__(512) fused_block(const u16* __restrict__ x, const u16* __restrict__ wqkv, const u16* __restrict__ wo,
                                                    const u16* __restrict__ w1, const u16* __restrict__ w2, const u16* __restrict__ cvec,
                                                    const u16* __restrict__ wmod, u16* __restrict__ out, char* __restrict__ ws) {
  __shared__ __attribute__((aligned(16))) char lds_arr[LDS_BYTES];
  const lds_ptr L = (lds_ptr)lds_arr;
  const unsigned lds0 = (unsigned)(uintptr_t)lds_arr;
  const int t = threadIdx.x, lane = t & 63, wv = __builtin_amdgcn_readfirstlane(t >> 6), r = lane & 31, h = lane >> 5;
  __builtin_assume(wv < 8);
  const int g16 = (lane >> 4) & 1, tq = (lane & 15) >> 2, tp = lane & 3;
  const int b = blockIdx.x, xg = b & 7, rk = b >> 3;
  const __amdgpu_buffer_rsrc_t rs = __builtin_amdgcn_make_buffer_rsrc(ws, 0, WS_USED, 0x00020000);
  asm volatile("" :: "s"(x), "s"(wqkv), "s"(wo), "s"(w1), "s"(w2), "s"(cvec), "s"(wmod), "s"(out), "s"(ws));
  STAMP_DECL();
  STAMP(0);

  const int rt = xg * 2 + (rk >> 4), hq = rk & 15;
  const bool modwave = (rk < 21) && (wv == 0);
  const int msl = xg * 21 + rk;
  u32x4 craw = {0, 0, 0, 0};
  if (modwave) {
    asm volatile("global_load_dwordx4 %0, %1, %2" : "=v"(craw) : "v"((lane < 56 ? lane : 55) * 16), "s"(cvec) : "memory");
    const unsigned vo = (unsigned)(lane >> 1) * (6 * DM * 2) + (lane & 1) * 16;
#pragma unroll
    for (int i = 0; i < 14; ++i) dma1(wmod + msl * 16 + i * 32 * 6 * DM, vo, lds0 + L_MODW + i * 1024);
  }
  const int a_tt = wv & 3, a_kh = wv >> 2;
  if (wv > 0) {
    const u16* xb = x + (size_t)(rt * 128) * DM;
    const u16* xw = xb + (wv - 1) * 16 * DM;
    const unsigned dw = lds0 + L_XS + (wv - 1) * 14336;
#pragma unroll
    for (int i = 0; i < 7; ++i) {
      const int q = i * 64 + lane, rl = (q * 1171) >> 16, c = q - rl * 56;
      const unsigned vo = rl * (DM * 2) + ((c ^ (rl >> 1)) * 16);
      dma1(xw, vo, dw + i * 1024);
      dma1(xw + 8 * DM, vo ^ 64, dw + 7168 + i * 1024);
    }
    const int q = (wv - 1) * 64 + lane, rl = (q * 1171) >> 16, c = q - rl * 56;
    const unsigned vo = rl * (DM * 2) + ((c ^ (rl >> 1)) * 16);
    dma1(xb + 112 * DM, vo, lds0 + L_XS + 14 * 7168 + (wv - 1) * 1024);
    dma1(xb + 120 * DM, vo ^ 64, lds0 + L_XS + 15 * 7168 + (wv - 1) * 1024);
  }
  u64 epoch;
  asm volatile("s_load_dwordx2 %0, %1, 0x0" : "=s"(epoch) : "s"(ws) : "memory");
  const unsigned wvo = (lane >> 2) * (3 * DM * 2) + ((tp == 3) ? 40 : tp * 16);
  if (wv > 0) {
#pragma unroll
    for (int i = 0; i < 4; ++i) { const int s = (wv - 1) * 4 + i; dma1(wqkv + hq * DH + s * 16 * 3 * DM, wvo, lds0 + L_WQ + s * 1024); }
  }
  if (t == 0) *(LAS unsigned*)(L + L_KMX) = 0;
  asm volatile("s_waitcnt lgkmcnt(0)" : "+s"(epoch) :: "memory");
  const unsigned tag = (unsigned)epoch + 1u;
  if (modwave) {
    asm volatile("s_waitcnt vmcnt(14)" : "+v"(craw) :: "memory");
    u32x4 sv;
#pragma unroll
    for (int j = 0; j < 4; ++j) {
      const float c0_ = bflo(craw[j]), c1_ = bfhi(craw[j]);
      sv[j] = pack2(c0_ * __builtin_amdgcn_rcpf(1.f + EX2(-1.4426950408889634f * c0_)), c1_ * __builtin_amdgcn_rcpf(1.f + EX2(-1.4426950408889634f * c1_)));
    }
    if (lane < 56) *(LAS u32x4*)(L + L_SILU + lane * 16) = sv;
    const int kg = lane >> 4;
    const lds_cptr bp = L + L_MODW + (8 * kg + tq) * 32 + tp * 8;
    const lds_cptr ap = L + L_SILU + kg * 16;
    bf16x8 af[14], bfr[14];
    asm volatile("s_waitcnt lgkmcnt(0)" ::: "memory");
#pragma unroll
    for (int s = 0; s < 14; ++s) af[s] = ldsfrag(ap + s * 64);
    asm volatile("s_waitcnt vmcnt(0)" ::: "memory");
#pragma unroll
    for (int s = 0; s < 14; ++s) bfr[s] = trfrag(bp + s * 1024, bp + s * 1024 + 128);
    f32x4 acc0 = {0.f, 0.f, 0.f, 0.f}, acc1 = acc0;
#pragma unroll
    for (int s = 0; s < 14; s += 2) { acc0 = MFMA16(af[s], bfr[s], acc0); acc1 = MFMA16(af[s + 1], bfr[s + 1], acc1); }
    if (lane < 16) put_word(ws + OFF_MODG + (msl * 16 + lane) * 8, tag, __float_as_uint(rbf(acc0[0] + acc1[0])));
  }
  STAMP(1);
  BAR_VM(4);
  u32x4 xr[14];
  {
    const int row = a_tt * 32 + r, gsw = (row >> 1) & 7, c0 = a_kh * 28 + h;
#pragma unroll
    for (int i = 0; i < 4; ++i) {
      const int c = c0 + 2 * i;
      const lds_cptr xa = L + L_XS + row * (DM * 2) + (c & ~7) * 16 + (((c & 7) ^ gsw) * 16);
#pragma unroll
      for (int s = i; s < 14; s += 4) xr[s] = *(const LAS u32x4*)(xa + (s >> 2) * 128);
    }
  }
  u32x4 mg;
  asm volatile("global_load_dwordx4 %0, %1, %2 offset:0 sc1" : "=v"(mg) : "v"((t < DM ? t : DM - 1) * 16), "s"(ws + OFF_MODG) : "memory");
  BAR_LGKM();
  STAMP(2);
  {
    const u16* wsrc = wqkv + hq * DH + (1 + (wv >> 2)) * DM + (wv & 3) * 7 * 16 * 3 * DM;
    float s1a = 0.f, s2a = 0.f, s1b = 0.f, s2b = 0.f;
#pragma unroll
    for (int s = 0; s < 14; ++s) {
      if ((s & 1) == 0) dma1(wsrc + (s >> 1) * 16 * 3 * DM, wvo, lds0 + L_WKV + (wv * 7 + (s >> 1)) * 1024);
      s1a = dot2bf(xr[s][0], 0x3f803f80u, s1a); s2a = dot2bf(xr[s][0], xr[s][0], s2a);
      s1b = dot2bf(xr[s][1], 0x3f803f80u, s1b); s2b = dot2bf(xr[s][1], xr[s][1], s2b);
      s1a = dot2bf(xr[s][2], 0x3f803f80u, s1a); s2a = dot2bf(xr[s][2], xr[s][2], s2a);
      s1b = dot2bf(xr[s][3], 0x3f803f80u, s1b); s2b = dot2bf(xr[s][3], xr[s][3], s2b);
      PIN(s1a); PIN(s2a); PIN(s1b); PIN(s2b); SBAR();
    }
    const float s1 = xhalf_add(s1a + s1b), s2 = xhalf_add(s2a + s2b);
    if (h == 0) { f32x2 v = {s1, s2}; *(LAS f32x2*)(L + L_STAT + (wv * 32 + r) * 8) = v; }
    asm volatile("s_waitcnt vmcnt(7)" : "+v"(mg) :: "memory");
    if (wv < 7) {
      if (!__all(mg[1] == tag && mg[3] == tag)) {
        const u64 t0 = __builtin_amdgcn_s_memrealtime();
        for (;;) {
          mg = __builtin_amdgcn_raw_buffer_load_b128(rs, OFF_MODG + t * 16, 0, 16);
          if (__all(mg[1] == tag && mg[3] == tag)) break;
          if (__builtin_amdgcn_s_memrealtime() - t0 > 2000000ull) break;
          __builtin_amdgcn_s_sleep(1);
        }
      }
      f32x2 o2 = {__uint_as_float(mg[0]), __uint_as_float(mg[2])};
      const bool isg = t >= 224;
      if (isg) { o2[0] = rbf(1.f + o2[0]); o2[1] = rbf(1.f + o2[1]); }
      *(LAS f32x2*)(L + L_GB + (isg ? 2 * t - DM : DM + 2 * t) * 4) = o2;
    }
    BAR_LGKM();
    STAMP(3);
    const lds_cptr gp = L + L_GB + (a_kh * 224 + 8 * h) * 4;
    f32x4 gq[3][4];
#define LOADGB(slot, s) do { gq[slot][0] = *(const LAS f32x4*)(gp + (s) * 64); gq[slot][1] = *(const LAS f32x4*)(gp + (s) * 64 + 16); \
      gq[slot][2] = *(const LAS f32x4*)(gp + DM * 4 + (s) * 64); gq[slot][3] = *(const LAS f32x4*)(gp + DM * 4 + (s) * 64 + 16); } while (0)
    const f32x2 o2 = *(const LAS f32x2*)(L + L_STAT + ((wv ^ 4) * 32 + r) * 8);
    LOADGB(0, 0); LOADGB(1, 1);
    const float mu = (s1 + o2[0]) * (1.f / DM);
    const float var = fmaxf((s2 + o2[1]) * (1.f / DM) - mu * mu, 0.f);
    const float rstd = __builtin_amdgcn_rsqf(var + 1e-5f);
    const float nb = -mu * rstd;
    SBAR();
#pragma unroll
    for (int s = 0; s < 14; ++s) {
      if (s + 2 < 14) LOADGB((s + 2) % 3, s + 2);
      SBAR();
      const f32x4 g0 = gq[s % 3][0], g1 = gq[s % 3][1], b0 = gq[s % 3][2], b1 = gq[s % 3][3];
      xr[s][0] = pack2((bflo(xr[s][0]) * rstd + nb) * g0[0] + b0[0], (bfhi(xr[s][0]) * rstd + nb) * g0[1] + b0[1]);
      xr[s][1] = pack2((bflo(xr[s][1]) * rstd + nb) * g0[2] + b0[2], (bfhi(xr[s][1]) * rstd + nb) * g0[3] + b0[3]);
      xr[s][2] = pack2((bflo(xr[s][2]) * rstd + nb) * g1[0] + b1[0], (bfhi(xr[s][2]) * rstd + nb) * g1[1] + b1[1]);
      xr[s][3] = pack2((bflo(xr[s][3]) * rstd + nb) * g1[2] + b1[2], (bfhi(xr[s][3]) * rstd + nb) * g1[3] + b1[3]);
      PIN(xr[s]); SBAR();
    }
  }
  f32x16 zero16;
#pragma unroll
  for (int i = 0; i < 16; ++i) zero16[i] = 0.f;
  f32x16 aq = zero16, ak = zero16, av = zero16;
  {
    const int wro = a_kh * 14 * 1024 + (8 * h + tq) * 64 + g16 * 32 + tp * 8;
    const lds_cptr wqp = L + L_WQ + wro, wkp = L + L_WKV + wro, wvp = L + L_WKV + 28672 + wro;
    bf16x8 wa[14], wb[14];
#pragma unroll
    for (int s = 0; s < 14; ++s) wa[s] = trfrag(wqp + s * 1024, wqp + s * 1024 + 256);
    SBAR();
#pragma unroll
    for (int s = 0; s < 7; ++s) { aq = MFMA32(wa[s], BC8(xr[s]), aq); PIN(aq); SBAR(); }
    BAR_VM(0);
    STAMP(4);
#pragma unroll
    for (int s = 7; s < 14; ++s) {
      aq = MFMA32(wa[s], BC8(xr[s]), aq); PIN(aq); SBAR();
      wb[2 * (s - 7)] = trfrag(wkp + 2 * (s - 7) * 1024, wkp + 2 * (s - 7) * 1024 + 256);
      wb[2 * (s - 7) + 1] = trfrag(wkp + (2 * (s - 7) + 1) * 1024, wkp + (2 * (s - 7) + 1) * 1024 + 256); SBAR();
    }
#pragma unroll
    for (int s = 0; s < 14; ++s) {
      ak = MFMA32(wb[s], BC8(xr[s]), ak); PIN(ak); SBAR();
      wa[s] = trfrag(wvp + s * 1024, wvp + s * 1024 + 256); SBAR();
    }
#pragma unroll
    for (int s = 0; s < 14; ++s) { av = MFMA32(BC8(xr[s]), wa[s], av); PIN(av); SBAR(); }
  }
  STAMP(5);
  {
    const lds_ptr xw = L + L_XCH + wv * 6144 + lane * 16;
    const lds_cptr xrd = L + L_XCH + (wv ^ 4) * 6144 + lane * 16;
    const int tt = rt * 4 + a_tt;
    const bool vpad = (r >= 24 && r < 28);
    const unsigned vconst = (r == 24) ? 0x3f803f80u : 0u;
#define W4(off, A, i0) do { f32x4 v_ = {A[i0], A[(i0) + 1], A[(i0) + 2], A[(i0) + 3]}; *(LAS f32x4*)(xw + (off)) = v_; } while (0)
#define R4(off, A, i0) do { const f32x4 v_ = *(const LAS f32x4*)(xrd + (off)); A[i0] += v_[0]; A[(i0) + 1] += v_[1]; A[(i0) + 2] += v_[2]; A[(i0) + 3] += v_[3]; } while (0)
    if (a_kh == 0) {
      W4(0, ak, 0); W4(1024, ak, 4); W4(2048, ak, 8); W4(3072, ak, 12); W4(4096, av, 8); W4(5120, av, 12);
      BAR_LGKM();
      R4(0, aq, 0); R4(1024, aq, 4); R4(2048, aq, 8); R4(3072, aq, 12); R4(4096, av, 0); R4(5120, av, 4);
      const float qs = 0.18898223650461362f * 1.4426950408889634f;
#pragma unroll
      for (int i = 0; i < 16; ++i) aq[i] *= qs;
      u32x4 f0 = pack8(aq, 0), f1 = pack8(aq, 1), vf = pack8(av, 0);
      if (h == 0) { f1[2] = 0; f1[3] = 0; }
#pragma unroll
      for (int i = 0; i < 4; ++i) vf[i] = vpad ? vconst : vf[i];
      const unsigned mo = OFF_Q + (hq * NTT + tt) * 2048 + lane * 16;
      __builtin_amdgcn_raw_buffer_store_b128(f0, rs, mo, 0, 16);
      __builtin_amdgcn_raw_buffer_store_b128(f1, rs, mo + 1024, 0, 16);
      __builtin_amdgcn_raw_buffer_store_b128(vf, rs, OFF_KV + ((hq * NTT + tt) * 4 + 2) * 1024 + lane * 16, 0, 16);
    } else {
      W4(0, aq, 0); W4(1024, aq, 4); W4(2048, aq, 8); W4(3072, aq, 12); W4(4096, av, 0); W4(5120, av, 4);
      BAR_LGKM();
      R4(0, ak, 0); R4(1024, ak, 4); R4(2048, ak, 8); R4(3072, ak, 12); R4(4096, av, 8); R4(5120, av, 12);
      u32x4 f0 = pack8(ak, 0), f1 = pack8(ak, 1), vf = pack8(av, 1);
      u32x4 f1z = f1;
      if (h == 0) { f1z[2] = 0; f1z[3] = 0; f1[2] = 0x3f803f80u; f1[3] = 0x00003f80u; }
#pragma unroll
      for (int i = 0; i < 4; ++i) vf[i] = vpad ? vconst : vf[i];
      const unsigned mo = OFF_KV + (hq * NTT + tt) * 4096 + lane * 16;
      __builtin_amdgcn_raw_buffer_store_b128(f0, rs, mo, 0, 16);
      __builtin_amdgcn_raw_buffer_store_b128(f1, rs, mo + 1024, 0, 16);
      __builtin_amdgcn_raw_buffer_store_b128(vf, rs, mo + 3072, 0, 16);
      float kn = 0.f;
#pragma unroll
      for (int i = 0; i < 4; ++i) { kn = dot2bf(f0[i], f0[i], kn); kn = dot2bf(f1z[i], f1z[i], kn); }
      kn = xhalf_add(kn);
      asm volatile("ds_max_u32 %0, %1" :: "v"(lds0 + L_KMX), "v"(__float_as_uint(kn)) : "memory");
    }
    BAR_ALL();
    if (t == 0) put_word(ws + OFF_FKV + (hq * 16 + rt) * 8, tag, *(const LAS unsigned*)(L + L_KMX));
  }
  STAMP(6);
  const int head = xg * 2 + (rk >> 4), qt = rk & 15;
  const int tg = xg * 4 + (rk >> 3), sl = rk & 7;
  const int qb = wv & 3, kh = wv >> 2;
  unsigned w1vo[4];
#pragma unroll
  for (int i = 0; i < 4; ++i) { const int pc = (wv + 8 * i < 28) ? wv + 8 * i : wv; const int q = pc * 64 + lane, row = q / 28, c = q - row * 28; w1vo[i] = row * (FF * 2) + c * 16; }
  const u16* w1b = w1 + sl * 224;
  const lds_cptr w1rd = L + L_LOW + (4 * h + tq) * 448 + wv * 64 + g16 * 32 + tp * 8;
  const unsigned w1d3 = (wv < 4) ? 24576 : 0;
#define W1_DMA(c, buf) do { const u16* src_ = w1b + (size_t)(c) * 64 * FF; const unsigned dst_ = lds0 + L_LOW + (buf) * 28672 + wv * 1024; \
    dma1(src_, w1vo[0], dst_); dma1(src_, w1vo[1], dst_ + 8192); dma1(src_, w1vo[2], dst_ + 16384); dma1(src_, w1vo[3], dst_ + w1d3); } while (0)
  if (wv == 7) {
    float km = __uint_as_float(wait_words(ws + OFF_FKV + head * 128, 16, tag, lane));
#pragma unroll
    for (int m = 8; m >= 1; m >>= 1) km = fmaxf(km, __shfl_xor(km, m));
    if (lane == 0) *(LAS float*)(L + L_SM) = km;
  } else {
    const u64 t0 = __builtin_amdgcn_s_memrealtime();
    u32x4 v0, v1;
    for (;;) {
      v0 = __builtin_amdgcn_raw_buffer_load_b128(rs, OFF_MODG + 7168 + t * 16, 0, 16);
      v1 = __builtin_amdgcn_raw_buffer_load_b128(rs, OFF_MODG + 14336 + t * 16, 0, 16);
      if (__all(v0[1] == tag && v0[3] == tag && v1[1] == tag && v1[3] == tag)) break;
      if (__builtin_amdgcn_s_memrealtime() - t0 > 2000000ull) break;
      __builtin_amdgcn_s_sleep(1);
    }
    const int e = (t < 224) ? 2 * t : 2 * t - DM;
    const int pk = (e >> 4) * 32 + ((e >> 2) & 1) * 16 + ((e >> 3) & 1) * 8 + (e & 3) * 2;
    if (t < 224) {
      f32x2 a = {__uint_as_float(v0[0]), __uint_as_float(v0[2])};
      f32x2 g = {rbf(1.f + __uint_as_float(v1[0])), rbf(1.f + __uint_as_float(v1[2]))};
      *(LAS f32x2*)(L + L_GA + e * 4) = a;
      *(LAS f32x2*)(L + L_G2 + e * 4) = g;
      *(LAS unsigned*)(L + L_PK + pk) = pack2(g[0], g[1]);
    } else {
      f32x2 g = {__uint_as_float(v1[0]), __uint_as_float(v1[2])};
      *(LAS f32x2*)(L + L_GBV + e * 4) = g;
      *(LAS unsigned*)(L + L_PK + 896 + pk) = pack2(__uint_as_float(v0[0]), __uint_as_float(v0[2]));
    }
  }
  BAR_LGKM();
  STAMP(7);
  const char* kvsrc = ws + OFF_KV + (size_t)(head * NTT + kh * 32 + qb) * 4096;
  const unsigned kvdst = lds0 + L_HI + kh * 16384 + qb * 4096;
  const unsigned l16 = lane * 16;
#define DMA_KV(i, buf) dma4c(kvsrc + (size_t)(i) * 16384, l16, kvdst + (buf))
  const unsigned w1to = (unsigned)(((rk >> 3) * 16 + wv * 2 + ((lane >> 2) & 1)) * (FF * 2) + ((sl * 448) & ~127) + (lane & 3) * 128);
#define W1_TOUCH(c) touch1(w1 + (size_t)(c) * 64 * FF, w1to, lds0 + L_SM + 256)
  bf16x8 qf0, qf1;
  {
    u32x4 q0, q1;
    asm volatile("global_load_dwordx4 %0, %2, %3 sc1\n\tglobal_load_dwordx4 %1, %2, %3 offset:1024 sc1" : "=&v"(q0), "=&v"(q1)
                 : "v"(l16), "s"(ws + OFF_Q + (head * NTT + qt * 4 + qb) * 2048) : "memory");
    DMA_KV(0, 0);
    DMA_KV(1, 32768);
    W1_DMA(0, 0);
    const float km = *(const LAS float*)(L + L_SM);
    asm volatile("s_waitcnt vmcnt(12)" : "+v"(q0), "+v"(q1) :: "memory");
    float qn = 0.f;
#pragma unroll
    for (int i = 0; i < 4; ++i) { qn = dot2bf(q0[i], q0[i], qn); qn = dot2bf(q1[i], q1[i], qn); }
    qn = xhalf_add(qn);
    const float bound = __builtin_amdgcn_sqrtf(qn * km) * 1.004f + 1e-30f;
    if (h == 0) q1[2] = ((__float_as_uint(bound) + 0xffffu) >> 16) | 0x8000u;
    qf0 = BC8(q0); qf1 = BC8(q1);
  }
  bf16x8 wf[28];
  f32x16 o, sA, sB;
  u32x4 pw0, pw1;
  bf16x8 kf0, kf1, vf0, vf1;
  float e12, e13, e14, e15;
  const lds_cptr abase = L + L_HI + kh * 16384 + l16;
  BAR_VM(8);
  STAMP(8);
  ATTN_PROLOGUE();
#define W1_RD1(c, i_) (wf[4 * (c) + (i_)] = trfrag(w1rd + ((c) & 1) * 28672 + (i_) * 16 * 448, w1rd + ((c) & 1) * 28672 + ((i_) * 16 + 8) * 448))
#define W1_READ(c) do { W1_RD1(c, 0); W1_RD1(c, 1); W1_RD1(c, 2); W1_RD1(c, 3); } while (0)
#define KV1(st, j) do { if ((st) < 8) dma1c(kvsrc + (size_t)(st) * 16384 + (j) * 1024, l16, kvdst + ((st) % 3) * 32768 + (j) * 1024); } while (0)
#define W1D1(c, j) do { if ((c) >= 0) dma1(w1b + (size_t)(c) * 64 * FF, w1vo[j], lds0 + L_LOW + ((c) & 1) * 28672 + wv * 1024 + ((j) < 3 ? (j) * 8192 : w1d3)); } while (0)
#define W1R1(c, j) do { if ((c) >= 0) W1_RD1(((c) >= 0 ? (c) : 0), j); } while (0)
#define STAGE(st, NW, RC, DC, TCH) do { \
    constexpr int cur = ((st) % 3) * 32768, nxt = (((st) + 1) % 3) * 32768; \
    const lds_cptr bc = abase + cur, bn = abase + nxt; \
    ASTEP(sA, sB, bc + 2 * 4096, bc + 0 * 4096 + 3072, bc + 1 * 4096 + 2048); \
    ASTEP(sB, sA, bc + 3 * 4096, bc + 1 * 4096 + 3072, bc + 2 * 4096 + 2048); \
    BAR_VM(NW); \
    ASTEPH(sA, sB, bn + 0 * 4096, bc + 2 * 4096 + 3072, bc + 3 * 4096 + 2048, KV1((st) + 2, 0); W1R1(RC, 0), KV1((st) + 2, 1); W1R1(RC, 1), KV1((st) + 2, 2); W1R1(RC, 2), KV1((st) + 2, 3); W1R1(RC, 3)); \
    ASTEPH(sB, sA, bn + 1 * 4096, bc + 3 * 4096 + 3072, bn + 0 * 4096 + 2048, W1D1(DC, 0), W1D1(DC, 1), W1D1(DC, 2), W1D1(DC, 3); TCH); \
  } while (0)
  const int tl = wv * 9 + (lane < 9 ? lane : 8);
#define TOUCH_WO(i) touch1(wo, (rk * 98 + ((i) * 72 + tl < 98 ? (i) * 72 + tl : 97)) * 128, lds0 + L_SM + 256)
#define TOUCH_W2(i) touch1(w2, (rk * 392 + ((i) * 72 + tl < 392 ? (i) * 72 + tl : 391)) * 128, lds0 + L_SM + 256)
  STAGE(0, 4, -1, 1, TOUCH_WO(0));
  STAGE(1, 5, 0, -1, TOUCH_WO(1); TOUCH_W2(0));
  STAGE(2, 2, 1, 2, TOUCH_W2(1));
  STAGE(3, 5, -1, 3, TOUCH_W2(2));
  STAGE(4, 5, 2, -1, TOUCH_W2(3); W1_TOUCH(6));
  STAGE(5, 2, 3, 4, TOUCH_W2(4));
  STAGE(6, 5, -1, 5, TOUCH_W2(5));
  STAGE(7, 5, 4, -1, NOH);
  STAMP(9);
  float fin[8];
  float lsum;
#pragma unroll 1
  for (int pass = 0; pass < 2; ++pass) {
    pw1[2] = pack2(e12, e13); pw1[3] = pack2(e14, e15);
    o = MFMA32(vf1, BC8(pw1), o);
    constexpr int L_X = L_HI + 65536;
    {
      f32x4 a, b2;
      if (kh) {
#pragma unroll
        for (int i = 0; i < 4; ++i) { a[i] = o[i]; b2[i] = o[4 + i]; fin[i] = o[8 + i]; fin[4 + i] = o[12 + i]; }
      } else {
#pragma unroll
        for (int i = 0; i < 4; ++i) { a[i] = o[8 + i]; b2[i] = o[12 + i]; fin[i] = o[i]; fin[4 + i] = o[4 + i]; }
      }
      *(LAS f32x4*)(L + L_X + 8192 + (wv * 2 + 0) * 1024 + l16) = a;
      *(LAS f32x4*)(L + L_X + 8192 + (wv * 2 + 1) * 1024 + l16) = b2;
      if (h == 0) *(LAS float*)(L + L_X + 24576 + (wv * 32 + r) * 4) = o[12];
    }
    BAR_LGKM();
    {
      const int pw = wv ^ 4;
      const f32x4 a = *(const LAS f32x4*)(L + L_X + 8192 + (pw * 2 + 0) * 1024 + l16);
      const f32x4 b2 = *(const LAS f32x4*)(L + L_X + 8192 + (pw * 2 + 1) * 1024 + l16);
      const float c0 = *(const LAS float*)(L + L_X + 24576 + lane * 4) + *(const LAS float*)(L + L_X + 24576 + (128 + lane) * 4);
      const float c1 = *(const LAS float*)(L + L_X + 24576 + (64 + lane) * 4) + *(const LAS float*)(L + L_X + 24576 + (192 + lane) * 4);
#pragma unroll
      for (int i = 0; i < 4; ++i) { fin[i] += a[i]; fin[4 + i] += b2[i]; }
      lsum = *(const LAS float*)(L + L_X + 24576 + (qb * 32 + r) * 4) + *(const LAS float*)(L + L_X + 24576 + ((qb + 4) * 32 + r) * 4);
      const bool ok = (c0 >= 9.094947e-13f) && (c1 >= 9.094947e-13f);
      if (pass == 1 || __all(ok)) break;
    }
    {
      u32x4 q1 = __builtin_bit_cast(u32x4, qf1);
      if (h == 0) { q1[2] = 0; q1[3] = 0; }
      qf1 = BC8(q1);
      float mx = -INFINITY;
      const unsigned ko = OFF_KV + (head * NTT + kh * 32) * 4096 + l16;
#pragma unroll 1
      for (int kt = 0; kt < 32; ++kt) {
        const u32x4 k0 = __builtin_amdgcn_raw_buffer_load_b128(rs, ko + kt * 4096, 0, 16);
        const u32x4 k1 = __builtin_amdgcn_raw_buffer_load_b128(rs, ko + kt * 4096 + 1024, 0, 16);
        f32x16 s = MFMA32(BC8(k0), qf0, zero16);
        s = MFMA32(BC8(k1), qf1, s);
#pragma unroll
        for (int i = 0; i < 16; ++i) mx = fmaxf(mx, s[i]);
      }
      mx = xhalf_max(mx);
      if (h == 0) *(LAS float*)(L + L_X + 28672 + (wv * 32 + r) * 4) = mx;
      BAR_LGKM();
      mx = fmaxf(*(const LAS float*)(L + L_X + 28672 + (qb * 32 + r) * 4), *(const LAS float*)(L + L_X + 28672 + ((qb + 4) * 32 + r) * 4));
      const float m0 = rbf(mx), m1 = rbf(mx - m0), m2 = rbf(mx - m0 - m1);
      if (h == 0) { q1[2] = pack2(-m0, -m1); q1[3] = pack2(-m2, 0.f); }
      qf1 = BC8(q1);
      DMA_KV(0, 0);
      DMA_KV(1, 32768);
      asm volatile("s_waitcnt vmcnt(4) lgkmcnt(0)\n\ts_barrier" ::: "memory");
      ATTN_PROLOGUE();
      int cur = 0, nxt = 32768, nn = 65536;
#pragma unroll 1
      for (int st = 0; st < 8; ++st) {
        const lds_cptr bc = abase + cur, bn = abase + nxt;
        ASTEP(sA, sB, bc + 2 * 4096, bc + 0 * 4096 + 3072, bc + 1 * 4096 + 2048);
        ASTEP(sB, sA, bc + 3 * 4096, bc + 1 * 4096 + 3072, bc + 2 * 4096 + 2048);
        BAR_VM(0);
        if (st + 2 < 8) DMA_KV(st + 2, nn);
        ASTEP(sA, sB, bn + 0 * 4096, bc + 2 * 4096 + 3072, bc + 3 * 4096 + 2048);
        ASTEP(sB, sA, bn + 1 * 4096, bc + 3 * 4096 + 3072, bn + 0 * 4096 + 2048);
        const int tmp = cur; cur = nxt; nxt = nn; nn = tmp;
      }
    }
  }
  {
    const float inv = __builtin_amdgcn_rcpf(lsum);
    u32x4 ov;
#pragma unroll
    for (int i = 0; i < 4; ++i) ov[i] = pack2(fin[2 * i] * inv, fin[2 * i + 1] * inv);
    if (kh == 1 && h == 0) { ov[2] = 0; ov[3] = 0; }
    __builtin_amdgcn_raw_buffer_store_b128(ov, rs, OFF_O + ((qt * 4 + qb) * 32 + head * 2 + kh) * 1024 + l16, 0, 16);
    BAR_ALL();
    if (t == 0) put_word(ws + OFF_FO + (qt * 16 + head) * 8, tag, 0);
  }
  STAMP(10);
  const int tb = wv & 1, kq = wv >> 1;
  const int tt_own = tg * 2 + tb;
  const int n0 = (sl * 2 + (kq >> 1)) * 32 + (kq & 1) * 16 + 4 * h;
  const unsigned rvo = (lane >> 3) * (DM * 2) + (((lane & 7) ^ (((lane >> 4) & 1) << 2)) * 16);
  const int rsw = ((tq >> 1) & 1) * 64;
  const lds_ptr redb = L + L_HI + tb * 32768 + l16;
  u32x2 xa2, xb2;
  unsigned xc0;
  W1_READ(5);
  {
    const u16* src_ = w1b + (size_t)6 * 64 * FF; const unsigned dst_ = lds0 + L_HI + 65536 + wv * 1024;
    dma1(src_, w1vo[0], dst_); dma1(src_, w1vo[1], dst_ + 8192); dma1(src_, w1vo[2], dst_ + 16384); dma1(src_, w1vo[3], dst_ + w1d3);
  }
  BAR_LGKM();
  if (sl < 7) {
#pragma unroll
    for (int i = 0; i < 7; ++i) { const int blk = wv * 7 + i; dma1(wo + sl * 64 + blk * 8 * DM, rvo, lds0 + L_LOW + blk * 1024); }
    asm volatile("global_load_dwordx2 %0, %3, %5\n\tglobal_load_dwordx2 %1, %3, %5 offset:16\n\tglobal_load_ushort %2, %4, %5"
                 : "=&v"(xa2), "=&v"(xb2), "=&v"(xc0) : "v"((r * DM + n0) * 2), "v"(r * DM * 2), "s"(x + (size_t)tt_own * 32 * DM) : "memory");
  }
  f32x16 uv = zero16;
  const lds_cptr pp = L + L_PK + g16 * 896 + h * 16;
  if (wv < 7) {
    bf16x8 pf[12];
#pragma unroll
    for (int hf2 = 0; hf2 < 2; ++hf2) {
#pragma unroll
      for (int i = 0; i < 12; ++i) pf[i] = ldsfrag(pp + (hf2 * 12 + i) * 32);
      SBAR();
#pragma unroll
      for (int i = 0; i < 12; ++i) uv = MFMA32(wf[hf2 * 12 + i], pf[i], uv);
      PIN(uv); SBAR();
    }
  }
#define W1_READ6() do { const lds_cptr p_ = w1rd + (L_HI + 65536 - L_LOW); \
    _Pragma("unroll") for (int i_ = 0; i_ < 4; ++i_) wf[24 + i_] = trfrag(p_ + i_ * 16 * 448, p_ + (i_ * 16 + 8) * 448); } while (0)
  STAMP(11);
  float x1[8];
  if (sl < 7) {
    if (wv == 7) wait_words(ws + OFF_FO + (tg >> 1) * 128, 16, tag, lane);
    asm volatile("s_waitcnt vmcnt(0) lgkmcnt(0)\n\ts_barrier" : "+v"(xa2), "+v"(xb2), "+v"(xc0) :: "memory");
    STAMP(12);
    u32x4 ob[8];
#pragma unroll
    for (int i = 0; i < 8; ++i) ob[i] = __builtin_amdgcn_raw_buffer_load_b128(rs, OFF_O + (tt_own * 32 + kq * 8 + i) * 1024 + l16, 0, 16);
    SBAR();
    W1_READ6();
    f32x16 acc0 = zero16, acc1 = zero16;
    {
      const lds_cptr wop = L + L_LOW + (kq * 4 * DH + 4 * h + tq) * 128 + g16 * 32 + tp * 8;
      const lds_cptr p0 = wop + rsw, p1 = wop + (64 ^ rsw);
      bf16x8 wa0[4], wa1[4];
#define WOROW0(i) (((i) >> 1) * DH + 16 * ((i) & 1))
#define WOROW1(i) (((i) >> 1) * DH + (((i) & 1) ? 20 : 8))
#pragma unroll
      for (int i = 0; i < 4; ++i) {
        wa0[i] = trfrag(p0 + WOROW0(i) * 128, p0 + WOROW1(i) * 128);
        wa1[i] = trfrag(p1 + WOROW0(i) * 128, p1 + WOROW1(i) * 128);
      }
      SBAR();
#pragma unroll
      for (int i = 0; i < 8; ++i) {
        acc0 = MFMA32(wa0[i & 3], BC8(ob[i]), acc0); acc1 = MFMA32(wa1[i & 3], BC8(ob[i]), acc1); PIN(acc0); PIN(acc1); SBAR();
        if (i < 4) {
          wa0[i] = trfrag(p0 + WOROW0(i + 4) * 128, p0 + WOROW1(i + 4) * 128);
          wa1[i] = trfrag(p1 + WOROW0(i + 4) * 128, p1 + WOROW1(i + 4) * 128);
        }
        SBAR();
      }
    }
    STAMP(13);
#define RW1(A, o8, p, KQ) do { f32x4 v0 = {A[o8], A[(o8) + 1], A[(o8) + 2], A[(o8) + 3]}, v1 = {A[(o8) + 4], A[(o8) + 5], A[(o8) + 6], A[(o8) + 7]}; \
        *(LAS f32x4*)(redb + (((p) * 4 + (KQ)) * 2) * 1024) = v0; *(LAS f32x4*)(redb + (((p) * 4 + (KQ)) * 2 + 1) * 1024) = v1; } while (0)
#define RKEEP(AT, A, o8) do { _Pragma("unroll") for (int i = 0; i < 8; ++i) AT[i] = A[(o8) + i]; } while (0)
#define RED_WRITE(AT, A0, A1) do { \
      if (kq == 0)      { RW1(A0, 8, 1, 0); RW1(A1, 0, 2, 0); RW1(A1, 8, 3, 0); RKEEP(AT, A0, 0); } \
      else if (kq == 1) { RW1(A0, 0, 0, 1); RW1(A1, 0, 2, 1); RW1(A1, 8, 3, 1); RKEEP(AT, A0, 8); } \
      else if (kq == 2) { RW1(A0, 0, 0, 2); RW1(A0, 8, 1, 2); RW1(A1, 8, 3, 2); RKEEP(AT, A1, 0); } \
      else              { RW1(A0, 0, 0, 3); RW1(A0, 8, 1, 3); RW1(A1, 0, 2, 3); RKEEP(AT, A1, 8); } } while (0)
#define RED_READ(AT) do { \
      _Pragma("unroll") for (int d = 1; d < 4; ++d) { const int src = (kq + d) & 3; \
        const f32x4 v0 = *(const LAS f32x4*)(redb + ((kq * 4 + src) * 2) * 1024); \
        const f32x4 v1 = *(const LAS f32x4*)(redb + ((kq * 4 + src) * 2 + 1) * 1024); \
        _Pragma("unroll") for (int i = 0; i < 4; ++i) { AT[i] += v0[i]; AT[4 + i] += v1[i]; } } } while (0)
    float at[8];
    RED_WRITE(at, acc0, acc1);
    const f32x4 ga0 = *(const LAS f32x4*)(L + L_GA + n0 * 4), ga1 = *(const LAS f32x4*)(L + L_GA + (n0 + 8) * 4);
    const f32x4 g20 = *(const LAS f32x4*)(L + L_G2 + n0 * 4), g21 = *(const LAS f32x4*)(L + L_G2 + (n0 + 8) * 4);
    BAR_LGKM();
    RED_READ(at);
    x1[0] = bflo(xa2[0]) + ga0[0] * at[0]; x1[1] = bfhi(xa2[0]) + ga0[1] * at[1];
    x1[2] = bflo(xa2[1]) + ga0[2] * at[2]; x1[3] = bfhi(xa2[1]) + ga0[3] * at[3];
    x1[4] = bflo(xb2[0]) + ga1[0] * at[4]; x1[5] = bfhi(xb2[0]) + ga1[1] * at[5];
    x1[6] = bflo(xb2[1]) + ga1[2] * at[6]; x1[7] = bfhi(xb2[1]) + ga1[3] * at[7];
    const float cshift = __uint_as_float(xc0 << 16);
    float p1 = 0.f, p2 = 0.f, y[8];
#pragma unroll
    for (int i = 0; i < 8; ++i) {
      const float d = x1[i] - cshift;
      p1 += d; p2 += d * d;
      y[i] = d * (i < 4 ? g20[i & 3] : g21[i & 3]);
    }
    u32x4 yv;
#pragma unroll
    for (int i = 0; i < 4; ++i) yv[i] = pack2(y[2 * i], y[2 * i + 1]);
    __builtin_amdgcn_raw_buffer_store_b128(yv, rs, OFF_Y + (tt_own * 28 + sl * 4 + kq) * 1024 + l16, 0, 16);
    p1 = xhalf_add(p1); p2 = xhalf_add(p2);
    if (h == 0) {
      u32x2 sv = {__float_as_uint(p1), __float_as_uint(p2)};
      __builtin_amdgcn_raw_buffer_store_b64(sv, rs, OFF_ST + ((tt_own * 28 + sl * 4 + kq) * 32 + r) * 8, 0, 16);
    }
    BAR_ALL();
    if (t == 0) put_word(ws + OFF_FY + tg * 128 + sl * 8, tag, 0);
    if (b == 0 && t == 64) __hip_atomic_store((gu64*)ws, epoch + 1ull, RLX_AGENT);
#pragma unroll
    for (int i = 0; i < 12; ++i) {
      const int pc = wv * 12 + i, q4 = pc / 24, rr8 = pc - q4 * 24;
      dma1(w2 + sl * 64 + (size_t)(q4 * 448 + rr8 * 8) * DM, rvo, lds0 + L_HI + pc * 1024);
    }
  } else {
    BAR_ALL();
    W1_READ6();
  }
  STAMP(14);
#pragma unroll
  for (int i = 24; i < 28; ++i) uv = MFMA32(wf[i], ldsfrag(pp + i * 32), uv);
  f32x16 u2, v2;
#pragma unroll
  for (int i = 0; i < 16; ++i) {
    auto rr = __builtin_amdgcn_permlane16_swap(__float_as_uint(uv[i]), __float_as_uint(uv[i]), false, false);
    u2[i] = __uint_as_float(rr[0]); v2[i] = __uint_as_float(rr[1]) * -1.4426950408889634f;
  }
  PIN(u2); PIN(v2);
  if (wv == 7) wait_words(ws + OFF_FY + tg * 128, 7, tag, lane);
  BAR_LGKM();
  STAMP(15);
  {
    const char* ysrc = ws + OFF_Y + (size_t)tg * 57344;
    if (wv < 7) { dma4c(ysrc + wv * 8192, l16, lds0 + L_LOW + wv * 8192); dma4c(ysrc + wv * 8192 + 4096, l16, lds0 + L_LOW + wv * 8192 + 4096); }
    else {
      float a1 = 0.f, a2 = 0.f;
      u32x2 sv[28];
#pragma unroll
      for (int i = 0; i < 28; ++i) sv[i] = __builtin_amdgcn_raw_buffer_load_b64(rs, OFF_ST + (((tg * 2 + h) * 28 + i) * 32 + r) * 8, 0, 16);
#pragma unroll
      for (int i = 0; i < 28; ++i) { a1 += __uint_as_float(sv[i][0]); a2 += __uint_as_float(sv[i][1]); }
      const float m = a1 * (1.f / DM);
      const float var = fmaxf(a2 * (1.f / DM) - m * m, 0.f);
      f32x2 o2 = {__builtin_amdgcn_rsqf(var + 1e-5f), m};
      *(LAS f32x2*)(L + L_SM + 512 + lane * 8) = o2;
    }
  }
  BAR_ALL();
  STAMP(16);
  f32x16 c0 = zero16, c1 = zero16;
  f32x2 st0, st1;
  f32x4 gb0, gb1;
  const unsigned ho1 = OFF_H + ((tg * 2) * 112 + (sl * 7 + wv) * 2) * 1024 + l16;
#define SILU1(C, ST, i) do { const float t_ = __builtin_fmaf(-ST[1], u2[i], C[i]); const float q_ = __builtin_fmaf(ST[0], t_, v2[i]); \
      C[i] = q_ * __builtin_amdgcn_rcpf(1.f + EX2(q_)); } while (0)
  gb0 = *(const LAS f32x4*)(L + L_GBV + (sl < 7 ? n0 : 0) * 4); gb1 = *(const LAS f32x4*)(L + L_GBV + ((sl < 7 ? n0 : 0) + 8) * 4);
  if (wv < 7) {
    const lds_cptr yp = L + L_LOW + l16;
    st0 = *(const LAS f32x2*)(L + L_SM + 512 + r * 8); st1 = *(const LAS f32x2*)(L + L_SM + 512 + (32 + r) * 8);
    st0[0] *= -1.4426950408889634f; st1[0] *= -1.4426950408889634f;
    bf16x8 yb[4];
#pragma unroll
    for (int i = 0; i < 4; ++i) yb[i] = ldsfrag(yp + i * 1024);
    SBAR();
#pragma unroll
    for (int ks = 0; ks < 28; ++ks) {
      c0 = MFMA32(wf[ks], yb[ks & 3], c0); SBAR();
      yb[ks & 3] = ldsfrag(yp + (ks + 4) * 1024); SBAR();
    }
#pragma unroll
    for (int ks = 0; ks < 28; ++ks) {
      c1 = MFMA32(wf[ks], yb[ks & 3], c1); SBAR();
      if (ks + 4 < 28) yb[ks & 3] = ldsfrag(yp + (28 + ks + 4) * 1024);
      if (ks >= 2 && ks < 18) { SILU1(c0, st0, ks - 2); PIN(c0); }
      if (ks == 19) __builtin_amdgcn_raw_buffer_store_b128(pack8(c0, 0), rs, ho1, 0, 16);
      if (ks == 21) __builtin_amdgcn_raw_buffer_store_b128(pack8(c0, 1), rs, ho1 + 1024, 0, 16);
      SBAR();
    }
  }
  BAR_LGKM();
  STAMP(17);
  if (wv < 7) {
#pragma unroll
    for (int i = 0; i < 16; ++i) SILU1(c1, st1, i);
    __builtin_amdgcn_raw_buffer_store_b128(pack8(c1, 0), rs, ho1 + 112 * 1024, 0, 16);
    __builtin_amdgcn_raw_buffer_store_b128(pack8(c1, 1), rs, ho1 + 113 * 1024, 0, 16);
  }
  STAMP(18);
  BAR_ALL();
  if (t == 0) put_word(ws + OFF_FH + tg * 128 + sl * 8, tag, 0);
  STAMP(19);
  if (sl == 7) { STAMP_DUMP(); return; }
#pragma unroll
  for (int i = 0; i < 8; ++i) {
    const int pc = wv * 8 + i, q4 = pc >> 4, rr8 = pc & 15;
    dma1(w2 + sl * 64 + (size_t)(q4 * 448 + 192 + rr8 * 8) * DM, rvo, lds0 + pc * 1024);
  }
  BAR_VM(0);
  {
    const int rowoff = (4 * h + tq) * 128 + g16 * 32 + tp * 8;
    const lds_cptr lo0 = L + kq * 16384 + rowoff + rsw, lo1 = L + kq * 16384 + rowoff + (64 ^ rsw);
    bf16x8 r0[8], r1[8];
#pragma unroll
    for (int i = 0; i < 8; ++i) {
      r0[i] = trfrag(lo0 + i * 2048, lo0 + i * 2048 + 1024);
      r1[i] = trfrag(lo1 + i * 2048, lo1 + i * 2048 + 1024);
    }
    BAR_LGKM();
#pragma unroll
    for (int i = 0; i < 8; ++i) {
      const int pc = wv * 8 + i, q4 = pc >> 4, rr8 = pc & 15;
      dma1(w2 + sl * 64 + (size_t)(q4 * 448 + 320 + rr8 * 8) * DM, rvo, lds0 + pc * 1024);
    }
    if (wv == 7) wait_words(ws + OFF_FH + tg * 128, 8, tag, lane);
    BAR_ALL();
    STAMP(20);
    u32x4 hb[28];
    const unsigned ho = OFF_H + (tt_own * 112 + kq * 28) * 1024 + l16;
#pragma unroll
    for (int i = 0; i < 28; ++i) {
      const int ks = (i < 8) ? 12 + i : (i < 20) ? i - 8 : i;
      hb[i] = __builtin_amdgcn_raw_buffer_load_b128(rs, ho + ks * 1024, 0, 16);
    }
    SBAR();
    f32x16 d0 = zero16, d1 = zero16;
    const lds_cptr hi0 = L + L_HI + kq * 24576 + rowoff + rsw, hi1 = L + L_HI + kq * 24576 + rowoff + (64 ^ rsw);
#pragma unroll
    for (int i = 0; i < 28; ++i) {
      d0 = MFMA32(r0[i & 7], BC8(hb[i]), d0); PIN(d0); SBAR();
      if (i + 8 < 20) r0[i & 7] = trfrag(hi0 + i * 2048, hi0 + i * 2048 + 1024);
      else if (i + 8 < 28) r0[i & 7] = trfrag(lo0 + (i - 12) * 2048, lo0 + (i - 12) * 2048 + 1024);
      SBAR();
      d1 = MFMA32(r1[i & 7], BC8(hb[i]), d1); PIN(d1); SBAR();
      if (i + 8 < 20) r1[i & 7] = trfrag(hi1 + i * 2048, hi1 + i * 2048 + 1024);
      else if (i + 8 < 28) r1[i & 7] = trfrag(lo1 + (i - 12) * 2048, lo1 + (i - 12) * 2048 + 1024);
      SBAR();
    }
    STAMP(21);
    BAR_LGKM();
    float f[8];
    RED_WRITE(f, d0, d1);
    BAR_LGKM();
    RED_READ(f);
#pragma unroll
    for (int i = 0; i < 4; ++i) { gb0[i] *= -0.6931471805599453f; gb1[i] *= -0.6931471805599453f; }
    u32x2 oa, ob2;
    oa[0] = pack2(x1[0] + gb0[0] * f[0], x1[1] + gb0[1] * f[1]);
    oa[1] = pack2(x1[2] + gb0[2] * f[2], x1[3] + gb0[3] * f[3]);
    ob2[0] = pack2(x1[4] + gb1[0] * f[4], x1[5] + gb1[1] * f[5]);
    ob2[1] = pack2(x1[6] + gb1[2] * f[6], x1[7] + gb1[3] * f[7]);
    u16* orow = out + (size_t)(tt_own * 32 + r) * DM;
    *(u32x2*)(orow + n0) = oa;
    *(u32x2*)(orow + n0 + 8) = ob2;
  }
  STAMP(22);
  STAMP_DUMP();
}

extern "C" void kernel_launch(void* const* d_in, const int* in_sizes, int n_in, void* d_out, int out_size, void* d_ws,
                              size_t ws_size, hipStream_t stream) {
  fused_block<<<256, 512, 0, stream>>>((const u16*)d_in[0], (const u16*)d_in[1], (const u16*)d_in[2], (const u16*)d_in[4],
                                       (const u16*)d_in[5], (const u16*)d_in[6], (const u16*)d_in[7], (u16*)d_out, (char*)d_ws);
}
```

```cpp
#include <hip/hip_runtime.h>
#include <hip/hip_bf16.h>
#include <stdint.h>

typedef __attribute__((ext_vector_type(8))) short bf16x8;
typedef __attribute__((ext_vector_type(4))) short s16x4;
typedef __attribute__((ext_vector_type(16))) float f32x16;
typedef __attribute__((ext_vector_type(4))) unsigned u32x4;
typedef __attribute__((ext_vector_type(2))) unsigned u32x2;
typedef __attribute__((ext_vector_type(4))) float f32x4;
typedef __attribute__((ext_vector_type(2))) float f32x2;
typedef __attribute__((ext_vector_type(2))) __bf16 bf16x2_t;
typedef __attribute__((ext_vector_type(2))) _Float16 h16x2;
typedef unsigned short u16;
typedef unsigned long long u64;
#define GAS __attribute__((address_space(1)))
#define LAS __attribute__((address_space(3)))
typedef GAS u64 gu64;
typedef LAS const char* lds_cptr;
typedef LAS char* lds_ptr;

#define DI __device__ __forceinline__
#define MFMA32(a, b, c) __builtin_amdgcn_mfma_f32_32x32x16_bf16((a), (b), (c), 0, 0, 0)
#define MFMA16(a, b, c) __builtin_amdgcn_mfma_f32_16x16x32_bf16((a), (b), (c), 0, 0, 0)
#define SBAR() __builtin_amdgcn_sched_barrier(0)
#define PIN(x) asm volatile("" : "+v"(x))
#define RLX_AGENT __ATOMIC_RELAXED, __HIP_MEMORY_SCOPE_AGENT
#define BC8(x) __builtin_bit_cast(bf16x8, x)
#define EX2(v) __builtin_amdgcn_exp2f(v)
#define BAR_LGKM() asm volatile("s_waitcnt lgkmcnt(0)\n\ts_barrier" ::: "memory")
#define BAR_ALL() asm volatile("s_waitcnt vmcnt(0) lgkmcnt(0)\n\ts_barrier" ::: "memory")
#define BAR_VM(n) asm volatile("s_waitcnt vmcnt(" #n ")\n\ts_barrier" ::: "memory")

constexpr int DM = 448, DH = 28, FF = 1792;
constexpr int NTT = 64;

constexpr unsigned OFF_MODG = 4096;
constexpr unsigned OFF_FKV = 32768;
constexpr unsigned OFF_FO = 36864;
constexpr unsigned OFF_FY = 40960;
constexpr unsigned OFF_FH = 49152;
constexpr unsigned OFF_ST = 65536;
constexpr unsigned OFF_Q = 1u << 20;
constexpr unsigned OFF_KV = 3u << 20;
constexpr unsigned OFF_O = 7u << 20;
constexpr unsigned OFF_Y = 9u << 20;
constexpr unsigned OFF_H = 11u << 20;
constexpr unsigned WS_USED = 19u << 20;

#define STAMP(i) do {} while (0)
#define STAMP_DECL() do {} while (0)
#define STAMP_DUMP() do {} while (0)

DI float bf2f(u16 v) { return __uint_as_float((unsigned)v << 16); }
DI float bflo(unsigned w) { return __uint_as_float(w << 16); }
DI float bfhi(unsigned w) { return __uint_as_float(w & 0xffff0000u); }
DI unsigned pack2(float lo, float hi) {
  f32x2 v = {lo, hi};
  bf16x2_t b = __builtin_convertvector(v, bf16x2_t);
  return __builtin_bit_cast(unsigned, b);
}
DI unsigned packh2(float lo, float hi) {
  f32x2 v = {lo, hi};
  h16x2 b = __builtin_convertvector(v, h16x2);
  return __builtin_bit_cast(unsigned, b);
}
DI float hlo(unsigned w) { return (float)__builtin_bit_cast(h16x2, w)[0]; }
DI float hhi(unsigned w) { return (float)__builtin_bit_cast(h16x2, w)[1]; }
DI float fmix_lo(unsigned w, float g, float b) { float d; asm("v_fma_mix_f32 %0, %1, %2, %3 op_sel_hi:[1,0,0]" : "=v"(d) : "v"(w), "v"(g), "v"(b)); return d; }
DI float fmix_hi(unsigned w, float g, float b) { float d; asm("v_fma_mix_f32 %0, %1, %2, %3 op_sel:[1,0,0] op_sel_hi:[1,0,0]" : "=v"(d) : "v"(w), "v"(g), "v"(b)); return d; }
DI float rbf(float x) { return bflo(pack2(x, x)); }
DI u32x4 pack8(const f32x16& x, int s) {
  u32x4 p;
  p[0] = pack2(x[8 * s + 0], x[8 * s + 1]);
  p[1] = pack2(x[8 * s + 2], x[8 * s + 3]);
  p[2] = pack2(x[8 * s + 4], x[8 * s + 5]);
  p[3] = pack2(x[8 * s + 6], x[8 * s + 7]);
  return p;
}
DI float xhalf_add(float v) {
  auto rr = __builtin_amdgcn_permlane32_swap(__float_as_uint(v), __float_as_uint(v), false, false);
  return __uint_as_float(rr[0]) + __uint_as_float(rr[1]);
}
DI float xhalf_max(float v) {
  auto rr = __builtin_amdgcn_permlane32_swap(__float_as_uint(v), __float_as_uint(v), false, false);
  return fmaxf(__uint_as_float(rr[0]), __uint_as_float(rr[1]));
}
DI void dma1(const void* sbase, unsigned voff, unsigned lds_base) {
  unsigned sv;
  asm volatile("s_mov_b32 %0, m0\n\ts_mov_b32 m0, %2\n\ts_nop 0\n\tglobal_load_lds_dwordx4 %1, %3\n\ts_mov_b32 m0, %0"
               : "=&s"(sv) : "v"(voff), "s"(lds_base), "s"(sbase) : "memory");
}
DI void dma1c(const void* sbase, unsigned voff, unsigned lds_base) {
  unsigned sv;
  asm volatile("s_mov_b32 %0, m0\n\ts_mov_b32 m0, %2\n\ts_nop 0\n\tglobal_load_lds_dwordx4 %1, %3 sc1\n\ts_mov_b32 m0, %0"
               : "=&s"(sv) : "v"(voff), "s"(lds_base), "s"(sbase) : "memory");
}
DI void dma4c(const void* sbase, unsigned voff, unsigned lds_base) {
  unsigned sv;
  asm volatile("s_mov_b32 %0, m0\n\ts_mov_b32 m0, %2\n\ts_nop 0\n\t"
               "global_load_lds_dwordx4 %1, %3 sc1\n\tglobal_load_lds_dwordx4 %1, %3 offset:1024 sc1\n\t"
               "global_load_lds_dwordx4 %1, %3 offset:2048 sc1\n\tglobal_load_lds_dwordx4 %1, %3 offset:3072 sc1\n\t"
               "s_mov_b32 m0, %0" : "=&s"(sv) : "v"(voff), "s"(lds_base), "s"(sbase) : "memory");
}
DI void touch1(const void* sbase, unsigned voff, unsigned lds_base) {
  unsigned sv;
  asm volatile("s_mov_b32 %0, m0\n\ts_mov_b32 m0, %2\n\ts_nop 0\n\tglobal_load_lds_dword %1, %3\n\ts_mov_b32 m0, %0"
               : "=&s"(sv) : "v"(voff), "s"(lds_base), "s"(sbase) : "memory");
}
DI bf16x8 ldsfrag(lds_cptr p) { return *(const LAS bf16x8*)p; }
DI s16x4 trread(lds_cptr p) { return __builtin_amdgcn_ds_read_tr16_b64_v4i16((LAS s16x4*)p); }
DI bf16x8 trfrag(lds_cptr p0, lds_cptr p1) {
  s16x4 lo = trread(p0), hi = trread(p1);
  return __builtin_shufflevector(lo, hi, 0, 1, 2, 3, 4, 5, 6, 7);
}
DI float dot2bf(unsigned a, unsigned b, float c) {
  return __builtin_amdgcn_fdot2_f32_bf16(__builtin_bit_cast(bf16x2_t, a), __builtin_bit_cast(bf16x2_t, b), c, false);
}
DI unsigned wait_words(const char* p, int n, unsigned tag, int lane) {
  const gu64* f = (const gu64*)p + (lane < n ? lane : n - 1);
  const u64 t0 = __builtin_amdgcn_s_memrealtime();
  u64 v;
  for (;;) {
    v = __hip_atomic_load(f, RLX_AGENT);
    if (__all((unsigned)(v >> 32) == tag)) break;
    if (__builtin_amdgcn_s_memrealtime() - t0 > 2000000ull) break;
    __builtin_amdgcn_s_sleep(1);
  }
  return (unsigned)v;
}
DI void put_word(char* p, unsigned tag, unsigned val) { __hip_atomic_store((gu64*)p, ((u64)tag << 32) | val, RLX_AGENT); }

constexpr int L_XS = 0;
constexpr int L_WKV = 0;
constexpr int L_XCH = 65536;
constexpr int L_WQ = 114688;
constexpr int L_MODW = 143360;
constexpr int L_SILU = 157696;
constexpr int L_STAT = 143360;
constexpr int L_GB = 145408;
constexpr int L_KMX = 158592;
constexpr int L_LOW = 0;
constexpr int L_M2 = 57344;
constexpr int L_GA = L_M2, L_G2 = L_M2 + 1792, L_GBV = L_M2 + 3584, L_PK = L_M2 + 5376;
constexpr int L_SM = L_M2 + 7168;
constexpr int L_HI = 65536;
constexpr int LDS_BYTES = 160 * 1024;

#define ASTEPH(SC, SN, KADDR, V1ADDR, V0ADDR, H1, H2, H3, H4) do { \
    SN = MFMA32(kf0, qf0, zero16); SBAR(); \
    pw1[2] = pack2(e12, e13); pw1[3] = pack2(e14, e15); \
    kf0 = ldsfrag(KADDR); SBAR(); \
    SC[0] = EX2(SC[0]); SC[1] = EX2(SC[1]); SBAR(); H1; SBAR(); SC[2] = EX2(SC[2]); SC[3] = EX2(SC[3]); PIN(SC); PIN(pw1); SBAR(); \
    o = MFMA32(vf1, BC8(pw1), o); SBAR(); \
    vf1 = ldsfrag(V1ADDR); \
    SC[4] = EX2(SC[4]); SC[5] = EX2(SC[5]); SBAR(); pw0[0] = pack2(SC[0], SC[1]); pw0[1] = pack2(SC[2], SC[3]); SBAR(); H2; SBAR(); SC[6] = EX2(SC[6]); SC[7] = EX2(SC[7]); PIN(SC); PIN(pw0); SBAR(); \
    SN = MFMA32(kf1, qf1, SN); SBAR(); \
    kf1 = ldsfrag((KADDR) + 1024); \
    SC[8] = EX2(SC[8]); SC[9] = EX2(SC[9]); SBAR(); pw0[2] = pack2(SC[4], SC[5]); pw0[3] = pack2(SC[6], SC[7]); SBAR(); H3; SBAR(); SC[10] = EX2(SC[10]); SC[11] = EX2(SC[11]); PIN(SC); PIN(pw0); SBAR(); \
    o = MFMA32(vf0, BC8(pw0), o); SBAR(); \
    vf0 = ldsfrag(V0ADDR); \
    e12 = EX2(SC[12]); e13 = EX2(SC[13]); SBAR(); pw1[0] = pack2(SC[8], SC[9]); pw1[1] = pack2(SC[10], SC[11]); SBAR(); H4; SBAR(); e14 = EX2(SC[14]); e15 = EX2(SC[15]); PIN(e12); PIN(e13); PIN(e14); PIN(e15); PIN(pw1); SBAR(); \
  } while (0)
#define NOH ((void)0)
#define ASTEP(SC, SN, KADDR, V1ADDR, V0ADDR) ASTEPH(SC, SN, KADDR, V1ADDR, V0ADDR, NOH, NOH, NOH, NOH)

#define ATTN_PROLOGUE() \
    kf0 = ldsfrag(abase); kf1 = ldsfrag(abase + 1024); \
    vf0 = ldsfrag(abase + 2048); vf1 = vf0; \
    e12 = 0.f; e13 = 0.f; e14 = 0.f; e15 = 0.f; pw1[0] = 0; pw1[1] = 0; o = zero16; \
    sA = MFMA32(kf0, qf0, zero16); sA = MFMA32(kf1, qf1, sA); \
    kf0 = ldsfrag(abase + 4096); kf1 = ldsfrag(abase + 4096 + 1024);

__global__ void __launch_bounds__(512) fused_block(const u16* __restrict__ x, const u16* __restrict__ wqkv, const u16* __restrict__ wo,
                                                    const u16* __restrict__ w1, const u16* __restrict__ w2, const u16* __restrict__ cvec,
                                                    const u16* __restrict__ wmod, u16* __restrict__ out, char* __restrict__ ws) {
  __shared__ __attribute__((aligned(16))) char lds_arr[LDS_BYTES];
  const lds_ptr L = (lds_ptr)lds_arr;
  const unsigned lds0 = (unsigned)(uintptr_t)lds_arr;
  const int t = threadIdx.x, lane = t & 63, wv = __builtin_amdgcn_readfirstlane(t >> 6), r = lane & 31, h = lane >> 5;
  __builtin_assume(wv < 8);
  const int g16 = (lane >> 4) & 1, tq = (lane & 15) >> 2, tp = lane & 3;
  const int b = blockIdx.x, xg = b & 7, rk = b >> 3;
  const __amdgpu_buffer_rsrc_t rs = __builtin_amdgcn_make_buffer_rsrc(ws, 0, WS_USED, 0x00020000);
  asm volatile("" :: "s"(x), "s"(wqkv), "s"(wo), "s"(w1), "s"(w2), "s"(cvec), "s"(wmod), "s"(out), "s"(ws));
  STAMP_DECL();
  STAMP(0);

  const int rt = xg * 2 + (rk >> 4), hq = rk & 15;
  const bool modwave = (rk < 21) && (wv == 0);
  const int msl = xg * 21 + rk;
  u32x4 craw = {0, 0, 0, 0};
  if (modwave) {
    asm volatile("global_load_dwordx4 %0, %1, %2" : "=v"(craw) : "v"((lane < 56 ? lane : 55) * 16), "s"(cvec) : "memory");
    const unsigned vo = (unsigned)(lane >> 1) * (6 * DM * 2) + (lane & 1) * 16;
#pragma unroll
    for (int i = 0; i < 14; ++i) dma1(wmod + msl * 16 + i * 32 * 6 * DM, vo, lds0 + L_MODW + i * 1024);
  }
  const int a_tt = wv & 3, a_kh = wv >> 2;
  if (wv > 0) {
    const u16* xb = x + (size_t)(rt * 128) * DM;
    const u16* xw = xb + (wv - 1) * 16 * DM;
    const unsigned dw = lds0 + L_XS + (wv - 1) * 14336;
#pragma unroll
    for (int i = 0; i < 7; ++i) {
      const int q = i * 64 + lane, rl = (q * 1171) >> 16, c = q - rl * 56;
      const unsigned vo = rl * (DM * 2) + ((c ^ (rl >> 1)) * 16);
      dma1(xw, vo, dw + i * 1024);
      dma1(xw + 8 * DM, vo ^ 64, dw + 7168 + i * 1024);
    }
    const int q = (wv - 1) * 64 + lane, rl = (q * 1171) >> 16, c = q - rl * 56;
    const unsigned vo = rl * (DM * 2) + ((c ^ (rl >> 1)) * 16);
    dma1(xb + 112 * DM, vo, lds0 + L_XS + 14 * 7168 + (wv - 1) * 1024);
    dma1(xb + 120 * DM, vo ^ 64, lds0 + L_XS + 15 * 7168 + (wv - 1) * 1024);
  }
  u64 epoch;
  asm volatile("s_load_dwordx2 %0, %1, 0x0" : "=s"(epoch) : "s"(ws) : "memory");
  const unsigned wvo = (lane >> 2) * (3 * DM * 2) + ((tp == 3) ? 40 : tp * 16);
  if (wv > 0) {
#pragma unroll
    for (int i = 0; i < 4; ++i) { const int s = (wv - 1) * 4 + i; dma1(wqkv + hq * DH + s * 16 * 3 * DM, wvo, lds0 + L_WQ + s * 1024); }
  }
  if (t == 0) *(LAS unsigned*)(L + L_KMX) = 0;
  asm volatile("s_waitcnt lgkmcnt(0)" : "+s"(epoch) :: "memory");
  const unsigned tag = (unsigned)epoch + 1u;
  if (modwave) {
    asm volatile("s_waitcnt vmcnt(14)" : "+v"(craw) :: "memory");
    u32x4 sv;
#pragma unroll
    for (int j = 0; j < 4; ++j) {
      const float c0_ = bflo(craw[j]), c1_ = bfhi(craw[j]);
      sv[j] = pack2(c0_ * __builtin_amdgcn_rcpf(1.f + EX2(-1.4426950408889634f * c0_)), c1_ * __builtin_amdgcn_rcpf(1.f + EX2(-1.4426950408889634f * c1_)));
    }
    if (lane < 56) *(LAS u32x4*)(L + L_SILU + lane * 16) = sv;
    const int kg = lane >> 4;
    const lds_cptr bp = L + L_MODW + (8 * kg + tq) * 32 + tp * 8;
    const lds_cptr ap = L + L_SILU + kg * 16;
    bf16x8 af[14], bfr[14];
    asm volatile("s_waitcnt lgkmcnt(0)" ::: "memory");
#pragma unroll
    for (int s = 0; s < 14; ++s) af[s] = ldsfrag(ap + s * 64);
    asm volatile("s_waitcnt vmcnt(0)" ::: "memory");
#pragma unroll
    for (int s = 0; s < 14; ++s) bfr[s] = trfrag(bp + s * 1024, bp + s * 1024 + 128);
    f32x4 acc0 = {0.f, 0.f, 0.f, 0.f}, acc1 = acc0;
#pragma unroll
    for (int s = 0; s < 14; s += 2) { acc0 = MFMA16(af[s], bfr[s], acc0); acc1 = MFMA16(af[s + 1], bfr[s + 1], acc1); }
    if (lane < 16) put_word(ws + OFF_MODG + (msl * 16 + lane) * 8, tag, __float_as_uint(rbf(acc0[0] + acc1[0])));
  }
  STAMP(1);
  BAR_VM(4);
  u32x4 xr[14];
  {
    const int row = a_tt * 32 + r, gsw = (row >> 1) & 7, c0 = a_kh * 28 + h;
#pragma unroll
    for (int i = 0; i < 4; ++i) {
      const int c = c0 + 2 * i;
      const lds_cptr xa = L + L_XS + row * (DM * 2) + (c & ~7) * 16 + (((c & 7) ^ gsw) * 16);
#pragma unroll
      for (int s = i; s < 14; s += 4) xr[s] = *(const LAS u32x4*)(xa + (s >> 2) * 128);
    }
  }
  u32x4 mg;
  asm volatile("global_load_dwordx4 %0, %1, %2 offset:0 sc1" : "=v"(mg) : "v"((t < DM ? t : DM - 1) * 16), "s"(ws + OFF_MODG) : "memory");
  BAR_LGKM();
  STAMP(2);
  {
    const u16* wsrc = wqkv + hq * DH + (1 + (wv >> 2)) * DM + (wv & 3) * 7 * 16 * 3 * DM;
    float s1a = 0.f, s2a = 0.f, s1b = 0.f, s2b = 0.f;
#pragma unroll
    for (int s = 0; s < 14; ++s) {
      if ((s & 1) == 0) dma1(wsrc + (s >> 1) * 16 * 3 * DM, wvo, lds0 + L_WKV + (wv * 7 + (s >> 1)) * 1024);
      s1a = dot2bf(xr[s][0], 0x3f803f80u, s1a); s2a = dot2bf(xr[s][0], xr[s][0], s2a);
      s1b = dot2bf(xr[s][1], 0x3f803f80u, s1b); s2b = dot2bf(xr[s][1], xr[s][1], s2b);
      s1a = dot2bf(xr[s][2], 0x3f803f80u, s1a); s2a = dot2bf(xr[s][2], xr[s][2], s2a);
      s1b = dot2bf(xr[s][3], 0x3f803f80u, s1b); s2b = dot2bf(xr[s][3], xr[s][3], s2b);
      PIN(s1a); PIN(s2a); PIN(s1b); PIN(s2b); SBAR();
    }
    const float s1 = xhalf_add(s1a + s1b), s2 = xhalf_add(s2a + s2b);
    if (h == 0) { f32x2 v = {s1, s2}; *(LAS f32x2*)(L + L_STAT + (wv * 32 + r) * 8) = v; }
    asm volatile("s_waitcnt vmcnt(7)" : "+v"(mg) :: "memory");
    if (wv < 7) {
      if (!__all(mg[1] == tag && mg[3] == tag)) {
        const u64 t0 = __builtin_amdgcn_s_memrealtime();
        for (;;) {
          mg = __builtin_amdgcn_raw_buffer_load_b128(rs, OFF_MODG + t * 16, 0, 16);
          if (__all(mg[1] == tag && mg[3] == tag)) break;
          if (__builtin_amdgcn_s_memrealtime() - t0 > 2000000ull) break;
          __builtin_amdgcn_s_sleep(1);
        }
      }
      f32x2 o2 = {__uint_as_float(mg[0]), __uint_as_float(mg[2])};
      const bool isg = t >= 224;
      if (isg) { o2[0] = rbf(1.f + o2[0]); o2[1] = rbf(1.f + o2[1]); }
      *(LAS f32x2*)(L + L_GB + (isg ? 2 * t - DM : DM + 2 * t) * 4) = o2;
    }
    BAR_LGKM();
    STAMP(3);
    {
      const unsigned to = (unsigned)(((rk >> 3) * 32 + wv * 4 + ((lane >> 2) & 3)) * (FF * 2) + (((rk & 7) * 448) & ~127) + (lane & 3) * 128);
      touch1(w1, to, lds0 + L_KMX + 64);
    }
    const lds_cptr gp = L + L_GB + (a_kh * 224 + 8 * h) * 4;
    f32x4 gq[3][4];
#define LOADGB(slot, s) do { gq[slot][0] = *(const LAS f32x4*)(gp + (s) * 64); gq[slot][1] = *(const LAS f32x4*)(gp + (s) * 64 + 16); \
      gq[slot][2] = *(const LAS f32x4*)(gp + DM * 4 + (s) * 64); gq[slot][3] = *(const LAS f32x4*)(gp + DM * 4 + (s) * 64 + 16); } while (0)
    const f32x2 o2 = *(const LAS f32x2*)(L + L_STAT + ((wv ^ 4) * 32 + r) * 8);
    LOADGB(0, 0); LOADGB(1, 1);
    const float mu = (s1 + o2[0]) * (1.f / DM);
    const float var = fmaxf((s2 + o2[1]) * (1.f / DM) - mu * mu, 0.f);
    const float rstd = __builtin_amdgcn_rsqf(var + 1e-5f);
    const float nb = -mu * rstd;
    SBAR();
#pragma unroll
    for (int s = 0; s < 14; ++s) {
      if (s + 2 < 14) LOADGB((s + 2) % 3, s + 2);
      SBAR();
      const f32x4 g0 = gq[s % 3][0], g1 = gq[s % 3][1], b0 = gq[s % 3][2], b1 = gq[s % 3][3];
      xr[s][0] = pack2((bflo(xr[s][0]) * rstd + nb) * g0[0] + b0[0], (bfhi(xr[s][0]) * rstd + nb) * g0[1] + b0[1]);
      xr[s][1] = pack2((bflo(xr[s][1]) * rstd + nb) * g0[2] + b0[2], (bfhi(xr[s][1]) * rstd + nb) * g0[3] + b0[3]);
      xr[s][2] = pack2((bflo(xr[s][2]) * rstd + nb) * g1[0] + b1[0], (bfhi(xr[s][2]) * rstd + nb) * g1[1] + b1[1]);
      xr[s][3] = pack2((bflo(xr[s][3]) * rstd + nb) * g1[2] + b1[2], (bfhi(xr[s][3]) * rstd + nb) * g1[3] + b1[3]);
      PIN(xr[s]); SBAR();
    }
  }
  f32x16 zero16;
#pragma unroll
  for (int i = 0; i < 16; ++i) zero16[i] = 0.f;
  f32x16 aq = zero16, ak = zero16, av = zero16;
  {
    const int wro = a_kh * 14 * 1024 + (8 * h + tq) * 64 + g16 * 32 + tp * 8;
    const lds_cptr wqp = L + L_WQ + wro, wkp = L + L_WKV + wro, wvp = L + L_WKV + 28672 + wro;
    bf16x8 wa[14], wb[14];
#pragma unroll
    for (int s = 0; s < 14; ++s) wa[s] = trfrag(wqp + s * 1024, wqp + s * 1024 + 256);
    SBAR();
#pragma unroll
    for (int s = 0; s < 7; ++s) { aq = MFMA32(wa[s], BC8(xr[s]), aq); PIN(aq); SBAR(); }
    BAR_VM(0);
    STAMP(4);
#pragma unroll
    for (int s = 7; s < 14; ++s) {
      aq = MFMA32(wa[s], BC8(xr[s]), aq); PIN(aq); SBAR();
      wb[2 * (s - 7)] = trfrag(wkp + 2 * (s - 7) * 1024, wkp + 2 * (s - 7) * 1024 + 256);
      wb[2 * (s - 7) + 1] = trfrag(wkp + (2 * (s - 7) + 1) * 1024, wkp + (2 * (s - 7) + 1) * 1024 + 256); SBAR();
    }
#pragma unroll
    for (int s = 0; s < 14; ++s) {
      ak = MFMA32(wb[s], BC8(xr[s]), ak); PIN(ak); SBAR();
      wa[s] = trfrag(wvp + s * 1024, wvp + s * 1024 + 256); SBAR();
    }
#pragma unroll
    for (int s = 0; s < 14; ++s) { av = MFMA32(BC8(xr[s]), wa[s], av); PIN(av); SBAR(); }
  }
  STAMP(5);
  {
    const lds_ptr xw = L + L_XCH + wv * 6144 + lane * 16;
    const lds_cptr xrd = L + L_XCH + (wv ^ 4) * 6144 + lane * 16;
    const int tt = rt * 4 + a_tt;
    const bool vpad = (r >= 24 && r < 28);
    const unsigned vconst = (r == 24) ? 0x3f803f80u : 0u;
#define W4(off, A, i0) do { f32x4 v_ = {A[i0], A[(i0) + 1], A[(i0) + 2], A[(i0) + 3]}; *(LAS f32x4*)(xw + (off)) = v_; } while (0)
#define R4(off, A, i0) do { const f32x4 v_ = *(const LAS f32x4*)(xrd + (off)); A[i0] += v_[0]; A[(i0) + 1] += v_[1]; A[(i0) + 2] += v_[2]; A[(i0) + 3] += v_[3]; } while (0)
    if (a_kh == 0) {
      W4(0, ak, 0); W4(1024, ak, 4); W4(2048, ak, 8); W4(3072, ak, 12); W4(4096, av, 8); W4(5120, av, 12);
      BAR_LGKM();
      R4(0, aq, 0); R4(1024, aq, 4); R4(2048, aq, 8); R4(3072, aq, 12); R4(4096, av, 0); R4(5120, av, 4);
      const float qs = 0.18898223650461362f * 1.4426950408889634f;
#pragma unroll
      for (int i = 0; i < 16; ++i) aq[i] *= qs;
      u32x4 f0 = pack8(aq, 0), f1 = pack8(aq, 1), vf = pack8(av, 0);
      if (h == 0) { f1[2] = 0; f1[3] = 0; }
#pragma unroll
      for (int i = 0; i < 4; ++i) vf[i] = vpad ? vconst : vf[i];
      const unsigned mo = OFF_Q + (hq * NTT + tt) * 2048 + lane * 16;
      __builtin_amdgcn_raw_buffer_store_b128(f0, rs, mo, 0, 16);
      __builtin_amdgcn_raw_buffer_store_b128(f1, rs, mo + 1024, 0, 16);
      __builtin_amdgcn_raw_buffer_store_b128(vf, rs, OFF_KV + ((hq * NTT + tt) * 4 + 2) * 1024 + lane * 16, 0, 16);
    } else {
      W4(0, aq, 0); W4(1024, aq, 4); W4(2048, aq, 8); W4(3072, aq, 12); W4(4096, av, 0); W4(5120, av, 4);
      BAR_LGKM();
      R4(0, ak, 0); R4(1024, ak, 4); R4(2048, ak, 8); R4(3072, ak, 12); R4(4096, av, 8); R4(5120, av, 12);
      u32x4 f0 = pack8(ak, 0), f1 = pack8(ak, 1), vf = pack8(av, 1);
      u32x4 f1z = f1;
      if (h == 0) { f1z[2] = 0; f1z[3] = 0; f1[2] = 0x3f803f80u; f1[3] = 0x00003f80u; }
#pragma unroll
      for (int i = 0; i < 4; ++i) vf[i] = vpad ? vconst : vf[i];
      const unsigned mo = OFF_KV + (hq * NTT + tt) * 4096 + lane * 16;
      __builtin_amdgcn_raw_buffer_store_b128(f0, rs, mo, 0, 16);
      __builtin_amdgcn_raw_buffer_store_b128(f1, rs, mo + 1024, 0, 16);
      __builtin_amdgcn_raw_buffer_store_b128(vf, rs, mo + 3072, 0, 16);
      float kn = 0.f;
#pragma unroll
      for (int i = 0; i < 4; ++i) { kn = dot2bf(f0[i], f0[i], kn); kn = dot2bf(f1z[i], f1z[i], kn); }
      kn = xhalf_add(kn);
      asm volatile("ds_max_u32 %0, %1" :: "v"(lds0 + L_KMX), "v"(__float_as_uint(kn)) : "memory");
    }
    BAR_ALL();
    if (t == 0) put_word(ws + OFF_FKV + (hq * 16 + rt) * 8, tag, *(const LAS unsigned*)(L + L_KMX));
  }
  STAMP(6);
  const int head = xg * 2 + (rk >> 4), qt = rk & 15;
  const int tg = xg * 4 + (rk >> 3), sl = rk & 7;
  const int qb = wv & 3, kh = wv >> 2;
  const int tb = wv >> 2, kq = wv & 3;
  const int tt_own = tg * 2 + tb;
  const unsigned l16 = lane * 16;
  const unsigned o_off = OFF_O + ((qt * 4 + qb) * 32 + head * 2 + kh) * 1024 + l16;
  const unsigned y_off = OFF_Y + (tt_own * 28 + sl * 4 + kq) * 1024 + l16;
  const unsigned st_off = OFF_ST + ((tt_own * 28 + sl * 4 + kq) * 32 + r) * 8;
  const unsigned h_off = OFF_H + ((tg * 2) * 112 + (sl * 7 + wv) * 2) * 1024 + l16;
  const u32x4 ones4 = {~0u, ~0u, ~0u, ~0u};
  const u32x2 ones2 = {~0u, ~0u};
#define MARK(j) do { \
    if ((j) == 0) __builtin_amdgcn_raw_buffer_store_b128(ones4, rs, o_off, 0, 16); \
    if ((j) == 1) __builtin_amdgcn_raw_buffer_store_b128(ones4, rs, sl < 7 ? y_off : o_off, 0, 16); \
    if ((j) == 2) __builtin_amdgcn_raw_buffer_store_b64(ones2, rs, sl < 7 ? st_off : o_off, 0, 16); \
    if ((j) == 3) __builtin_amdgcn_raw_buffer_store_b128(ones4, rs, wv < 7 ? h_off : o_off, 0, 16); \
    if ((j) == 4) __builtin_amdgcn_raw_buffer_store_b128(ones4, rs, wv < 7 ? h_off + 1024 : o_off, 0, 16); \
    if ((j) == 5) __builtin_amdgcn_raw_buffer_store_b128(ones4, rs, wv < 7 ? h_off + 112 * 1024 : o_off, 0, 16); \
    if ((j) == 6) __builtin_amdgcn_raw_buffer_store_b128(ones4, rs, wv < 7 ? h_off + 113 * 1024 : o_off, 0, 16); } while (0)
  unsigned w1vo[4];
#pragma unroll
  for (int i = 0; i < 4; ++i) { const int pc = (wv + 8 * i < 28) ? wv + 8 * i : wv; const int q = pc * 64 + lane, row = q / 28, c = q - row * 28; w1vo[i] = row * (FF * 2) + c * 16; }
  const u16* w1b = w1 + sl * 224;
  const lds_cptr w1rd = L + L_LOW + (4 * h + tq) * 448 + wv * 64 + g16 * 32 + tp * 8;
  const unsigned w1d3 = (wv < 4) ? 24576 : 0;
#define W1D1(c, j) do { if ((c) >= 0) dma1(w1b + (size_t)(c) * 64 * FF, w1vo[j], lds0 + L_LOW + ((c) & 1) * 28672 + wv * 1024 + ((j) < 3 ? (j) * 8192 : w1d3)); } while (0)
#define W1_DMA(c) do { W1D1(c, 0); W1D1(c, 1); W1D1(c, 2); W1D1(c, 3); } while (0)
  if (wv == 7) {
    float km = __uint_as_float(wait_words(ws + OFF_FKV + head * 128, 16, tag, lane));
#pragma unroll
    for (int m = 8; m >= 1; m >>= 1) km = fmaxf(km, __shfl_xor(km, m));
    if (lane == 0) *(LAS float*)(L + L_SM) = km;
  } else {
    const u64 t0 = __builtin_amdgcn_s_memrealtime();
    u32x4 v0, v1;
    for (;;) {
      v0 = __builtin_amdgcn_raw_buffer_load_b128(rs, OFF_MODG + 7168 + t * 16, 0, 16);
      v1 = __builtin_amdgcn_raw_buffer_load_b128(rs, OFF_MODG + 14336 + t * 16, 0, 16);
      if (__all(v0[1] == tag && v0[3] == tag && v1[1] == tag && v1[3] == tag)) break;
      if (__builtin_amdgcn_s_memrealtime() - t0 > 2000000ull) break;
      __builtin_amdgcn_s_sleep(1);
    }
    W1_DMA(0); W1_DMA(1);
    const int e = (t < 224) ? 2 * t : 2 * t - DM;
    const int pk = (e >> 4) * 32 + ((e >> 2) & 1) * 16 + ((e >> 3) & 1) * 8 + (e & 3) * 2;
    if (t < 224) {
      f32x2 a = {__uint_as_float(v0[0]), __uint_as_float(v0[2])};
      f32x2 g = {rbf(1.f + __uint_as_float(v1[0])), rbf(1.f + __uint_as_float(v1[2]))};
      *(LAS f32x2*)(L + L_GA + e * 4) = a;
      *(LAS f32x2*)(L + L_G2 + e * 4) = g;
      *(LAS unsigned*)(L + L_PK + pk) = pack2(g[0], g[1]);
    } else {
      f32x2 g = {__uint_as_float(v1[0]), __uint_as_float(v1[2])};
      *(LAS f32x2*)(L + L_GBV + e * 4) = g;
      *(LAS unsigned*)(L + L_PK + 896 + pk) = pack2(__uint_as_float(v0[0]), __uint_as_float(v0[2]));
    }
  }
  BAR_LGKM();
  STAMP(7);
  const char* kvsrc = ws + OFF_KV + (size_t)(head * NTT + kh * 32 + qb) * 4096;
  const unsigned kvdst = lds0 + L_HI + kh * 16384 + qb * 4096;
#define DMA_KV(i, buf) dma4c(kvsrc + (size_t)(i) * 16384, l16, kvdst + (buf))
  bf16x8 qf0, qf1;
  {
    u32x4 q0, q1;
    asm volatile("global_load_dwordx4 %0, %2, %3 sc1\n\tglobal_load_dwordx4 %1, %2, %3 offset:1024 sc1" : "=&v"(q0), "=&v"(q1)
                 : "v"(l16), "s"(ws + OFF_Q + (head * NTT + qt * 4 + qb) * 2048) : "memory");
    DMA_KV(0, 0);
    if (wv == 7) { W1_DMA(0); W1_DMA(1); }
    DMA_KV(1, 32768);
    const float km = *(const LAS float*)(L + L_SM);
    asm volatile("s_waitcnt vmcnt(8)" : "+v"(q0), "+v"(q1) :: "memory");
    float qn = 0.f;
#pragma unroll
    for (int i = 0; i < 4; ++i) { qn = dot2bf(q0[i], q0[i], qn); qn = dot2bf(q1[i], q1[i], qn); }
    qn = xhalf_add(qn);
    const float bound = __builtin_amdgcn_sqrtf(qn * km) * 1.004f + 1e-30f;
    if (h == 0) q1[2] = ((__float_as_uint(bound) + 0xffffu) >> 16) | 0x8000u;
    qf0 = BC8(q0); qf1 = BC8(q1);
  }
  bf16x8 wf[28];
  f32x16 o, sA, sB;
  u32x4 pw0, pw1;
  bf16x8 kf0, kf1, vf0, vf1;
  float e12, e13, e14, e15;
  const lds_cptr abase = L + L_HI + kh * 16384 + l16;
  BAR_VM(4);
  STAMP(8);
  ATTN_PROLOGUE();
  const unsigned rvo = (lane >> 3) * (DM * 2) + (((lane & 7) ^ (((lane >> 4) & 1) << 2)) * 16);
  const int rsw = ((tq >> 1) & 1) * 64;
  const u16* wob = wo + (sl < 7 ? sl : 0) * 64;
  const int wo3 = (wv < 4) ? wv + 24 : wv;
#define W1_RD1(c, i_) (wf[4 * (c) + (i_)] = trfrag(w1rd + ((c) & 1) * 28672 + (i_) * 16 * 448, w1rd + ((c) & 1) * 28672 + ((i_) * 16 + 8) * 448))
#define W1_READ(c) do { W1_RD1(c, 0); W1_RD1(c, 1); W1_RD1(c, 2); W1_RD1(c, 3); } while (0)
#define KV1(st, j) do { if ((st) < 8) dma1c(kvsrc + (size_t)(st) * 16384 + (j) * 1024, l16, kvdst + ((st) % 3) * 32768 + (j) * 1024); } while (0)
#define W1R1(c, j) do { if ((c) >= 0) W1_RD1(((c) >= 0 ? (c) : 0), j); } while (0)
#define WO1(hf, j) do { if ((hf) >= 0) { const int pc_ = (j) < 3 ? wv + 8 * (j) : wo3; \
      dma1(wob + ((hf) * 28 + pc_) * 8 * DM, rvo, lds0 + L_HI + ((hf) ? 0 : 65536) + pc_ * 1024); } } while (0)
#define MK(on, j) do { if (on) MARK(j); } while (0)
#define STAGE(st, NW, RA, RB, RC, DC, WH, TCH) do { \
    constexpr int cur = ((st) % 3) * 32768, nxt = (((st) + 1) % 3) * 32768; \
    const lds_cptr bc = abase + cur, bn = abase + nxt; \
    ASTEPH(sA, sB, bc + 2 * 4096, bc + 0 * 4096 + 3072, bc + 1 * 4096 + 2048, W1R1(RA, 0), W1R1(RA, 1), W1R1(RA, 2), W1R1(RA, 3)); \
    ASTEPH(sB, sA, bc + 3 * 4096, bc + 1 * 4096 + 3072, bc + 2 * 4096 + 2048, W1R1(RB, 0), W1R1(RB, 1), W1R1(RB, 2), W1R1(RB, 3)); \
    BAR_VM(NW); \
    ASTEPH(sA, sB, bn + 0 * 4096, bc + 2 * 4096 + 3072, bc + 3 * 4096 + 2048, KV1((st) + 2, 0); WO1(WH, 0); W1R1(RC, 0), KV1((st) + 2, 1); WO1(WH, 1); W1R1(RC, 1), \
           KV1((st) + 2, 2); WO1(WH, 2); W1R1(RC, 2), KV1((st) + 2, 3); WO1(WH, 3); W1R1(RC, 3)); \
    ASTEPH(sB, sA, bn + 1 * 4096, bc + 3 * 4096 + 3072, bn + 0 * 4096 + 2048, W1D1(DC, 0), W1D1(DC, 1), W1D1(DC, 2), W1D1(DC, 3); TCH); \
  } while (0)
  const int tl = wv * 9 + (lane < 9 ? lane : 8);
#define TOUCH_WO(i) touch1(wo, (rk * 98 + ((i) * 72 + tl < 98 ? (i) * 72 + tl : 97)) * 128, lds0 + L_SM + 256)
#define TOUCH_W2(i) touch1(w2, (rk * 392 + ((i) * 72 + tl < 392 ? (i) * 72 + tl : 391)) * 128, lds0 + L_SM + 256)
  u32x2 pfl;
  const unsigned pfo = (lane & 16) ? OFF_FY + tg * 128 + (lane & 7) * 8 : OFF_FO + (tg >> 1) * 128 + (lane & 15) * 8;
#define PF_LOAD() asm volatile("global_load_dwordx2 %0, %1, %2 sc1" : "=v"(pfl) : "v"(pfo), "s"(ws) : "memory")
  STAGE(0, 0,  0,  1, -1,  2, -1, TOUCH_WO(0));
  STAGE(1, 5, -1, -1, -1,  3, -1, TOUCH_WO(1); TOUCH_W2(0));
  STAGE(2, 6, -1, -1,  2, -1, -1, TOUCH_W2(1));
  STAGE(3, 1, -1, -1,  3,  4, -1, TOUCH_W2(2));
  STAGE(4, 5, -1, -1, -1,  5, -1, TOUCH_W2(3));
  STAGE(5, 5, -1, -1,  4, -1, -1, TOUCH_W2(4); TOUCH_W2(5));
  STAGE(6, 2, -1, -1,  5,  6,  0, NOH);
  STAGE(7, 8, -1, -1, -1, -1,  1, NOH);
  STAMP(9);
  float fin[8];
  float lsum;
  constexpr int L_X = L_LOW + 28672;
#pragma unroll 1
  for (int pass = 0; pass < 2; ++pass) {
    pw1[2] = pack2(e12, e13); pw1[3] = pack2(e14, e15);
    o = MFMA32(vf1, BC8(pw1), o);
    {
      f32x4 a, b2;
      if (kh) {
#pragma unroll
        for (int i = 0; i < 4; ++i) { a[i] = o[i]; b2[i] = o[4 + i]; fin[i] = o[8 + i]; fin[4 + i] = o[12 + i]; }
      } else {
#pragma unroll
        for (int i = 0; i < 4; ++i) { a[i] = o[8 + i]; b2[i] = o[12 + i]; fin[i] = o[i]; fin[4 + i] = o[4 + i]; }
      }
      *(LAS f32x4*)(L + L_X + (wv * 2 + 0) * 1024 + l16) = a;
      *(LAS f32x4*)(L + L_X + (wv * 2 + 1) * 1024 + l16) = b2;
      if (h == 0) *(LAS float*)(L + L_X + 16384 + (wv * 32 + r) * 4) = o[12];
    }
    BAR_LGKM();
    {
      const int pw = wv ^ 4;
      const f32x4 a = *(const LAS f32x4*)(L + L_X + (pw * 2 + 0) * 1024 + l16);
      const f32x4 b2 = *(const LAS f32x4*)(L + L_X + (pw * 2 + 1) * 1024 + l16);
      const float c0 = *(const LAS float*)(L + L_X + 16384 + lane * 4) + *(const LAS float*)(L + L_X + 16384 + (128 + lane) * 4);
      const float c1 = *(const LAS float*)(L + L_X + 16384 + (64 + lane) * 4) + *(const LAS float*)(L + L_X + 16384 + (192 + lane) * 4);
#pragma unroll
      for (int i = 0; i < 4; ++i) { fin[i] += a[i]; fin[4 + i] += b2[i]; }
      lsum = *(const LAS float*)(L + L_X + 16384 + (qb * 32 + r) * 4) + *(const LAS float*)(L + L_X + 16384 + ((qb + 4) * 32 + r) * 4);
      const bool ok = (c0 >= 9.094947e-13f) && (c1 >= 9.094947e-13f);
      if (pass == 1 || __all(ok)) break;
    }
    {
      u32x4 q1 = __builtin_bit_cast(u32x4, qf1);
      if (h == 0) { q1[2] = 0; q1[3] = 0; }
      qf1 = BC8(q1);
      float mx = -INFINITY;
      const unsigned ko = OFF_KV + (head * NTT + kh * 32) * 4096 + l16;
#pragma unroll 1
      for (int kt = 0; kt < 32; ++kt) {
        const u32x4 k0 = __builtin_amdgcn_raw_buffer_load_b128(rs, ko + kt * 4096, 0, 16);
        const u32x4 k1 = __builtin_amdgcn_raw_buffer_load_b128(rs, ko + kt * 4096 + 1024, 0, 16);
        f32x16 s = MFMA32(BC8(k0), qf0, zero16);
        s = MFMA32(BC8(k1), qf1, s);
#pragma unroll
        for (int i = 0; i < 16; ++i) mx = fmaxf(mx, s[i]);
      }
      mx = xhalf_max(mx);
      if (h == 0) *(LAS float*)(L + L_X + 20480 + (wv * 32 + r) * 4) = mx;
      BAR_ALL();
      mx = fmaxf(*(const LAS float*)(L + L_X + 20480 + (qb * 32 + r) * 4), *(const LAS float*)(L + L_X + 20480 + ((qb + 4) * 32 + r) * 4));
      const float m0 = rbf(mx), m1 = rbf(mx - m0), m2 = rbf(mx - m0 - m1);
      if (h == 0) { q1[2] = pack2(-m0, -m1); q1[3] = pack2(-m2, 0.f); }
      qf1 = BC8(q1);
      DMA_KV(0, 0);
      DMA_KV(1, 32768);
      asm volatile("s_waitcnt vmcnt(4) lgkmcnt(0)\n\ts_barrier" ::: "memory");
      ATTN_PROLOGUE();
      int cur = 0, nxt = 32768, nn = 65536;
#pragma unroll 1
      for (int st = 0; st < 8; ++st) {
        const lds_cptr bc = abase + cur, bn = abase + nxt;
        ASTEP(sA, sB, bc + 2 * 4096, bc + 0 * 4096 + 3072, bc + 1 * 4096 + 2048);
        ASTEP(sB, sA, bc + 3 * 4096, bc + 1 * 4096 + 3072, bc + 2 * 4096 + 2048);
        BAR_VM(0);
        if (st + 2 < 8) DMA_KV(st + 2, nn);
        if (st == 7) { WO1(0, 0); WO1(0, 1); WO1(0, 2); WO1(0, 3); WO1(1, 0); WO1(1, 1); WO1(1, 2); WO1(1, 3); }
        ASTEP(sA, sB, bn + 0 * 4096, bc + 2 * 4096 + 3072, bc + 3 * 4096 + 2048);
        ASTEP(sB, sA, bn + 1 * 4096, bc + 3 * 4096 + 3072, bn + 0 * 4096 + 2048);
        const int tmp = cur; cur = nxt; nxt = nn; nn = tmp;
      }
    }
  }
  {
    const float inv = __builtin_amdgcn_rcpf(lsum);
    u32x4 ov;
#pragma unroll
    for (int i = 0; i < 4; ++i) ov[i] = pack2(fin[2 * i] * inv, fin[2 * i + 1] * inv);
    if (kh == 1 && h == 0) { ov[2] = 0; ov[3] = 0; }
    __builtin_amdgcn_raw_buffer_store_b128(ov, rs, o_off, 0, 16);
    BAR_ALL();
    if (t == 0) put_word(ws + OFF_FO + (qt * 16 + head) * 8, tag, 0);
  }
  STAMP(10);
  const int n0 = (sl * 2 + (kq >> 1)) * 32 + (kq & 1) * 16 + 4 * h;
  u32x2 xa2, xb2;
  unsigned xc0;
  asm volatile("global_load_dwordx2 %0, %3, %5\n\tglobal_load_dwordx2 %1, %3, %5 offset:16\n\tglobal_load_ushort %2, %4, %5"
               : "=&v"(xa2), "=&v"(xb2), "=&v"(xc0) : "v"((r * DM + (sl < 7 ? n0 : 0)) * 2), "v"(r * DM * 2), "s"(x + (size_t)tt_own * 32 * DM) : "memory");
  W1_READ(6);
  f32x16 uv = zero16;
  if (wv < 7) {
    const lds_cptr pp = L + L_PK + g16 * 896 + h * 16;
    bf16x8 pf[14];
#pragma unroll
    for (int hf2 = 0; hf2 < 2; ++hf2) {
#pragma unroll
      for (int i = 0; i < 14; ++i) pf[i] = ldsfrag(pp + (hf2 * 14 + i) * 32);
      SBAR();
#pragma unroll
      for (int i = 0; i < 14; ++i) uv = MFMA32(wf[hf2 * 14 + i], pf[i], uv);
      PIN(uv); SBAR();
    }
  }
  STAMP(11);
  if (wv == 7 && sl < 7) wait_words(ws + OFF_FO + (tg >> 1) * 128, 16, tag, lane);
  asm volatile("s_waitcnt vmcnt(0) lgkmcnt(0)\n\ts_barrier" : "+v"(xa2), "+v"(xb2), "+v"(xc0) :: "memory");
#define MAX4(m, v) do { m = __builtin_elementwise_max(__builtin_elementwise_max(m, v[0]), v[1]); m = __builtin_elementwise_max(__builtin_elementwise_max(m, v[2]), v[3]); } while (0)
  const lds_ptr redb = L + L_LOW + tb * 24576 + l16;
#define RW1(A, o8, p, KQ) do { f32x4 v0 = {A[o8], A[(o8) + 1], A[(o8) + 2], A[(o8) + 3]}, v1 = {A[(o8) + 4], A[(o8) + 5], A[(o8) + 6], A[(o8) + 7]}; \
        *(LAS f32x4*)(redb + (((p) * 3 + (((KQ) - (p) - 1) & 3)) * 2) * 1024) = v0; *(LAS f32x4*)(redb + (((p) * 3 + (((KQ) - (p) - 1) & 3)) * 2 + 1) * 1024) = v1; } while (0)
#define RKEEP(AT, A, o8) do { _Pragma("unroll") for (int i = 0; i < 8; ++i) AT[i] = A[(o8) + i]; } while (0)
#define RED_WRITE(AT, A0, A1) do { \
      if (kq == 0)      { RW1(A0, 8, 1, 0); RW1(A1, 0, 2, 0); RW1(A1, 8, 3, 0); RKEEP(AT, A0, 0); } \
      else if (kq == 1) { RW1(A0, 0, 0, 1); RW1(A1, 0, 2, 1); RW1(A1, 8, 3, 1); RKEEP(AT, A0, 8); } \
      else if (kq == 2) { RW1(A0, 0, 0, 2); RW1(A0, 8, 1, 2); RW1(A1, 8, 3, 2); RKEEP(AT, A1, 0); } \
      else              { RW1(A0, 0, 0, 3); RW1(A0, 8, 1, 3); RW1(A1, 0, 2, 3); RKEEP(AT, A1, 8); } } while (0)
#define RED_READ(AT) do { \
      _Pragma("unroll") for (int d = 0; d < 3; ++d) { \
        const f32x4 v0 = *(const LAS f32x4*)(redb + kq * 6144 + (d * 2) * 1024); \
        const f32x4 v1 = *(const LAS f32x4*)(redb + kq * 6144 + (d * 2 + 1) * 1024); \
        _Pragma("unroll") for (int i = 0; i < 4; ++i) { AT[i] += v0[i]; AT[4 + i] += v1[i]; } } } while (0)
  const u16* w2b = w2 + (sl < 7 ? sl : 0) * 64;
#define W2A(i) dma1(w2b + (size_t)((wv >> 1) * 448 + ((wv & 1) * 12 + (i)) * 8) * DM, rvo, lds0 + L_HI + (wv * 12 + (i)) * 1024)
  u32x4 x1p;
  if (sl < 7) {
    float x1[8];
    STAMP(12);
    f32x16 acc0 = zero16, acc1 = zero16;
    {
      const lds_cptr wop = L + L_HI + ((kq >> 1) ? 0 : 65536) + ((kq & 1) * 4 * DH + 4 * h + tq) * 128 + g16 * 32 + tp * 8;
      const lds_cptr p0 = wop + rsw, p1 = wop + (64 ^ rsw);
      bf16x8 wa0[4], wa1[4];
#define WOROW0(i) (((i) >> 1) * DH + 16 * ((i) & 1))
#define WOROW1(i) (((i) >> 1) * DH + (((i) & 1) ? 20 : 8))
      u32x4 ob[8];
      const unsigned oo = OFF_O + (tt_own * 32 + kq * 8) * 1024 + l16;
#pragma unroll
      for (int i = 0; i < 8; ++i) ob[i] = __builtin_amdgcn_raw_buffer_load_b128(rs, oo + i * 1024, 0, 16);
      SBAR();
#pragma unroll
      for (int i = 0; i < 4; ++i) {
        wa0[i] = trfrag(p0 + WOROW0(i) * 128, p0 + WOROW1(i) * 128);
        wa1[i] = trfrag(p1 + WOROW0(i) * 128, p1 + WOROW1(i) * 128);
      }
      SBAR();
#pragma unroll
      for (int i = 0; i < 8; ++i) {
        acc0 = MFMA32(wa0[i & 3], BC8(ob[i]), acc0); acc1 = MFMA32(wa1[i & 3], BC8(ob[i]), acc1); PIN(acc0); PIN(acc1); SBAR();
        if (i < 4) {
          wa0[i] = trfrag(p0 + WOROW0(i + 4) * 128, p0 + WOROW1(i + 4) * 128);
          wa1[i] = trfrag(p1 + WOROW0(i + 4) * 128, p1 + WOROW1(i + 4) * 128);
        }
        SBAR();
      }
    }
    STAMP(13);
    float at[8];
    RED_WRITE(at, acc0, acc1);
    const f32x4 ga0 = *(const LAS f32x4*)(L + L_GA + n0 * 4), ga1 = *(const LAS f32x4*)(L + L_GA + (n0 + 8) * 4);
    const f32x4 g20 = *(const LAS f32x4*)(L + L_G2 + n0 * 4), g21 = *(const LAS f32x4*)(L + L_G2 + (n0 + 8) * 4);
    BAR_LGKM();
    RED_READ(at);
    x1[0] = bflo(xa2[0]) + ga0[0] * at[0]; x1[1] = bfhi(xa2[0]) + ga0[1] * at[1];
    x1[2] = bflo(xa2[1]) + ga0[2] * at[2]; x1[3] = bfhi(xa2[1]) + ga0[3] * at[3];
    x1[4] = bflo(xb2[0]) + ga1[0] * at[4]; x1[5] = bfhi(xb2[0]) + ga1[1] * at[5];
    x1[6] = bflo(xb2[1]) + ga1[2] * at[6]; x1[7] = bfhi(xb2[1]) + ga1[3] * at[7];
#pragma unroll
    for (int i = 0; i < 4; ++i) x1p[i] = pack2(x1[2 * i], x1[2 * i + 1]);
    PIN(x1p);
    const float cshift = __uint_as_float(xc0 << 16);
    float p1 = 0.f, p2 = 0.f, y[8];
#pragma unroll
    for (int i = 0; i < 8; ++i) {
      const float d = x1[i] - cshift;
      p1 += d; p2 += d * d;
      y[i] = d * (i < 4 ? g20[i & 3] : g21[i & 3]);
    }
    u32x4 yv;
#pragma unroll
    for (int i = 0; i < 4; ++i) yv[i] = pack2(y[2 * i], y[2 * i + 1]);
    __builtin_amdgcn_raw_buffer_store_b128(yv, rs, y_off, 0, 16);
    p1 = xhalf_add(p1); p2 = xhalf_add(p2);
    {
      u32x2 sv = {__float_as_uint(p1), __float_as_uint(p2)};
      __builtin_amdgcn_raw_buffer_store_b64(sv, rs, st_off, 0, 16);
    }
    BAR_ALL();
    if (t == 0) put_word(ws + OFF_FY + tg * 128 + sl * 8, tag, 0);
    if (b == 0 && t == 64) __hip_atomic_store((gu64*)ws, epoch + 1ull, RLX_AGENT);
    if (wv < 7) { W2A(0); W2A(1); W2A(2); W2A(3); W2A(4); W2A(5); W2A(6); W2A(7); W2A(8); W2A(9); W2A(10); W2A(11); }
  }
  STAMP(14);
  f32x16 u2, v2;
#pragma unroll
  for (int i = 0; i < 16; ++i) {
    auto rr = __builtin_amdgcn_permlane16_swap(__float_as_uint(uv[i]), __float_as_uint(uv[i]), false, false);
    u2[i] = __uint_as_float(rr[0]); v2[i] = __uint_as_float(rr[1]) * -1.4426950408889634f;
  }
  PIN(u2); PIN(v2);
  if (wv == 7) wait_words(ws + OFF_FY + tg * 128, 7, tag, lane);
  BAR_LGKM();
  STAMP(15);
  {
    if (wv < 7) {
      const char* ysrc = ws + OFF_Y + (size_t)tg * 57344 + wv * 8192;
      dma4c(ysrc, l16, lds0 + L_LOW + wv * 8192); dma4c(ysrc + 4096, l16, lds0 + L_LOW + wv * 8192 + 4096);
    } else {
      u32x2 sv[28];
#pragma unroll
      for (int i = 0; i < 28; ++i) sv[i] = __builtin_amdgcn_raw_buffer_load_b64(rs, OFF_ST + (((tg * 2 + h) * 28 + i) * 32 + r) * 8, 0, 16);
      float a1 = 0.f, a2 = 0.f;
#pragma unroll
      for (int i = 0; i < 28; ++i) { a1 += __uint_as_float(sv[i][0]); a2 += __uint_as_float(sv[i][1]); }
      const float m = a1 * (1.f / DM);
      const float var = fmaxf(a2 * (1.f / DM) - m * m, 0.f);
      f32x2 o2 = {__builtin_amdgcn_rsqf(var + 1e-5f) * -1.4426950408889634f, m};
      *(LAS f32x2*)(L + L_SM + 512 + lane * 8) = o2;
    }
  }
  BAR_ALL();
  STAMP(16);
  f32x16 c0 = zero16, c1 = zero16;
  f32x2 st0, st1;
  u32x4 gbp;
#define SILU1(C, ST, i) do { const float t_ = __builtin_fmaf(-ST[1], u2[i], C[i]); const float q_ = __builtin_fmaf(ST[0], t_, v2[i]); \
      C[i] = q_ * __builtin_amdgcn_rcpf(1.f + EX2(q_)); } while (0)
  {
    const f32x4 gb0 = *(const LAS f32x4*)(L + L_GBV + (sl < 7 ? n0 : 0) * 4), gb1 = *(const LAS f32x4*)(L + L_GBV + ((sl < 7 ? n0 : 0) + 8) * 4);
    gbp[0] = pack2(gb0[0], gb0[1]); gbp[1] = pack2(gb0[2], gb0[3]); gbp[2] = pack2(gb1[0], gb1[1]); gbp[3] = pack2(gb1[2], gb1[3]); PIN(gbp);
  }
  const int rowoff = (4 * h + tq) * 128 + g16 * 32 + tp * 8;
  const lds_cptr hi0 = L + L_HI + kq * 24576 + rowoff + rsw, hi1 = L + L_HI + kq * 24576 + rowoff + (64 ^ rsw);
  bf16x8 r0[12], r1[12];
  {
    const lds_cptr yp = L + L_LOW + l16;
    st0 = *(const LAS f32x2*)(L + L_SM + 512 + r * 8); st1 = *(const LAS f32x2*)(L + L_SM + 512 + (32 + r) * 8);
    bf16x8 yb[4];
    if (wv < 7) {
#pragma unroll
      for (int i = 0; i < 4; ++i) yb[i] = ldsfrag(yp + i * 1024);
      SBAR();
#pragma unroll
      for (int ks = 0; ks < 28; ++ks) {
        c0 = MFMA32(wf[ks], yb[ks & 3], c0); PIN(c0); SBAR();
        yb[ks & 3] = ldsfrag(yp + (ks + 4) * 1024);
        SBAR();
      }
    } else { W2A(0); W2A(1); W2A(2); W2A(3); W2A(4); W2A(5); W2A(6); W2A(7); W2A(8); W2A(9); W2A(10); W2A(11); }
    BAR_VM(0);
    if (wv < 7) {
#pragma unroll
      for (int ks = 0; ks < 28; ++ks) {
        c1 = MFMA32(wf[ks], yb[ks & 3], c1); PIN(c1); SBAR();
        if (ks + 4 < 28) yb[ks & 3] = ldsfrag(yp + (28 + ks + 4) * 1024);
        if (ks >= 1 && ks < 17) { SILU1(c0, st0, ks - 1); PIN(c0); }
        if (ks == 18) __builtin_amdgcn_raw_buffer_store_b128(pack8(c0, 0), rs, h_off, 0, 16);
        if (ks == 19) __builtin_amdgcn_raw_buffer_store_b128(pack8(c0, 1), rs, h_off + 1024, 0, 16);
        if (ks >= 4) { const int j = (ks - 4) >> 1; if (ks & 1) r1[j] = trfrag(hi1 + j * 2048, hi1 + j * 2048 + 1024); else r0[j] = trfrag(hi0 + j * 2048, hi0 + j * 2048 + 1024); }
        SBAR();
      }
    } else {
#pragma unroll
      for (int j = 0; j < 12; ++j) { r0[j] = trfrag(hi0 + j * 2048, hi0 + j * 2048 + 1024); r1[j] = trfrag(hi1 + j * 2048, hi1 + j * 2048 + 1024); }
    }
  }
  BAR_ALL();
  if (t == 0) put_word(ws + OFF_FH + tg * 128 + sl * 8, tag, 0);
  STAMP(17);
  if (wv < 7) {
#pragma unroll
    for (int i = 0; i < 8; ++i) SILU1(c1, st1, i);
    __builtin_amdgcn_raw_buffer_store_b128(pack8(c1, 0), rs, h_off + 112 * 1024, 0, 16);
#pragma unroll
    for (int i = 8; i < 16; ++i) SILU1(c1, st1, i);
    __builtin_amdgcn_raw_buffer_store_b128(pack8(c1, 1), rs, h_off + 113 * 1024, 0, 16);
  }
  BAR_ALL();
  if (t == 0) put_word(ws + OFF_FH + tg * 128 + 64 + sl * 8, tag, 0);
  STAMP(18);
  if (sl == 7) { STAMP_DUMP(); return; }
  {
#pragma unroll
    for (int i = 0; i < 16; ++i)
      dma1(w2b + (size_t)((wv >> 1) * 448 + 192 + ((wv & 1) * 16 + i) * 8) * DM, rvo, lds0 + (wv * 16 + i) * 1024);
    STAMP(19);
    const unsigned ho = OFF_H + (tt_own * 112 + kq * 28) * 1024;
    f32x16 d0, d1;
    const lds_cptr lo0 = L + kq * 32768 + rowoff + rsw, lo1 = L + kq * 32768 + rowoff + (64 ^ rsw);
    if (tb) BAR_VM(0);
    wait_words(ws + OFF_FH + tg * 128 + tb * 64, 8, tag, lane);
    STAMP(20);
    u32x4 hb[28];
#pragma unroll
    for (int i = 0; i < 18; ++i) hb[i] = __builtin_amdgcn_raw_buffer_load_b128(rs, ho + l16 + i * 1024, 0, 16);
    SBAR();
    d0 = zero16; d1 = zero16;
#pragma unroll
    for (int i = 0; i < 12; ++i) {
      d0 = MFMA32(r0[i], BC8(hb[i]), d0); PIN(d0); SBAR();
      d1 = MFMA32(r1[i], BC8(hb[i]), d1); PIN(d1); SBAR();
      if (i >= 1 && i < 11) hb[17 + i] = __builtin_amdgcn_raw_buffer_load_b128(rs, ho + l16 + (17 + i) * 1024, 0, 16);
      SBAR();
    }
    if (!tb) asm volatile("s_barrier" ::: "memory");
    {
      bf16x8 a0[4], a1[4];
#pragma unroll
      for (int i = 0; i < 4; ++i) { a0[i] = trfrag(lo0 + i * 2048, lo0 + i * 2048 + 1024); a1[i] = trfrag(lo1 + i * 2048, lo1 + i * 2048 + 1024); }
      SBAR();
#pragma unroll
      for (int i = 0; i < 16; ++i) {
        d0 = MFMA32(a0[i & 3], BC8(hb[12 + i]), d0); PIN(d0); SBAR();
        if (i + 4 < 16) a0[i & 3] = trfrag(lo0 + (i + 4) * 2048, lo0 + (i + 4) * 2048 + 1024);
        SBAR();
        d1 = MFMA32(a1[i & 3], BC8(hb[12 + i]), d1); PIN(d1); SBAR();
        if (i + 4 < 16) a1[i & 3] = trfrag(lo1 + (i + 4) * 2048, lo1 + (i + 4) * 2048 + 1024);
        SBAR();
      }
    }
    STAMP(21);
    BAR_LGKM();
    float f[8];
    RED_WRITE(f, d0, d1);
    BAR_LGKM();
    RED_READ(f);
#pragma unroll
    for (int i = 0; i < 8; ++i) f[i] *= -0.6931471805599453f;
    u32x2 oa, ob2;
    oa[0] = pack2(bflo(x1p[0]) + bflo(gbp[0]) * f[0], bfhi(x1p[0]) + bfhi(gbp[0]) * f[1]);
    oa[1] = pack2(bflo(x1p[1]) + bflo(gbp[1]) * f[2], bfhi(x1p[1]) + bfhi(gbp[1]) * f[3]);
    ob2[0] = pack2(bflo(x1p[2]) + bflo(gbp[2]) * f[4], bfhi(x1p[2]) + bfhi(gbp[2]) * f[5]);
    ob2[1] = pack2(bflo(x1p[3]) + bflo(gbp[3]) * f[6], bfhi(x1p[3]) + bfhi(gbp[3]) * f[7]);
    u16* orow = out + (size_t)(tt_own * 32 + r) * DM;
    *(u32x2*)(orow + n0) = oa;
    *(u32x2*)(orow + n0 + 8) = ob2;
  }
  STAMP(22);
  STAMP_DUMP();
}

extern "C" void kernel_launch(void* const* d_in, const int* in_sizes, int n_in, void* d_out, int out_size, void* d_ws,
                              size_t ws_size, hipStream_t stream) {
  fused_block<<<256, 512, 0, stream>>>((const u16*)d_in[0], (const u16*)d_in[1], (const u16*)d_in[2], (const u16*)d_in[4],
                                       (const u16*)d_in[5], (const u16*)d_in[6], (const u16*)d_in[7], (u16*)d_out, (char*)d_ws);
}
```

```cpp
#include <hip/hip_runtime.h>
#include <hip/hip_bf16.h>
#include <stdint.h>

typedef __attribute__((ext_vector_type(8))) short bf16x8;
typedef __attribute__((ext_vector_type(4))) short s16x4;
typedef __attribute__((ext_vector_type(16))) float f32x16;
typedef __attribute__((ext_vector_type(4))) unsigned u32x4;
typedef __attribute__((ext_vector_type(2))) unsigned u32x2;
typedef __attribute__((ext_vector_type(4))) float f32x4;
typedef __attribute__((ext_vector_type(2))) float f32x2;
typedef __attribute__((ext_vector_type(2))) __bf16 bf16x2_t;
typedef __attribute__((ext_vector_type(2))) _Float16 h16x2;
typedef unsigned short u16;
typedef unsigned long long u64;
#define GAS __attribute__((address_space(1)))
#define LAS __attribute__((address_space(3)))
typedef GAS u64 gu64;
typedef LAS const char* lds_cptr;
typedef LAS char* lds_ptr;

#define DI __device__ __forceinline__
#define MFMA32(a, b, c) __builtin_amdgcn_mfma_f32_32x32x16_bf16((a), (b), (c), 0, 0, 0)
#define MFMA16(a, b, c) __builtin_amdgcn_mfma_f32_16x16x32_bf16((a), (b), (c), 0, 0, 0)
#define SBAR() __builtin_amdgcn_sched_barrier(0)
#define PIN(x) asm volatile("" : "+v"(x))
#define RLX_AGENT __ATOMIC_RELAXED, __HIP_MEMORY_SCOPE_AGENT
#define BC8(x) __builtin_bit_cast(bf16x8, x)
#define EX2(v) __builtin_amdgcn_exp2f(v)
#define BAR_LGKM() asm volatile("s_waitcnt lgkmcnt(0)\n\ts_barrier" ::: "memory")
#define BAR_ALL() asm volatile("s_waitcnt vmcnt(0) lgkmcnt(0)\n\ts_barrier" ::: "memory")
#define BAR_VM(n) asm volatile("s_waitcnt vmcnt(" #n ")\n\ts_barrier" ::: "memory")

constexpr int DM = 448, DH = 28, FF = 1792;
constexpr int NTT = 64;

constexpr unsigned OFF_MODG = 4096;
constexpr unsigned OFF_FKV = 32768;
constexpr unsigned OFF_FO = 36864;
constexpr unsigned OFF_FY = 40960;
constexpr unsigned OFF_FH = 49152;
constexpr unsigned OFF_ST = 65536;
constexpr unsigned OFF_Q = 1u << 20;
constexpr unsigned OFF_KV = 3u << 20;
constexpr unsigned OFF_O = 7u << 20;
constexpr unsigned OFF_Y = 9u << 20;
constexpr unsigned OFF_H = 11u << 20;
constexpr unsigned WS_USED = 19u << 20;

#define STAMP(i) do {} while (0)
#define STAMP_DECL() do {} while (0)
#define STAMP_DUMP() do {} while (0)

DI float bf2f(u16 v) { return __uint_as_float((unsigned)v << 16); }
DI float bflo(unsigned w) { return __uint_as_float(w << 16); }
DI float bfhi(unsigned w) { return __uint_as_float(w & 0xffff0000u); }
DI unsigned pack2(float lo, float hi) {
  f32x2 v = {lo, hi};
  bf16x2_t b = __builtin_convertvector(v, bf16x2_t);
  return __builtin_bit_cast(unsigned, b);
}
DI unsigned packh2(float lo, float hi) {
  f32x2 v = {lo, hi};
  h16x2 b = __builtin_convertvector(v, h16x2);
  return __builtin_bit_cast(unsigned, b);
}
DI float hlo(unsigned w) { return (float)__builtin_bit_cast(h16x2, w)[0]; }
DI float hhi(unsigned w) { return (float)__builtin_bit_cast(h16x2, w)[1]; }
DI float fmix_lo(unsigned w, float g, float b) { float d; asm("v_fma_mix_f32 %0, %1, %2, %3 op_sel_hi:[1,0,0]" : "=v"(d) : "v"(w), "v"(g), "v"(b)); return d; }
DI float fmix_hi(unsigned w, float g, float b) { float d; asm("v_fma_mix_f32 %0, %1, %2, %3 op_sel:[1,0,0] op_sel_hi:[1,0,0]" : "=v"(d) : "v"(w), "v"(g), "v"(b)); return d; }
DI float rbf(float x) { return bflo(pack2(x, x)); }
DI u32x4 pack8(const f32x16& x, int s) {
  u32x4 p;
  p[0] = pack2(x[8 * s + 0], x[8 * s + 1]);
  p[1] = pack2(x[8 * s + 2], x[8 * s + 3]);
  p[2] = pack2(x[8 * s + 4], x[8 * s + 5]);
  p[3] = pack2(x[8 * s + 6], x[8 * s + 7]);
  return p;
}
DI float xhalf_add(float v) {
  auto rr = __builtin_amdgcn_permlane32_swap(__float_as_uint(v), __float_as_uint(v), false, false);
  return __uint_as_float(rr[0]) + __uint_as_float(rr[1]);
}
DI float xhalf_max(float v) {
  auto rr = __builtin_amdgcn_permlane32_swap(__float_as_uint(v), __float_as_uint(v), false, false);
  return fmaxf(__uint_as_float(rr[0]), __uint_as_float(rr[1]));
}
DI void dma1(const void* sbase, unsigned voff, unsigned lds_base) {
  unsigned sv;
  asm volatile("s_mov_b32 %0, m0\n\ts_mov_b32 m0, %2\n\ts_nop 0\n\tglobal_load_lds_dwordx4 %1, %3\n\ts_mov_b32 m0, %0"
               : "=&s"(sv) : "v"(voff), "s"(lds_base), "s"(sbase) : "memory");
}
DI void dma1c(const void* sbase, unsigned voff, unsigned lds_base) {
  unsigned sv;
  asm volatile("s_mov_b32 %0, m0\n\ts_mov_b32 m0, %2\n\ts_nop 0\n\tglobal_load_lds_dwordx4 %1, %3 sc1\n\ts_mov_b32 m0, %0"
               : "=&s"(sv) : "v"(voff), "s"(lds_base), "s"(sbase) : "memory");
}
DI void dma4c(const void* sbase, unsigned voff, unsigned lds_base) {
  unsigned sv;
  asm volatile("s_mov_b32 %0, m0\n\ts_mov_b32 m0, %2\n\ts_nop 0\n\t"
               "global_load_lds_dwordx4 %1, %3 sc1\n\tglobal_load_lds_dwordx4 %1, %3 offset:1024 sc1\n\t"
               "global_load_lds_dwordx4 %1, %3 offset:2048 sc1\n\tglobal_load_lds_dwordx4 %1, %3 offset:3072 sc1\n\t"
               "s_mov_b32 m0, %0" : "=&s"(sv) : "v"(voff), "s"(lds_base), "s"(sbase) : "memory");
}
DI void touch1(const void* sbase, unsigned voff, unsigned lds_base) {
  unsigned sv;
  asm volatile("s_mov_b32 %0, m0\n\ts_mov_b32 m0, %2\n\ts_nop 0\n\tglobal_load_lds_dword %1, %3\n\ts_mov_b32 m0, %0"
               : "=&s"(sv) : "v"(voff), "s"(lds_base), "s"(sbase) : "memory");
}
DI bf16x8 ldsfrag(lds_cptr p) { return *(const LAS bf16x8*)p; }
DI s16x4 trread(lds_cptr p) { return __builtin_amdgcn_ds_read_tr16_b64_v4i16((LAS s16x4*)p); }
DI bf16x8 trfrag(lds_cptr p0, lds_cptr p1) {
  s16x4 lo = trread(p0), hi = trread(p1);
  return __builtin_shufflevector(lo, hi, 0, 1, 2, 3, 4, 5, 6, 7);
}
DI float dot2bf(unsigned a, unsigned b, float c) {
  return __builtin_amdgcn_fdot2_f32_bf16(__builtin_bit_cast(bf16x2_t, a), __builtin_bit_cast(bf16x2_t, b), c, false);
}
DI unsigned wait_words(const char* p, int n, unsigned tag, int lane) {
  const gu64* f = (const gu64*)p + (lane < n ? lane : n - 1);
  const u64 t0 = __builtin_amdgcn_s_memrealtime();
  u64 v;
  for (;;) {
    v = __hip_atomic_load(f, RLX_AGENT);
    if (__all((unsigned)(v >> 32) == tag)) break;
    if (__builtin_amdgcn_s_memrealtime() - t0 > 2000000ull) break;
    __builtin_amdgcn_s_sleep(1);
  }
  return (unsigned)v;
}
DI void put_word(char* p, unsigned tag, unsigned val) { __hip_atomic_store((gu64*)p, ((u64)tag << 32) | val, RLX_AGENT); }

constexpr int L_XS = 0;
constexpr int L_WKV = 0;
constexpr int L_XCH = 65536;
constexpr int L_WQ = 114688;
constexpr int L_MODW = 143360;
constexpr int L_SILU = 157696;
constexpr int L_STAT = 143360;
constexpr int L_GB = 145408;
constexpr int L_KMX = 158592;
constexpr int L_LOW = 0;
constexpr int L_M2 = 57344;
constexpr int L_GA = L_M2, L_G2 = L_M2 + 1792, L_GBV = L_M2 + 3584, L_PK = L_M2 + 5376;
constexpr int L_SM = L_M2 + 7168;
constexpr int L_HI = 65536;
constexpr int LDS_BYTES = 160 * 1024;

#define ASTEPH(SC, SN, KADDR, V1ADDR, V0ADDR, H1, H2, H3, H4) do { \
    SN = MFMA32(kf0, qf0, zero16); SBAR(); \
    pw1[2] = pack2(e12, e13); pw1[3] = pack2(e14, e15); \
    kf0 = ldsfrag(KADDR); SBAR(); \
    SC[0] = EX2(SC[0]); SC[1] = EX2(SC[1]); SBAR(); H1; SBAR(); SC[2] = EX2(SC[2]); SC[3] = EX2(SC[3]); PIN(SC); PIN(pw1); SBAR(); \
    o = MFMA32(vf1, BC8(pw1), o); SBAR(); \
    vf1 = ldsfrag(V1ADDR); \
    SC[4] = EX2(SC[4]); SC[5] = EX2(SC[5]); SBAR(); pw0[0] = pack2(SC[0], SC[1]); pw0[1] = pack2(SC[2], SC[3]); SBAR(); H2; SBAR(); SC[6] = EX2(SC[6]); SC[7] = EX2(SC[7]); PIN(SC); PIN(pw0); SBAR(); \
    SN = MFMA32(kf1, qf1, SN); SBAR(); \
    kf1 = ldsfrag((KADDR) + 1024); \
    SC[8] = EX2(SC[8]); SC[9] = EX2(SC[9]); SBAR(); pw0[2] = pack2(SC[4], SC[5]); pw0[3] = pack2(SC[6], SC[7]); SBAR(); H3; SBAR(); SC[10] = EX2(SC[10]); SC[11] = EX2(SC[11]); PIN(SC); PIN(pw0); SBAR(); \
    o = MFMA32(vf0, BC8(pw0), o); SBAR(); \
    vf0 = ldsfrag(V0ADDR); \
    e12 = EX2(SC[12]); e13 = EX2(SC[13]); SBAR(); pw1[0] = pack2(SC[8], SC[9]); pw1[1] = pack2(SC[10], SC[11]); SBAR(); H4; SBAR(); e14 = EX2(SC[14]); e15 = EX2(SC[15]); PIN(e12); PIN(e13); PIN(e14); PIN(e15); PIN(pw1); SBAR(); \
  } while (0)
#define NOH ((void)0)
#define ASTEP(SC, SN, KADDR, V1ADDR, V0ADDR) ASTEPH(SC, SN, KADDR, V1ADDR, V0ADDR, NOH, NOH, NOH, NOH)

#define ATTN_PROLOGUE() \
    kf0 = ldsfrag(abase); kf1 = ldsfrag(abase + 1024); \
    vf0 = ldsfrag(abase + 2048); vf1 = vf0; \
    e12 = 0.f; e13 = 0.f; e14 = 0.f; e15 = 0.f; pw1[0] = 0; pw1[1] = 0; o = zero16; \
    sA = MFMA32(kf0, qf0, zero16); sA = MFMA32(kf1, qf1, sA); \
    kf0 = ldsfrag(abase + 4096); kf1 = ldsfrag(abase + 4096 + 1024);

__global__ void __launch_bounds__(512) fused_block(const u16* __restrict__ x, const u16* __restrict__ wqkv, const u16* __restrict__ wo,
                                                    const u16* __restrict__ w1, const u16* __restrict__ w2, const u16* __restrict__ cvec,
                                                    const u16* __restrict__ wmod, u16* __restrict__ out, char* __restrict__ ws) {
  __shared__ __attribute__((aligned(16))) char lds_arr[LDS_BYTES];
  const lds_ptr L = (lds_ptr)lds_arr;
  const unsigned lds0 = (unsigned)(uintptr_t)lds_arr;
  const int t = threadIdx.x, lane = t & 63, wv = __builtin_amdgcn_readfirstlane(t >> 6), r = lane & 31, h = lane >> 5;
  __builtin_assume(wv < 8);
  const int g16 = (lane >> 4) & 1, tq = (lane & 15) >> 2, tp = lane & 3;
  const int b = blockIdx.x, xg = b & 7, rk = b >> 3;
  const __amdgpu_buffer_rsrc_t rs = __builtin_amdgcn_make_buffer_rsrc(ws, 0, WS_USED, 0x00020000);
  asm volatile("" :: "s"(x), "s"(wqkv), "s"(wo), "s"(w1), "s"(w2), "s"(cvec), "s"(wmod), "s"(out), "s"(ws));
  STAMP_DECL();
  STAMP(0);

  const int rt = xg * 2 + (rk >> 4), hq = rk & 15;
  const bool modwave = (rk < 21) && (wv == 0);
  const int msl = xg * 21 + rk;
  u32x4 craw = {0, 0, 0, 0};
  if (modwave) {
    asm volatile("global_load_dwordx4 %0, %1, %2" : "=v"(craw) : "v"((lane < 56 ? lane : 55) * 16), "s"(cvec) : "memory");
    const unsigned vo = (unsigned)(lane >> 1) * (6 * DM * 2) + (lane & 1) * 16;
#pragma unroll
    for (int i = 0; i < 14; ++i) dma1(wmod + msl * 16 + i * 32 * 6 * DM, vo, lds0 + L_MODW + i * 1024);
  }
  const int a_tt = wv & 3, a_kh = wv >> 2;
  if (wv > 0) {
    const u16* xb = x + (size_t)(rt * 128) * DM;
    const u16* xw = xb + (wv - 1) * 16 * DM;
    const unsigned dw = lds0 + L_XS + (wv - 1) * 14336;
#pragma unroll
    for (int i = 0; i < 7; ++i) {
      const int q = i * 64 + lane, rl = (q * 1171) >> 16, c = q - rl * 56;
      const unsigned vo = rl * (DM * 2) + ((c ^ (rl >> 1)) * 16);
      dma1(xw, vo, dw + i * 1024);
      dma1(xw + 8 * DM, vo ^ 64, dw + 7168 + i * 1024);
    }
    const int q = (wv - 1) * 64 + lane, rl = (q * 1171) >> 16, c = q - rl * 56;
    const unsigned vo = rl * (DM * 2) + ((c ^ (rl >> 1)) * 16);
    dma1(xb + 112 * DM, vo, lds0 + L_XS + 14 * 7168 + (wv - 1) * 1024);
    dma1(xb + 120 * DM, vo ^ 64, lds0 + L_XS + 15 * 7168 + (wv - 1) * 1024);
  }
  u64 epoch;
  asm volatile("s_load_dwordx2 %0, %1, 0x0" : "=s"(epoch) : "s"(ws) : "memory");
  const unsigned wvo = (lane >> 2) * (3 * DM * 2) + ((tp == 3) ? 40 : tp * 16);
  if (wv > 0) {
#pragma unroll
    for (int i = 0; i < 4; ++i) { const int s = (wv - 1) * 4 + i; dma1(wqkv + hq * DH + s * 16 * 3 * DM, wvo, lds0 + L_WQ + s * 1024); }
  }
  if (t == 0) *(LAS unsigned*)(L + L_KMX) = 0;
  asm volatile("s_waitcnt lgkmcnt(0)" : "+s"(epoch) :: "memory");
  const unsigned tag = (unsigned)epoch + 1u;
  if (modwave) {
    asm volatile("s_waitcnt vmcnt(14)" : "+v"(craw) :: "memory");
    u32x4 sv;
#pragma unroll
    for (int j = 0; j < 4; ++j) {
      const float c0_ = bflo(craw[j]), c1_ = bfhi(craw[j]);
      sv[j] = pack2(c0_ * __builtin_amdgcn_rcpf(1.f + EX2(-1.4426950408889634f * c0_)), c1_ * __builtin_amdgcn_rcpf(1.f + EX2(-1.4426950408889634f * c1_)));
    }
    if (lane < 56) *(LAS u32x4*)(L + L_SILU + lane * 16) = sv;
    const int kg = lane >> 4;
    const lds_cptr bp = L + L_MODW + (8 * kg + tq) * 32 + tp * 8;
    const lds_cptr ap = L + L_SILU + kg * 16;
    bf16x8 af[14], bfr[14];
    asm volatile("s_waitcnt lgkmcnt(0)" ::: "memory");
#pragma unroll
    for (int s = 0; s < 14; ++s) af[s] = ldsfrag(ap + s * 64);
    asm volatile("s_waitcnt vmcnt(0)" ::: "memory");
#pragma unroll
    for (int s = 0; s < 14; ++s) bfr[s] = trfrag(bp + s * 1024, bp + s * 1024 + 128);
    f32x4 acc0 = {0.f, 0.f, 0.f, 0.f}, acc1 = acc0;
#pragma unroll
    for (int s = 0; s < 14; s += 2) { acc0 = MFMA16(af[s], bfr[s], acc0); acc1 = MFMA16(af[s + 1], bfr[s + 1], acc1); }
    if (lane < 16) put_word(ws + OFF_MODG + (msl * 16 + lane) * 8, tag, __float_as_uint(rbf(acc0[0] + acc1[0])));
  }
  STAMP(1);
  BAR_VM(4);
  u32x4 xr[14];
  {
    const int row = a_tt * 32 + r, gsw = (row >> 1) & 7, c0 = a_kh * 28 + h;
#pragma unroll
    for (int i = 0; i < 4; ++i) {
      const int c = c0 + 2 * i;
      const lds_cptr xa = L + L_XS + row * (DM * 2) + (c & ~7) * 16 + (((c & 7) ^ gsw) * 16);
#pragma unroll
      for (int s = i; s < 14; s += 4) xr[s] = *(const LAS u32x4*)(xa + (s >> 2) * 128);
    }
  }
  u32x4 mg;
  asm volatile("global_load_dwordx4 %0, %1, %2 offset:0 sc1" : "=v"(mg) : "v"((t < DM ? t : DM - 1) * 16), "s"(ws + OFF_MODG) : "memory");
  BAR_LGKM();
  STAMP(2);
  {
    const u16* wsrc = wqkv + hq * DH + (1 + (wv >> 2)) * DM + (wv & 3) * 7 * 16 * 3 * DM;
    float s1a = 0.f, s2a = 0.f, s1b = 0.f, s2b = 0.f;
#pragma unroll
    for (int s = 0; s < 14; ++s) {
      if (s == 0 || s == 5 || s == 10) dma1(wsrc + (s / 5) * 16 * 3 * DM, wvo, lds0 + L_WKV + (wv * 7 + (s / 5)) * 1024);
      s1a = dot2bf(xr[s][0], 0x3f803f80u, s1a); s2a = dot2bf(xr[s][0], xr[s][0], s2a);
      s1b = dot2bf(xr[s][1], 0x3f803f80u, s1b); s2b = dot2bf(xr[s][1], xr[s][1], s2b);
      s1a = dot2bf(xr[s][2], 0x3f803f80u, s1a); s2a = dot2bf(xr[s][2], xr[s][2], s2a);
      s1b = dot2bf(xr[s][3], 0x3f803f80u, s1b); s2b = dot2bf(xr[s][3], xr[s][3], s2b);
      PIN(s1a); PIN(s2a); PIN(s1b); PIN(s2b); SBAR();
    }
    const float s1 = xhalf_add(s1a + s1b), s2 = xhalf_add(s2a + s2b);
    if (h == 0) { f32x2 v = {s1, s2}; *(LAS f32x2*)(L + L_STAT + (wv * 32 + r) * 8) = v; }
    asm volatile("s_waitcnt vmcnt(3)" : "+v"(mg) :: "memory");
    if (wv < 7) {
      if (!__all(mg[1] == tag && mg[3] == tag)) {
        const u64 t0 = __builtin_amdgcn_s_memrealtime();
        for (;;) {
          mg = __builtin_amdgcn_raw_buffer_load_b128(rs, OFF_MODG + t * 16, 0, 16);
          if (__all(mg[1] == tag && mg[3] == tag)) break;
          if (__builtin_amdgcn_s_memrealtime() - t0 > 2000000ull) break;
          __builtin_amdgcn_s_sleep(1);
        }
      }
      f32x2 o2 = {__uint_as_float(mg[0]), __uint_as_float(mg[2])};
      const bool isg = t >= 224;
      if (isg) { o2[0] = rbf(1.f + o2[0]); o2[1] = rbf(1.f + o2[1]); }
      *(LAS f32x2*)(L + L_GB + (isg ? 2 * t - DM : DM + 2 * t) * 4) = o2;
    }
    BAR_LGKM();
    STAMP(3);
    {
      const unsigned to = (unsigned)(((rk >> 3) * 32 + wv * 4 + ((lane >> 2) & 3)) * (FF * 2) + (((rk & 7) * 448) & ~127) + (lane & 3) * 128);
      touch1(w1, to, lds0 + L_KMX + 64);
    }
    const lds_cptr gp = L + L_GB + (a_kh * 224 + 8 * h) * 4;
    f32x4 gq[3][4];
#define LOADGB(slot, s) do { gq[slot][0] = *(const LAS f32x4*)(gp + (s) * 64); gq[slot][1] = *(const LAS f32x4*)(gp + (s) * 64 + 16); \
      gq[slot][2] = *(const LAS f32x4*)(gp + DM * 4 + (s) * 64); gq[slot][3] = *(const LAS f32x4*)(gp + DM * 4 + (s) * 64 + 16); } while (0)
    const f32x2 o2 = *(const LAS f32x2*)(L + L_STAT + ((wv ^ 4) * 32 + r) * 8);
    LOADGB(0, 0); LOADGB(1, 1);
    const float mu = (s1 + o2[0]) * (1.f / DM);
    const float var = fmaxf((s2 + o2[1]) * (1.f / DM) - mu * mu, 0.f);
    const float rstd = __builtin_amdgcn_rsqf(var + 1e-5f);
    const float nb = -mu * rstd;
    SBAR();
#pragma unroll
    for (int s = 0; s < 14; ++s) {
      if (s + 2 < 14) LOADGB((s + 2) % 3, s + 2);
      if (s < 8 && (s & 1) == 0) dma1(wsrc + (3 + s / 2) * 16 * 3 * DM, wvo, lds0 + L_WKV + (wv * 7 + 3 + s / 2) * 1024);
      SBAR();
      const f32x4 g0 = gq[s % 3][0], g1 = gq[s % 3][1], b0 = gq[s % 3][2], b1 = gq[s % 3][3];
      xr[s][0] = pack2((bflo(xr[s][0]) * rstd + nb) * g0[0] + b0[0], (bfhi(xr[s][0]) * rstd + nb) * g0[1] + b0[1]);
      xr[s][1] = pack2((bflo(xr[s][1]) * rstd + nb) * g0[2] + b0[2], (bfhi(xr[s][1]) * rstd + nb) * g0[3] + b0[3]);
      xr[s][2] = pack2((bflo(xr[s][2]) * rstd + nb) * g1[0] + b1[0], (bfhi(xr[s][2]) * rstd + nb) * g1[1] + b1[1]);
      xr[s][3] = pack2((bflo(xr[s][3]) * rstd + nb) * g1[2] + b1[2], (bfhi(xr[s][3]) * rstd + nb) * g1[3] + b1[3]);
      PIN(xr[s]); SBAR();
    }
  }
  f32x16 zero16;
#pragma unroll
  for (int i = 0; i < 16; ++i) zero16[i] = 0.f;
  f32x16 aq = zero16, ak = zero16, av = zero16;
  {
    const int wro = a_kh * 14 * 1024 + (8 * h + tq) * 64 + g16 * 32 + tp * 8;
    const lds_cptr wqp = L + L_WQ + wro, wkp = L + L_WKV + wro, wvp = L + L_WKV + 28672 + wro;
    bf16x8 wa[14], wb[14];
#pragma unroll
    for (int s = 0; s < 14; ++s) wa[s] = trfrag(wqp + s * 1024, wqp + s * 1024 + 256);
    SBAR();
#pragma unroll
    for (int s = 0; s < 7; ++s) { aq = MFMA32(wa[s], BC8(xr[s]), aq); PIN(aq); SBAR(); }
    BAR_VM(0);
    STAMP(4);
#pragma unroll
    for (int s = 7; s < 14; ++s) {
      aq = MFMA32(wa[s], BC8(xr[s]), aq); PIN(aq); SBAR();
      wb[2 * (s - 7)] = trfrag(wkp + 2 * (s - 7) * 1024, wkp + 2 * (s - 7) * 1024 + 256);
      wb[2 * (s - 7) + 1] = trfrag(wkp + (2 * (s - 7) + 1) * 1024, wkp + (2 * (s - 7) + 1) * 1024 + 256); SBAR();
    }
#pragma unroll
    for (int s = 0; s < 14; ++s) {
      ak = MFMA32(wb[s], BC8(xr[s]), ak); PIN(ak); SBAR();
      wa[s] = trfrag(wvp + s * 1024, wvp + s * 1024 + 256); SBAR();
    }
#pragma unroll
    for (int s = 0; s < 14; ++s) { av = MFMA32(BC8(xr[s]), wa[s], av); PIN(av); SBAR(); }
  }
  STAMP(5);
  {
    const lds_ptr xw = L + L_XCH + wv * 6144 + lane * 16;
    const lds_cptr xrd = L + L_XCH + (wv ^ 4) * 6144 + lane * 16;
    const int tt = rt * 4 + a_tt;
    const bool vpad = (r >= 24 && r < 28);
    const unsigned vconst = (r == 24) ? 0x3f803f80u : 0u;
#define W4(off, A, i0) do { f32x4 v_ = {A[i0], A[(i0) + 1], A[(i0) + 2], A[(i0) + 3]}; *(LAS f32x4*)(xw + (off)) = v_; } while (0)
#define R4(off, A, i0) do { const f32x4 v_ = *(const LAS f32x4*)(xrd + (off)); A[i0] += v_[0]; A[(i0) + 1] += v_[1]; A[(i0) + 2] += v_[2]; A[(i0) + 3] += v_[3]; } while (0)
    if (a_kh == 0) {
      W4(0, ak, 0); W4(1024, ak, 4); W4(2048, ak, 8); W4(3072, ak, 12); W4(4096, av, 8); W4(5120, av, 12);
      BAR_LGKM();
      R4(0, aq, 0); R4(1024, aq, 4); R4(2048, aq, 8); R4(3072, aq, 12); R4(4096, av, 0); R4(5120, av, 4);
      const float qs = 0.18898223650461362f * 1.4426950408889634f;
#pragma unroll
      for (int i = 0; i < 16; ++i) aq[i] *= qs;
      u32x4 f0 = pack8(aq, 0), f1 = pack8(aq, 1), vf = pack8(av, 0);
      if (h == 0) { f1[2] = 0; f1[3] = 0; }
#pragma unroll
      for (int i = 0; i < 4; ++i) vf[i] = vpad ? vconst : vf[i];
      const unsigned mo = OFF_Q + (hq * NTT + tt) * 2048 + lane * 16;
      __builtin_amdgcn_raw_buffer_store_b128(f0, rs, mo, 0, 16);
      __builtin_amdgcn_raw_buffer_store_b128(f1, rs, mo + 1024, 0, 16);
      __builtin_amdgcn_raw_buffer_store_b128(vf, rs, OFF_KV + ((hq * NTT + tt) * 4 + 2) * 1024 + lane * 16, 0, 16);
    } else {
      W4(0, aq, 0); W4(1024, aq, 4); W4(2048, aq, 8); W4(3072, aq, 12); W4(4096, av, 0); W4(5120, av, 4);
      BAR_LGKM();
      R4(0, ak, 0); R4(1024, ak, 4); R4(2048, ak, 8); R4(3072, ak, 12); R4(4096, av, 8); R4(5120, av, 12);
      u32x4 f0 = pack8(ak, 0), f1 = pack8(ak, 1), vf = pack8(av, 1);
      u32x4 f1z = f1;
      if (h == 0) { f1z[2] = 0; f1z[3] = 0; f1[2] = 0x3f803f80u; f1[3] = 0x00003f80u; }
#pragma unroll
      for (int i = 0; i < 4; ++i) vf[i] = vpad ? vconst : vf[i];
      const unsigned mo = OFF_KV + (hq * NTT + tt) * 4096 + lane * 16;
      __builtin_amdgcn_raw_buffer_store_b128(f0, rs, mo, 0, 16);
      __builtin_amdgcn_raw_buffer_store_b128(f1, rs, mo + 1024, 0, 16);
      __builtin_amdgcn_raw_buffer_store_b128(vf, rs, mo + 3072, 0, 16);
      float kn = 0.f;
#pragma unroll
      for (int i = 0; i < 4; ++i) { kn = dot2bf(f0[i], f0[i], kn); kn = dot2bf(f1z[i], f1z[i], kn); }
      kn = xhalf_add(kn);
      asm volatile("ds_max_u32 %0, %1" :: "v"(lds0 + L_KMX), "v"(__float_as_uint(kn)) : "memory");
    }
    BAR_ALL();
    if (t == 0) put_word(ws + OFF_FKV + (hq * 16 + rt) * 8, tag, *(const LAS unsigned*)(L + L_KMX));
  }
  STAMP(6);
  const int head = xg * 2 + (rk >> 4), qt = rk & 15;
  const int tg = xg * 4 + (rk >> 3), sl = rk & 7;
  const int qb = wv & 3, kh = wv >> 2;
  const int tb = wv >> 2, kq = wv & 3;
  const int tt_own = tg * 2 + tb;
  const unsigned l16 = lane * 16;
  const unsigned o_off = OFF_O + ((qt * 4 + qb) * 32 + head * 2 + kh) * 1024 + l16;
  const unsigned y_off = OFF_Y + (tt_own * 28 + sl * 4 + kq) * 1024 + l16;
  const unsigned st_off = OFF_ST + ((tt_own * 28 + sl * 4 + kq) * 32 + r) * 8;
  const unsigned h_off = OFF_H + ((tg * 2) * 112 + (sl * 7 + wv) * 2) * 1024 + l16;
  unsigned w1vo[4];
#pragma unroll
  for (int i = 0; i < 4; ++i) { const int pc = (wv + 8 * i < 28) ? wv + 8 * i : wv; const int q = pc * 64 + lane, row = q / 28, c = q - row * 28; w1vo[i] = row * (FF * 2) + c * 16; }
  const u16* w1b = w1 + sl * 224;
  const lds_cptr w1rd = L + L_LOW + (4 * h + tq) * 448 + wv * 64 + g16 * 32 + tp * 8;
  const unsigned w1d3 = (wv < 4) ? 24576 : 0;
#define W1D1(c, j) do { if ((c) >= 0) dma1(w1b + (size_t)(c) * 64 * FF, w1vo[j], lds0 + L_LOW + ((c) & 1) * 28672 + wv * 1024 + ((j) < 3 ? (j) * 8192 : w1d3)); } while (0)
#define W1_DMA(c) do { W1D1(c, 0); W1D1(c, 1); W1D1(c, 2); W1D1(c, 3); } while (0)
  if (wv == 7) {
    float km = __uint_as_float(wait_words(ws + OFF_FKV + head * 128, 16, tag, lane));
#pragma unroll
    for (int m = 8; m >= 1; m >>= 1) km = fmaxf(km, __shfl_xor(km, m));
    if (lane == 0) *(LAS float*)(L + L_SM) = km;
  } else {
    const u64 t0 = __builtin_amdgcn_s_memrealtime();
    u32x4 v0, v1;
    for (;;) {
      v0 = __builtin_amdgcn_raw_buffer_load_b128(rs, OFF_MODG + 7168 + t * 16, 0, 16);
      v1 = __builtin_amdgcn_raw_buffer_load_b128(rs, OFF_MODG + 14336 + t * 16, 0, 16);
      if (__all(v0[1] == tag && v0[3] == tag && v1[1] == tag && v1[3] == tag)) break;
      if (__builtin_amdgcn_s_memrealtime() - t0 > 2000000ull) break;
      __builtin_amdgcn_s_sleep(1);
    }
    W1_DMA(0); W1_DMA(1);
    const int e = (t < 224) ? 2 * t : 2 * t - DM;
    const int pk = (e >> 4) * 32 + ((e >> 2) & 1) * 16 + ((e >> 3) & 1) * 8 + (e & 3) * 2;
    if (t < 224) {
      f32x2 a = {__uint_as_float(v0[0]), __uint_as_float(v0[2])};
      f32x2 g = {rbf(1.f + __uint_as_float(v1[0])), rbf(1.f + __uint_as_float(v1[2]))};
      *(LAS f32x2*)(L + L_GA + e * 4) = a;
      *(LAS f32x2*)(L + L_G2 + e * 4) = g;
      *(LAS unsigned*)(L + L_PK + pk) = pack2(g[0], g[1]);
    } else {
      f32x2 g = {__uint_as_float(v1[0]), __uint_as_float(v1[2])};
      *(LAS f32x2*)(L + L_GBV + e * 4) = g;
      *(LAS unsigned*)(L + L_PK + 896 + pk) = pack2(__uint_as_float(v0[0]), __uint_as_float(v0[2]));
    }
  }
  BAR_LGKM();
  STAMP(7);
  const char* kvsrc = ws + OFF_KV + (size_t)(head * NTT + kh * 32 + qb) * 4096;
  const unsigned kvdst = lds0 + L_HI + kh * 16384 + qb * 4096;
#define DMA_KV(i, buf) dma4c(kvsrc + (size_t)(i) * 16384, l16, kvdst + (buf))
  bf16x8 qf0, qf1;
  {
    u32x4 q0, q1;
    asm volatile("global_load_dwordx4 %0, %2, %3 sc1\n\tglobal_load_dwordx4 %1, %2, %3 offset:1024 sc1" : "=&v"(q0), "=&v"(q1)
                 : "v"(l16), "s"(ws + OFF_Q + (head * NTT + qt * 4 + qb) * 2048) : "memory");
    DMA_KV(0, 0);
    if (wv == 7) { W1_DMA(0); W1_DMA(1); }
    DMA_KV(1, 32768);
    const float km = *(const LAS float*)(L + L_SM);
    asm volatile("s_waitcnt vmcnt(8)" : "+v"(q0), "+v"(q1) :: "memory");
    float qn = 0.f;
#pragma unroll
    for (int i = 0; i < 4; ++i) { qn = dot2bf(q0[i], q0[i], qn); qn = dot2bf(q1[i], q1[i], qn); }
    qn = xhalf_add(qn);
    const float bound = __builtin_amdgcn_sqrtf(qn * km) * 1.004f + 1e-30f;
    if (h == 0) q1[2] = ((__float_as_uint(bound) + 0xffffu) >> 16) | 0x8000u;
    qf0 = BC8(q0); qf1 = BC8(q1);
  }
  bf16x8 wf[28];
  f32x16 o, sA, sB;
  u32x4 pw0, pw1;
  bf16x8 kf0, kf1, vf0, vf1;
  float e12, e13, e14, e15;
  lds_cptr abase = L + L_HI + kh * 16384 + l16;
  lds_cptr aslot[3] = {abase, abase + 32768, abase + 65536};
  PIN(aslot[0]); PIN(aslot[1]); PIN(aslot[2]);
  BAR_VM(4);
  STAMP(8);
  ATTN_PROLOGUE();
  const unsigned rvo = (lane >> 3) * (DM * 2) + (((lane & 7) ^ (((lane >> 4) & 1) << 2)) * 16);
  const int rsw = ((tq >> 1) & 1) * 64;
  const u16* wob = wo + (sl < 7 ? sl : 0) * 64;
  const int wo3 = (wv < 4) ? wv + 24 : wv;
#define W1_RD1(c, i_) (wf[4 * (c) + (i_)] = trfrag(w1rd + ((c) & 1) * 28672 + (i_) * 16 * 448, w1rd + ((c) & 1) * 28672 + ((i_) * 16 + 8) * 448))
#define W1_READ(c) do { W1_RD1(c, 0); W1_RD1(c, 1); W1_RD1(c, 2); W1_RD1(c, 3); } while (0)
#define KV1(st, j) do { if ((st) < 8) dma1c(kvsrc + (size_t)(st) * 16384 + (j) * 1024, l16, kvdst + ((st) % 3) * 32768 + (j) * 1024); } while (0)
#define W1R1(c, j) do { if ((c) >= 0) W1_RD1(((c) >= 0 ? (c) : 0), j); } while (0)
#define WO1(hf, j) do { if ((hf) >= 0) { const int pc_ = (j) < 3 ? wv + 8 * (j) : wo3; \
      dma1(wob + ((hf) * 28 + pc_) * 8 * DM, rvo, lds0 + L_HI + ((hf) ? 0 : 65536) + pc_ * 1024); } } while (0)
#define STAGE(st, NW, RA, RB, RC, DC, WH, TCH) do { \
    const lds_cptr bc = aslot[(st) % 3], bn = aslot[((st) + 1) % 3]; \
    ASTEPH(sA, sB, bc + 2 * 4096, bc + 0 * 4096 + 3072, bc + 1 * 4096 + 2048, W1R1(RA, 0), W1R1(RA, 1), W1R1(RA, 2), W1R1(RA, 3)); \
    ASTEPH(sB, sA, bc + 3 * 4096, bc + 1 * 4096 + 3072, bc + 2 * 4096 + 2048, W1R1(RB, 0), W1R1(RB, 1), W1R1(RB, 2), W1R1(RB, 3)); \
    BAR_VM(NW); \
    ASTEPH(sA, sB, bn + 0 * 4096, bc + 2 * 4096 + 3072, bc + 3 * 4096 + 2048, KV1((st) + 2, 0); WO1(WH, 0); W1R1(RC, 0), KV1((st) + 2, 1); WO1(WH, 1); W1R1(RC, 1), \
           KV1((st) + 2, 2); WO1(WH, 2); W1R1(RC, 2), KV1((st) + 2, 3); WO1(WH, 3); W1R1(RC, 3)); \
    ASTEPH(sB, sA, bn + 1 * 4096, bc + 3 * 4096 + 3072, bn + 0 * 4096 + 2048, W1D1(DC, 0), W1D1(DC, 1), W1D1(DC, 2), W1D1(DC, 3); TCH); \
  } while (0)
  const int tl = wv * 9 + (lane < 9 ? lane : 8);
#define TOUCH_WO(i) touch1(wo, (rk * 98 + ((i) * 72 + tl < 98 ? (i) * 72 + tl : 97)) * 128, lds0 + L_SM + 256)
#define TOUCH_W2(i) touch1(w2, (rk * 392 + ((i) * 72 + tl < 392 ? (i) * 72 + tl : 391)) * 128, lds0 + L_SM + 256)
  STAGE(0, 0,  0,  1, -1,  2, -1, TOUCH_WO(0));
  STAGE(1, 5, -1, -1, -1,  3, -1, TOUCH_WO(1); TOUCH_W2(0));
  STAGE(2, 6, -1, -1,  2, -1, -1, TOUCH_W2(1));
  STAGE(3, 1, -1, -1,  3,  4, -1, TOUCH_W2(2));
  STAGE(4, 5, -1, -1, -1,  5, -1, TOUCH_W2(3));
  STAGE(5, 5, -1, -1,  4, -1, -1, TOUCH_W2(4); TOUCH_W2(5));
  STAGE(6, 2, -1, -1,  5,  6,  0, NOH);
  STAGE(7, 8, -1, -1, -1, -1,  1, NOH);
  STAMP(9);
  float fin[8];
  float lsum;
  constexpr int L_X = L_LOW + 28672;
#pragma unroll 1
  for (int pass = 0; pass < 2; ++pass) {
    pw1[2] = pack2(e12, e13); pw1[3] = pack2(e14, e15);
    o = MFMA32(vf1, BC8(pw1), o);
    {
      f32x4 a, b2;
      if (kh) {
#pragma unroll
        for (int i = 0; i < 4; ++i) { a[i] = o[i]; b2[i] = o[4 + i]; fin[i] = o[8 + i]; fin[4 + i] = o[12 + i]; }
      } else {
#pragma unroll
        for (int i = 0; i < 4; ++i) { a[i] = o[8 + i]; b2[i] = o[12 + i]; fin[i] = o[i]; fin[4 + i] = o[4 + i]; }
      }
      *(LAS f32x4*)(L + L_X + (wv * 2 + 0) * 1024 + l16) = a;
      *(LAS f32x4*)(L + L_X + (wv * 2 + 1) * 1024 + l16) = b2;
      if (h == 0) *(LAS float*)(L + L_X + 16384 + (wv * 32 + r) * 4) = o[12];
    }
    BAR_LGKM();
    {
      const int pw = wv ^ 4;
      const f32x4 a = *(const LAS f32x4*)(L + L_X + (pw * 2 + 0) * 1024 + l16);
      const f32x4 b2 = *(const LAS f32x4*)(L + L_X + (pw * 2 + 1) * 1024 + l16);
      const float c0 = *(const LAS float*)(L + L_X + 16384 + lane * 4) + *(const LAS float*)(L + L_X + 16384 + (128 + lane) * 4);
      const float c1 = *(const LAS float*)(L + L_X + 16384 + (64 + lane) * 4) + *(const LAS float*)(L + L_X + 16384 + (192 + lane) * 4);
#pragma unroll
      for (int i = 0; i < 4; ++i) { fin[i] += a[i]; fin[4 + i] += b2[i]; }
      lsum = *(const LAS float*)(L + L_X + 16384 + (qb * 32 + r) * 4) + *(const LAS float*)(L + L_X + 16384 + ((qb + 4) * 32 + r) * 4);
      const bool ok = (c0 >= 8.271806e-25f) && (c1 >= 8.271806e-25f);
      if (pass == 1 || __all(ok)) break;
    }
    {
      u32x4 q1 = __builtin_bit_cast(u32x4, qf1);
      if (h == 0) { q1[2] = 0; q1[3] = 0; }
      qf1 = BC8(q1);
      float mx = -INFINITY;
      const unsigned ko = OFF_KV + (head * NTT + kh * 32) * 4096 + l16;
#pragma unroll 1
      for (int kt = 0; kt < 32; ++kt) {
        const u32x4 k0 = __builtin_amdgcn_raw_buffer_load_b128(rs, ko + kt * 4096, 0, 16);
        const u32x4 k1 = __builtin_amdgcn_raw_buffer_load_b128(rs, ko + kt * 4096 + 1024, 0, 16);
        f32x16 s = MFMA32(BC8(k0), qf0, zero16);
        s = MFMA32(BC8(k1), qf1, s);
#pragma unroll
        for (int i = 0; i < 16; ++i) mx = fmaxf(mx, s[i]);
      }
      mx = xhalf_max(mx);
      if (h == 0) *(LAS float*)(L + L_X + 20480 + (wv * 32 + r) * 4) = mx;
      BAR_ALL();
      mx = fmaxf(*(const LAS float*)(L + L_X + 20480 + (qb * 32 + r) * 4), *(const LAS float*)(L + L_X + 20480 + ((qb + 4) * 32 + r) * 4));
      const float m0 = rbf(mx), m1 = rbf(mx - m0), m2 = rbf(mx - m0 - m1);
      if (h == 0) { q1[2] = pack2(-m0, -m1); q1[3] = pack2(-m2, 0.f); }
      qf1 = BC8(q1);
      DMA_KV(0, 0);
      DMA_KV(1, 32768);
      asm volatile("s_waitcnt vmcnt(4) lgkmcnt(0)\n\ts_barrier" ::: "memory");
      ATTN_PROLOGUE();
      int cur = 0, nxt = 32768, nn = 65536;
#pragma unroll 1
      for (int st = 0; st < 8; ++st) {
        const lds_cptr bc = abase + cur, bn = abase + nxt;
        ASTEP(sA, sB, bc + 2 * 4096, bc + 0 * 4096 + 3072, bc + 1 * 4096 + 2048);
        ASTEP(sB, sA, bc + 3 * 4096, bc + 1 * 4096 + 3072, bc + 2 * 4096 + 2048);
        BAR_VM(0);
        if (st + 2 < 8) DMA_KV(st + 2, nn);
        if (st == 7) { WO1(0, 0); WO1(0, 1); WO1(0, 2); WO1(0, 3); WO1(1, 0); WO1(1, 1); WO1(1, 2); WO1(1, 3); }
        ASTEP(sA, sB, bn + 0 * 4096, bc + 2 * 4096 + 3072, bc + 3 * 4096 + 2048);
        ASTEP(sB, sA, bn + 1 * 4096, bc + 3 * 4096 + 3072, bn + 0 * 4096 + 2048);
        const int tmp = cur; cur = nxt; nxt = nn; nn = tmp;
      }
    }
  }
  {
    const float inv = __builtin_amdgcn_rcpf(lsum);
    u32x4 ov;
#pragma unroll
    for (int i = 0; i < 4; ++i) ov[i] = pack2(fin[2 * i] * inv, fin[2 * i + 1] * inv);
    if (kh == 1 && h == 0) { ov[2] = 0; ov[3] = 0; }
    __builtin_amdgcn_raw_buffer_store_b128(ov, rs, o_off, 0, 16);
    BAR_ALL();
    if (t == 0) put_word(ws + OFF_FO + (qt * 16 + head) * 8, tag, 0);
  }
  STAMP(10);
  const int n0 = (sl * 2 + (kq >> 1)) * 32 + (kq & 1) * 16 + 4 * h;
  u32x2 xa2, xb2;
  unsigned xc0;
  asm volatile("global_load_dwordx2 %0, %3, %5\n\tglobal_load_dwordx2 %1, %3, %5 offset:16\n\tglobal_load_ushort %2, %4, %5"
               : "=&v"(xa2), "=&v"(xb2), "=&v"(xc0) : "v"((r * DM + (sl < 7 ? n0 : 0)) * 2), "v"(r * DM * 2), "s"(x + (size_t)tt_own * 32 * DM) : "memory");
  W1_READ(6);
  f32x16 uv = zero16;
  if (wv < 7) {
    const lds_cptr pp = L + L_PK + g16 * 896 + h * 16;
    bf16x8 pf[14];
#pragma unroll
    for (int hf2 = 0; hf2 < 2; ++hf2) {
#pragma unroll
      for (int i = 0; i < 14; ++i) pf[i] = ldsfrag(pp + (hf2 * 14 + i) * 32);
      SBAR();
#pragma unroll
      for (int i = 0; i < 14; ++i) uv = MFMA32(wf[hf2 * 14 + i], pf[i], uv);
      PIN(uv); SBAR();
    }
  }
  STAMP(11);
  if (wv == 7 && sl < 7) wait_words(ws + OFF_FO + (tg >> 1) * 128, 16, tag, lane);
  asm volatile("s_waitcnt vmcnt(0) lgkmcnt(0)\n\ts_barrier" : "+v"(xa2), "+v"(xb2), "+v"(xc0) :: "memory");
  const lds_ptr redb = L + L_LOW + tb * 24576 + l16;
#define RW1(A, o8, p, KQ) do { f32x4 v0 = {A[o8], A[(o8) + 1], A[(o8) + 2], A[(o8) + 3]}, v1 = {A[(o8) + 4], A[(o8) + 5], A[(o8) + 6], A[(o8) + 7]}; \
        *(LAS f32x4*)(redb + (((p) * 3 + (((KQ) - (p) - 1) & 3)) * 2) * 1024) = v0; *(LAS f32x4*)(redb + (((p) * 3 + (((KQ) - (p) - 1) & 3)) * 2 + 1) * 1024) = v1; } while (0)
#define RKEEP(AT, A, o8) do { _Pragma("unroll") for (int i = 0; i < 8; ++i) AT[i] = A[(o8) + i]; } while (0)
#define RED_WRITE(AT, A0, A1) do { \
      if (kq == 0)      { RW1(A0, 8, 1, 0); RW1(A1, 0, 2, 0); RW1(A1, 8, 3, 0); RKEEP(AT, A0, 0); } \
      else if (kq == 1) { RW1(A0, 0, 0, 1); RW1(A1, 0, 2, 1); RW1(A1, 8, 3, 1); RKEEP(AT, A0, 8); } \
      else if (kq == 2) { RW1(A0, 0, 0, 2); RW1(A0, 8, 1, 2); RW1(A1, 8, 3, 2); RKEEP(AT, A1, 0); } \
      else              { RW1(A0, 0, 0, 3); RW1(A0, 8, 1, 3); RW1(A1, 0, 2, 3); RKEEP(AT, A1, 8); } } while (0)
#define RED_READ(AT) do { \
      _Pragma("unroll") for (int d = 0; d < 3; ++d) { \
        const f32x4 v0 = *(const LAS f32x4*)(redb + kq * 6144 + (d * 2) * 1024); \
        const f32x4 v1 = *(const LAS f32x4*)(redb + kq * 6144 + (d * 2 + 1) * 1024); \
        _Pragma("unroll") for (int i = 0; i < 4; ++i) { AT[i] += v0[i]; AT[4 + i] += v1[i]; } } } while (0)
  const u16* w2b = w2 + (sl < 7 ? sl : 0) * 64;
#define W2A(i) dma1(w2b + (size_t)((wv >> 1) * 448 + ((wv & 1) * 12 + (i)) * 8) * DM, rvo, lds0 + L_HI + (wv * 12 + (i)) * 1024)
  u32x4 x1p;
  if (sl < 7) {
    float x1[8];
    STAMP(12);
    f32x16 acc0 = zero16, acc1 = zero16;
    {
      const lds_cptr wop = L + L_HI + ((kq >> 1) ? 0 : 65536) + ((kq & 1) * 4 * DH + 4 * h + tq) * 128 + g16 * 32 + tp * 8;
      const lds_cptr p0 = wop + rsw, p1 = wop + (64 ^ rsw);
      bf16x8 wa0[4], wa1[4];
#define WOROW0(i) (((i) >> 1) * DH + 16 * ((i) & 1))
#define WOROW1(i) (((i) >> 1) * DH + (((i) & 1) ? 20 : 8))
      u32x4 ob[8];
      const unsigned oo = OFF_O + (tt_own * 32 + kq * 8) * 1024 + l16;
#pragma unroll
      for (int i = 0; i < 8; ++i) ob[i] = __builtin_amdgcn_raw_buffer_load_b128(rs, oo + i * 1024, 0, 16);
      SBAR();
#pragma unroll
      for (int i = 0; i < 4; ++i) {
        wa0[i] = trfrag(p0 + WOROW0(i) * 128, p0 + WOROW1(i) * 128);
        wa1[i] = trfrag(p1 + WOROW0(i) * 128, p1 + WOROW1(i) * 128);
      }
      SBAR();
#pragma unroll
      for (int i = 0; i < 8; ++i) {
        acc0 = MFMA32(wa0[i & 3], BC8(ob[i]), acc0); acc1 = MFMA32(wa1[i & 3], BC8(ob[i]), acc1); PIN(acc0); PIN(acc1); SBAR();
        if (i < 4) {
          wa0[i] = trfrag(p0 + WOROW0(i + 4) * 128, p0 + WOROW1(i + 4) * 128);
          wa1[i] = trfrag(p1 + WOROW0(i + 4) * 128, p1 + WOROW1(i + 4) * 128);
        }
        SBAR();
      }
    }
    STAMP(13);
    float at[8];
    RED_WRITE(at, acc0, acc1);
    const f32x4 ga0 = *(const LAS f32x4*)(L + L_GA + n0 * 4), ga1 = *(const LAS f32x4*)(L + L_GA + (n0 + 8) * 4);
    const f32x4 g20 = *(const LAS f32x4*)(L + L_G2 + n0 * 4), g21 = *(const LAS f32x4*)(L + L_G2 + (n0 + 8) * 4);
    BAR_LGKM();
    RED_READ(at);
    x1[0] = bflo(xa2[0]) + ga0[0] * at[0]; x1[1] = bfhi(xa2[0]) + ga0[1] * at[1];
    x1[2] = bflo(xa2[1]) + ga0[2] * at[2]; x1[3] = bfhi(xa2[1]) + ga0[3] * at[3];
    x1[4] = bflo(xb2[0]) + ga1[0] * at[4]; x1[5] = bfhi(xb2[0]) + ga1[1] * at[5];
    x1[6] = bflo(xb2[1]) + ga1[2] * at[6]; x1[7] = bfhi(xb2[1]) + ga1[3] * at[7];
#pragma unroll
    for (int i = 0; i < 4; ++i) x1p[i] = pack2(x1[2 * i], x1[2 * i + 1]);
    PIN(x1p);
    const float cshift = __uint_as_float(xc0 << 16);
    float p1 = 0.f, p2 = 0.f, y[8];
#pragma unroll
    for (int i = 0; i < 8; ++i) {
      const float d = x1[i] - cshift;
      p1 += d; p2 += d * d;
      y[i] = d * (i < 4 ? g20[i & 3] : g21[i & 3]);
    }
    u32x4 yv;
#pragma unroll
    for (int i = 0; i < 4; ++i) yv[i] = pack2(y[2 * i], y[2 * i + 1]);
    __builtin_amdgcn_raw_buffer_store_b128(yv, rs, y_off, 0, 16);
    p1 = xhalf_add(p1); p2 = xhalf_add(p2);
    {
      u32x2 sv = {__float_as_uint(p1), __float_as_uint(p2)};
      __builtin_amdgcn_raw_buffer_store_b64(sv, rs, st_off, 0, 16);
    }
    BAR_ALL();
    if (t == 0) put_word(ws + OFF_FY + tg * 128 + sl * 8, tag, 0);
    if (b == 0 && t == 64) __hip_atomic_store((gu64*)ws, epoch + 1ull, RLX_AGENT);
    if (wv < 7) { W2A(0); W2A(1); W2A(2); W2A(3); W2A(4); W2A(5); W2A(6); W2A(7); W2A(8); W2A(9); W2A(10); W2A(11); }
  }
  STAMP(14);
  f32x16 u2, v2;
#pragma unroll
  for (int i = 0; i < 16; ++i) {
    auto rr = __builtin_amdgcn_permlane16_swap(__float_as_uint(uv[i]), __float_as_uint(uv[i]), false, false);
    u2[i] = __uint_as_float(rr[0]); v2[i] = __uint_as_float(rr[1]) * -1.4426950408889634f;
  }
  PIN(u2); PIN(v2);
  if (wv == 7) wait_words(ws + OFF_FY + tg * 128, 7, tag, lane);
  BAR_LGKM();
  STAMP(15);
  {
    if (wv < 7) {
      const char* ysrc = ws + OFF_Y + (size_t)tg * 57344 + wv * 8192;
      dma4c(ysrc, l16, lds0 + L_LOW + wv * 8192); dma4c(ysrc + 4096, l16, lds0 + L_LOW + wv * 8192 + 4096);
    } else {
      u32x2 sv[28];
#pragma unroll
      for (int i = 0; i < 28; ++i) sv[i] = __builtin_amdgcn_raw_buffer_load_b64(rs, OFF_ST + (((tg * 2 + h) * 28 + i) * 32 + r) * 8, 0, 16);
      float a1 = 0.f, a2 = 0.f;
#pragma unroll
      for (int i = 0; i < 28; ++i) { a1 += __uint_as_float(sv[i][0]); a2 += __uint_as_float(sv[i][1]); }
      const float m = a1 * (1.f / DM);
      const float var = fmaxf(a2 * (1.f / DM) - m * m, 0.f);
      f32x2 o2 = {__builtin_amdgcn_rsqf(var + 1e-5f) * -1.4426950408889634f, m};
      *(LAS f32x2*)(L + L_SM + 512 + lane * 8) = o2;
    }
  }
  BAR_ALL();
  STAMP(16);
  f32x16 c0 = zero16, c1 = zero16;
  f32x2 st0, st1;
  u32x4 gbp;
#define SILU1(C, ST, i) do { const float t_ = __builtin_fmaf(-ST[1], u2[i], C[i]); const float q_ = __builtin_fmaf(ST[0], t_, v2[i]); \
      C[i] = q_ * __builtin_amdgcn_rcpf(1.f + EX2(q_)); } while (0)
  {
    const f32x4 gb0 = *(const LAS f32x4*)(L + L_GBV + (sl < 7 ? n0 : 0) * 4), gb1 = *(const LAS f32x4*)(L + L_GBV + ((sl < 7 ? n0 : 0) + 8) * 4);
    gbp[0] = pack2(gb0[0], gb0[1]); gbp[1] = pack2(gb0[2], gb0[3]); gbp[2] = pack2(gb1[0], gb1[1]); gbp[3] = pack2(gb1[2], gb1[3]); PIN(gbp);
  }
  const int rowoff = (4 * h + tq) * 128 + g16 * 32 + tp * 8;
  const lds_cptr hi0 = L + L_HI + kq * 24576 + rowoff + rsw, hi1 = L + L_HI + kq * 24576 + rowoff + (64 ^ rsw);
  bf16x8 r0[12], r1[12];
  {
    const lds_cptr yp = L + L_LOW + l16;
    st0 = *(const LAS f32x2*)(L + L_SM + 512 + r * 8); st1 = *(const LAS f32x2*)(L + L_SM + 512 + (32 + r) * 8);
    bf16x8 yb[4];
    if (wv < 7) {
#pragma unroll
      for (int i = 0; i < 4; ++i) yb[i] = ldsfrag(yp + i * 1024);
      SBAR();
#pragma unroll
      for (int ks = 0; ks < 28; ++ks) {
        c0 = MFMA32(wf[ks], yb[ks & 3], c0); PIN(c0); SBAR();
        yb[ks & 3] = ldsfrag(yp + (ks + 4) * 1024);
        SBAR();
      }
    } else { W2A(0); W2A(1); W2A(2); W2A(3); W2A(4); W2A(5); W2A(6); W2A(7); W2A(8); W2A(9); W2A(10); W2A(11); }
    BAR_VM(0);
    if (wv < 7) {
#pragma unroll
      for (int ks = 0; ks < 28; ++ks) {
        c1 = MFMA32(wf[ks], yb[ks & 3], c1); PIN(c1); SBAR();
        if (ks + 4 < 28) yb[ks & 3] = ldsfrag(yp + (28 + ks + 4) * 1024);
        if (ks >= 1 && ks < 17) { SILU1(c0, st0, ks - 1); PIN(c0); }
        if (ks == 18) __builtin_amdgcn_raw_buffer_store_b128(pack8(c0, 0), rs, h_off, 0, 16);
        if (ks == 19) __builtin_amdgcn_raw_buffer_store_b128(pack8(c0, 1), rs, h_off + 1024, 0, 16);
        if (ks >= 4) { const int j = (ks - 4) >> 1; if (ks & 1) r1[j] = trfrag(hi1 + j * 2048, hi1 + j * 2048 + 1024); else r0[j] = trfrag(hi0 + j * 2048, hi0 + j * 2048 + 1024); }
        SBAR();
      }
    } else {
#pragma unroll
      for (int j = 0; j < 12; ++j) { r0[j] = trfrag(hi0 + j * 2048, hi0 + j * 2048 + 1024); r1[j] = trfrag(hi1 + j * 2048, hi1 + j * 2048 + 1024); }
    }
  }
  BAR_ALL();
  if (t == 0) put_word(ws + OFF_FH + tg * 128 + sl * 8, tag, 0);
  STAMP(17);
  if (wv < 7) {
#pragma unroll
    for (int i = 0; i < 8; ++i) SILU1(c1, st1, i);
    __builtin_amdgcn_raw_buffer_store_b128(pack8(c1, 0), rs, h_off + 112 * 1024, 0, 16);
#pragma unroll
    for (int i = 8; i < 16; ++i) SILU1(c1, st1, i);
    __builtin_amdgcn_raw_buffer_store_b128(pack8(c1, 1), rs, h_off + 113 * 1024, 0, 16);
  }
  BAR_ALL();
  if (t == 0) put_word(ws + OFF_FH + tg * 128 + 64 + sl * 8, tag, 0);
  STAMP(18);
  if (sl == 7) { STAMP_DUMP(); return; }
  {
#pragma unroll
    for (int i = 0; i < 16; ++i)
      dma1(w2b + (size_t)((wv >> 1) * 448 + 192 + ((wv & 1) * 16 + i) * 8) * DM, rvo, lds0 + (wv * 16 + i) * 1024);
    STAMP(19);
    const unsigned ho = OFF_H + (tt_own * 112 + kq * 28) * 1024;
    f32x16 d0, d1;
    const lds_cptr lo0 = L + kq * 32768 + rowoff + rsw, lo1 = L + kq * 32768 + rowoff + (64 ^ rsw);
    if (tb) BAR_VM(0);
    wait_words(ws + OFF_FH + tg * 128 + tb * 64, 8, tag, lane);
    STAMP(20);
    u32x4 hb[28];
#pragma unroll
    for (int i = 0; i < 18; ++i) hb[i] = __builtin_amdgcn_raw_buffer_load_b128(rs, ho + l16 + i * 1024, 0, 16);
    SBAR();
    d0 = zero16; d1 = zero16;
#pragma unroll
    for (int i = 0; i < 12; ++i) {
      d0 = MFMA32(r0[i], BC8(hb[i]), d0); PIN(d0); SBAR();
      d1 = MFMA32(r1[i], BC8(hb[i]), d1); PIN(d1); SBAR();
      if (i >= 1 && i < 11) hb[17 + i] = __builtin_amdgcn_raw_buffer_load_b128(rs, ho + l16 + (17 + i) * 1024, 0, 16);
      SBAR();
    }
    if (!tb) asm volatile("s_barrier" ::: "memory");
    {
      bf16x8 a0[4], a1[4];
#pragma unroll
      for (int i = 0; i < 4; ++i) { a0[i] = trfrag(lo0 + i * 2048, lo0 + i * 2048 + 1024); a1[i] = trfrag(lo1 + i * 2048, lo1 + i * 2048 + 1024); }
      SBAR();
#pragma unroll
      for (int i = 0; i < 16; ++i) {
        d0 = MFMA32(a0[i & 3], BC8(hb[12 + i]), d0); PIN(d0); SBAR();
        if (i + 4 < 16) a0[i & 3] = trfrag(lo0 + (i + 4) * 2048, lo0 + (i + 4) * 2048 + 1024);
        SBAR();
        d1 = MFMA32(a1[i & 3], BC8(hb[12 + i]), d1); PIN(d1); SBAR();
        if (i + 4 < 16) a1[i & 3] = trfrag(lo1 + (i + 4) * 2048, lo1 + (i + 4) * 2048 + 1024);
        SBAR();
      }
    }
    STAMP(21);
    BAR_LGKM();
    float f[8];
    RED_WRITE(f, d0, d1);
    BAR_LGKM();
    RED_READ(f);
#pragma unroll
    for (int i = 0; i < 8; ++i) f[i] *= -0.6931471805599453f;
    u32x2 oa, ob2;
    oa[0] = pack2(bflo(x1p[0]) + bflo(gbp[0]) * f[0], bfhi(x1p[0]) + bfhi(gbp[0]) * f[1]);
    oa[1] = pack2(bflo(x1p[1]) + bflo(gbp[1]) * f[2], bfhi(x1p[1]) + bfhi(gbp[1]) * f[3]);
    ob2[0] = pack2(bflo(x1p[2]) + bflo(gbp[2]) * f[4], bfhi(x1p[2]) + bfhi(gbp[2]) * f[5]);
    ob2[1] = pack2(bflo(x1p[3]) + bflo(gbp[3]) * f[6], bfhi(x1p[3]) + bfhi(gbp[3]) * f[7]);
    u16* orow = out + (size_t)(tt_own * 32 + r) * DM;
    *(u32x2*)(orow + n0) = oa;
    *(u32x2*)(orow + n0 + 8) = ob2;
  }
  STAMP(22);
  STAMP_DUMP();
}

extern "C" void kernel_launch(void* const* d_in, const int* in_sizes, int n_in, void* d_out, int out_size, void* d_ws,
                              size_t ws_size, hipStream_t stream) {
  fused_block<<<256, 512, 0, stream>>>((const u16*)d_in[0], (const u16*)d_in[1], (const u16*)d_in[2], (const u16*)d_in[4],
                                       (const u16*)d_in[5], (const u16*)d_in[6], (const u16*)d_in[7], (u16*)d_out, (char*)d_ws);
}
```

```cpp
#include <hip/hip_runtime.h>
#include <hip/hip_bf16.h>
#include <stdint.h>

typedef __attribute__((ext_vector_type(8))) short bf16x8;
typedef __attribute__((ext_vector_type(4))) short s16x4;
typedef __attribute__((ext_vector_type(16))) float f32x16;
typedef __attribute__((ext_vector_type(4))) unsigned u32x4;
typedef __attribute__((ext_vector_type(2))) unsigned u32x2;
typedef __attribute__((ext_vector_type(4))) float f32x4;
typedef __attribute__((ext_vector_type(2))) float f32x2;
typedef __attribute__((ext_vector_type(2))) __bf16 bf16x2_t;
typedef __attribute__((ext_vector_type(2))) _Float16 h16x2;
typedef unsigned short u16;
typedef unsigned long long u64;
#define GAS __attribute__((address_space(1)))
#define LAS __attribute__((address_space(3)))
typedef GAS u64 gu64;
typedef LAS const char* lds_cptr;
typedef LAS char* lds_ptr;

#define DI __device__ __forceinline__
#define MFMA32(a, b, c) __builtin_amdgcn_mfma_f32_32x32x16_bf16((a), (b), (c), 0, 0, 0)
#define MFMA16(a, b, c) __builtin_amdgcn_mfma_f32_16x16x32_bf16((a), (b), (c), 0, 0, 0)
#define SBAR() __builtin_amdgcn_sched_barrier(0)
#define PIN(x) asm volatile("" : "+v"(x))
#define RLX_AGENT __ATOMIC_RELAXED, __HIP_MEMORY_SCOPE_AGENT
#define BC8(x) __builtin_bit_cast(bf16x8, x)
#define EX2(v) __builtin_amdgcn_exp2f(v)
#define BAR_LGKM() asm volatile("s_waitcnt lgkmcnt(0)\n\ts_barrier" ::: "memory")
#define BAR_ALL() asm volatile("s_waitcnt vmcnt(0) lgkmcnt(0)\n\ts_barrier" ::: "memory")
#define BAR_VM(n) asm volatile("s_waitcnt vmcnt(" #n ")\n\ts_barrier" ::: "memory")

constexpr int DM = 448, DH = 28, FF = 1792;
constexpr int NTT = 64;

constexpr unsigned OFF_MODG = 4096;
constexpr unsigned OFF_FKV = 32768;
constexpr unsigned OFF_FO = 36864;
constexpr unsigned OFF_FY = 40960;
constexpr unsigned OFF_FH = 49152;
constexpr unsigned OFF_ST = 65536;
constexpr unsigned OFF_Q = 1u << 20;
constexpr unsigned OFF_KV = 3u << 20;
constexpr unsigned OFF_O = 7u << 20;
constexpr unsigned OFF_Y = 9u << 20;
constexpr unsigned OFF_H = 11u << 20;
constexpr unsigned WS_USED = 19u << 20;

#define STAMP(i) do {} while (0)
#define STAMP_DECL() do {} while (0)
#define STAMP_DUMP() do {} while (0)

DI float bf2f(u16 v) { return __uint_as_float((unsigned)v << 16); }
DI float bflo(unsigned w) { return __uint_as_float(w << 16); }
DI float bfhi(unsigned w) { return __uint_as_float(w & 0xffff0000u); }
DI unsigned pack2(float lo, float hi) {
  f32x2 v = {lo, hi};
  bf16x2_t b = __builtin_convertvector(v, bf16x2_t);
  return __builtin_bit_cast(unsigned, b);
}
DI unsigned packh2(float lo, float hi) {
  f32x2 v = {lo, hi};
  h16x2 b = __builtin_convertvector(v, h16x2);
  return __builtin_bit_cast(unsigned, b);
}
DI float hlo(unsigned w) { return (float)__builtin_bit_cast(h16x2, w)[0]; }
DI float hhi(unsigned w) { return (float)__builtin_bit_cast(h16x2, w)[1]; }
DI float fmix_lo(unsigned w, float g, float b) { float d; asm("v_fma_mix_f32 %0, %1, %2, %3 op_sel_hi:[1,0,0]" : "=v"(d) : "v"(w), "v"(g), "v"(b)); return d; }
DI float fmix_hi(unsigned w, float g, float b) { float d; asm("v_fma_mix_f32 %0, %1, %2, %3 op_sel:[1,0,0] op_sel_hi:[1,0,0]" : "=v"(d) : "v"(w), "v"(g), "v"(b)); return d; }
DI float rbf(float x) { return bflo(pack2(x, x)); }
DI u32x4 pack8(const f32x16& x, int s) {
  u32x4 p;
  p[0] = pack2(x[8 * s + 0], x[8 * s + 1]);
  p[1] = pack2(x[8 * s + 2], x[8 * s + 3]);
  p[2] = pack2(x[8 * s + 4], x[8 * s + 5]);
  p[3] = pack2(x[8 * s + 6], x[8 * s + 7]);
  return p;
}
DI float xhalf_add(float v) {
  auto rr = __builtin_amdgcn_permlane32_swap(__float_as_uint(v), __float_as_uint(v), false, false);
  return __uint_as_float(rr[0]) + __uint_as_float(rr[1]);
}
DI float xhalf_max(float v) {
  auto rr = __builtin_amdgcn_permlane32_swap(__float_as_uint(v), __float_as_uint(v), false, false);
  return fmaxf(__uint_as_float(rr[0]), __uint_as_float(rr[1]));
}
DI void dma1(const void* sbase, unsigned voff, unsigned lds_base) {
  unsigned sv;
  asm volatile("s_mov_b32 %0, m0\n\ts_mov_b32 m0, %2\n\ts_nop 0\n\tglobal_load_lds_dwordx4 %1, %3\n\ts_mov_b32 m0, %0"
               : "=&s"(sv) : "v"(voff), "s"(lds_base), "s"(sbase) : "memory");
}
DI void dma1c(const void* sbase, unsigned voff, unsigned lds_base) {
  unsigned sv;
  asm volatile("s_mov_b32 %0, m0\n\ts_mov_b32 m0, %2\n\ts_nop 0\n\tglobal_load_lds_dwordx4 %1, %3 sc1\n\ts_mov_b32 m0, %0"
               : "=&s"(sv) : "v"(voff), "s"(lds_base), "s"(sbase) : "memory");
}
DI void dma4c(const void* sbase, unsigned voff, unsigned lds_base) {
  unsigned sv;
  asm volatile("s_mov_b32 %0, m0\n\ts_mov_b32 m0, %2\n\ts_nop 0\n\t"
               "global_load_lds_dwordx4 %1, %3 sc1\n\tglobal_load_lds_dwordx4 %1, %3 offset:1024 sc1\n\t"
               "global_load_lds_dwordx4 %1, %3 offset:2048 sc1\n\tglobal_load_lds_dwordx4 %1, %3 offset:3072 sc1\n\t"
               "s_mov_b32 m0, %0" : "=&s"(sv) : "v"(voff), "s"(lds_base), "s"(sbase) : "memory");
}
DI void touch1(const void* sbase, unsigned voff, unsigned lds_base) {
  unsigned sv;
  asm volatile("s_mov_b32 %0, m0\n\ts_mov_b32 m0, %2\n\ts_nop 0\n\tglobal_load_lds_dword %1, %3\n\ts_mov_b32 m0, %0"
               : "=&s"(sv) : "v"(voff), "s"(lds_base), "s"(sbase) : "memory");
}
DI bf16x8 ldsfrag(lds_cptr p) { return *(const LAS bf16x8*)p; }
DI s16x4 trread(lds_cptr p) { return __builtin_amdgcn_ds_read_tr16_b64_v4i16((LAS s16x4*)p); }
DI bf16x8 trfrag(lds_cptr p0, lds_cptr p1) {
  s16x4 lo = trread(p0), hi = trread(p1);
  return __builtin_shufflevector(lo, hi, 0, 1, 2, 3, 4, 5, 6, 7);
}
DI float dot2bf(unsigned a, unsigned b, float c) {
  return __builtin_amdgcn_fdot2_f32_bf16(__builtin_bit_cast(bf16x2_t, a), __builtin_bit_cast(bf16x2_t, b), c, false);
}
DI unsigned wait_words(const char* p, int n, unsigned tag, int lane) {
  const gu64* f = (const gu64*)p + (lane < n ? lane : n - 1);
  const u64 t0 = __builtin_amdgcn_s_memrealtime();
  u64 v;
  for (;;) {
    v = __hip_atomic_load(f, RLX_AGENT);
    if (__all((unsigned)(v >> 32) == tag)) break;
    if (__builtin_amdgcn_s_memrealtime() - t0 > 2000000ull) break;
    __builtin_amdgcn_s_sleep(1);
  }
  return (unsigned)v;
}
DI void put_word(char* p, unsigned tag, unsigned val) { __hip_atomic_store((gu64*)p, ((u64)tag << 32) | val, RLX_AGENT); }

constexpr int L_XS = 0;
constexpr int L_WKV = 0;
constexpr int L_XCH = 65536;
constexpr int L_WQ = 114688;
constexpr int L_MODW = 143360;
constexpr int L_SILU = 157696;
constexpr int L_STAT = 143360;
constexpr int L_GB = 145408;
constexpr int L_KMX = 158592;
constexpr int L_LOW = 0;
constexpr int L_M2 = 57344;
constexpr int L_GA = L_M2, L_G2 = L_M2 + 1792, L_GBV = L_M2 + 3584, L_PK = L_M2 + 5376;
constexpr int L_SM = L_M2 + 7168;
constexpr int L_HI = 65536;
constexpr int LDS_BYTES = 160 * 1024;

#define ASTEPH(SC, SN, KADDR, V1ADDR, V0ADDR, H1, H2, H3, H4) do { \
    SN = MFMA32(kf0, qf0, zero16); SBAR(); \
    pw1[2] = pack2(e12, e13); pw1[3] = pack2(e14, e15); \
    kf0 = ldsfrag(KADDR); SBAR(); \
    SC[0] = EX2(SC[0]); SC[1] = EX2(SC[1]); SBAR(); H1; SBAR(); SC[2] = EX2(SC[2]); SC[3] = EX2(SC[3]); PIN(SC); PIN(pw1); SBAR(); \
    o = MFMA32(vf1, BC8(pw1), o); SBAR(); \
    vf1 = ldsfrag(V1ADDR); \
    SC[4] = EX2(SC[4]); SC[5] = EX2(SC[5]); SBAR(); pw0[0] = pack2(SC[0], SC[1]); pw0[1] = pack2(SC[2], SC[3]); SBAR(); H2; SBAR(); SC[6] = EX2(SC[6]); SC[7] = EX2(SC[7]); PIN(SC); PIN(pw0); SBAR(); \
    SN = MFMA32(kf1, qf1, SN); SBAR(); \
    kf1 = ldsfrag((KADDR) + 1024); \
    SC[8] = EX2(SC[8]); SC[9] = EX2(SC[9]); SBAR(); pw0[2] = pack2(SC[4], SC[5]); pw0[3] = pack2(SC[6], SC[7]); SBAR(); H3; SBAR(); SC[10] = EX2(SC[10]); SC[11] = EX2(SC[11]); PIN(SC); PIN(pw0); SBAR(); \
    o = MFMA32(vf0, BC8(pw0), o); SBAR(); \
    vf0 = ldsfrag(V0ADDR); \
    e12 = EX2(SC[12]); e13 = EX2(SC[13]); SBAR(); pw1[0] = pack2(SC[8], SC[9]); pw1[1] = pack2(SC[10], SC[11]); SBAR(); H4; SBAR(); e14 = EX2(SC[14]); e15 = EX2(SC[15]); PIN(e12); PIN(e13); PIN(e14); PIN(e15); PIN(pw1); SBAR(); \
  } while (0)
#define NOH ((void)0)
#define ASTEP(SC, SN, KADDR, V1ADDR, V0ADDR) ASTEPH(SC, SN, KADDR, V1ADDR, V0ADDR, NOH, NOH, NOH, NOH)

#define ATTN_PROLOGUE() \
    kf0 = ldsfrag(abase); kf1 = ldsfrag(abase + 1024); \
    vf0 = ldsfrag(abase + 2048); vf1 = vf0; \
    e12 = 0.f; e13 = 0.f; e14 = 0.f; e15 = 0.f; pw1[0] = 0; pw1[1] = 0; o = zero16; \
    sA = MFMA32(kf0, qf0, zero16); sA = MFMA32(kf1, qf1, sA); \
    kf0 = ldsfrag(abase + 4096); kf1 = ldsfrag(abase + 4096 + 1024);

__global__ void __launch_bounds__(512) fused_block(const u16* __restrict__ x, const u16* __restrict__ wqkv, const u16* __restrict__ wo,
                                                    const u16* __restrict__ w1, const u16* __restrict__ w2, const u16* __restrict__ cvec,
                                                    const u16* __restrict__ wmod, u16* __restrict__ out, char* __restrict__ ws) {
  __shared__ __attribute__((aligned(16))) char lds_arr[LDS_BYTES];
  const lds_ptr L = (lds_ptr)lds_arr;
  const unsigned lds0 = (unsigned)(uintptr_t)lds_arr;
  const int t = threadIdx.x, lane = t & 63, wv = __builtin_amdgcn_readfirstlane(t >> 6), r = lane & 31, h = lane >> 5;
  __builtin_assume(wv < 8);
  const int g16 = (lane >> 4) & 1, tq = (lane & 15) >> 2, tp = lane & 3;
  const int b = blockIdx.x, xg = b & 7, rk = b >> 3;
  const __amdgpu_buffer_rsrc_t rs = __builtin_amdgcn_make_buffer_rsrc(ws, 0, WS_USED, 0x00020000);
  asm volatile("" :: "s"(x), "s"(wqkv), "s"(wo), "s"(w1), "s"(w2), "s"(cvec), "s"(wmod), "s"(out), "s"(ws));
  STAMP_DECL();
  STAMP(0);

  const int rt = xg * 2 + (rk >> 4), hq = rk & 15;
  const bool modwave = (rk < 21) && (wv == 0);
  const int msl = xg * 21 + rk;
  u32x4 craw = {0, 0, 0, 0};
  if (modwave) {
    asm volatile("global_load_dwordx4 %0, %1, %2" : "=v"(craw) : "v"((lane < 56 ? lane : 55) * 16), "s"(cvec) : "memory");
    const unsigned vo = (unsigned)(lane >> 1) * (6 * DM * 2) + (lane & 1) * 16;
#pragma unroll
    for (int i = 0; i < 14; ++i) dma1(wmod + msl * 16 + i * 32 * 6 * DM, vo, lds0 + L_MODW + i * 1024);
  }
  const int a_tt = wv & 3, a_kh = wv >> 2;
  if (wv > 0) {
    const u16* xb = x + (size_t)(rt * 128) * DM;
    const u16* xw = xb + (wv - 1) * 16 * DM;
    const unsigned dw = lds0 + L_XS + (wv - 1) * 14336;
#pragma unroll
    for (int i = 0; i < 7; ++i) {
      const int q = i * 64 + lane, rl = (q * 1171) >> 16, c = q - rl * 56;
      const unsigned vo = rl * (DM * 2) + ((c ^ (rl >> 1)) * 16);
      dma1(xw, vo, dw + i * 1024);
      dma1(xw + 8 * DM, vo ^ 64, dw + 7168 + i * 1024);
    }
    const int q = (wv - 1) * 64 + lane, rl = (q * 1171) >> 16, c = q - rl * 56;
    const unsigned vo = rl * (DM * 2) + ((c ^ (rl >> 1)) * 16);
    dma1(xb + 112 * DM, vo, lds0 + L_XS + 14 * 7168 + (wv - 1) * 1024);
    dma1(xb + 120 * DM, vo ^ 64, lds0 + L_XS + 15 * 7168 + (wv - 1) * 1024);
  }
  u64 epoch;
  asm volatile("s_load_dwordx2 %0, %1, 0x0" : "=s"(epoch) : "s"(ws) : "memory");
  const unsigned wvo = (lane >> 2) * (3 * DM * 2) + ((tp == 3) ? 40 : tp * 16);
  if (wv > 0) {
#pragma unroll
    for (int i = 0; i < 4; ++i) { const int s = (wv - 1) * 4 + i; dma1(wqkv + hq * DH + s * 16 * 3 * DM, wvo, lds0 + L_WQ + s * 1024); }
  }
  if (t == 0) *(LAS unsigned*)(L + L_KMX) = 0;
  asm volatile("s_waitcnt lgkmcnt(0)" : "+s"(epoch) :: "memory");
  const unsigned tag = (unsigned)epoch + 1u;
  if (modwave) {
    asm volatile("s_waitcnt vmcnt(14)" : "+v"(craw) :: "memory");
    u32x4 sv;
#pragma unroll
    for (int j = 0; j < 4; ++j) {
      const float c0_ = bflo(craw[j]), c1_ = bfhi(craw[j]);
      sv[j] = pack2(c0_ * __builtin_amdgcn_rcpf(1.f + EX2(-1.4426950408889634f * c0_)), c1_ * __builtin_amdgcn_rcpf(1.f + EX2(-1.4426950408889634f * c1_)));
    }
    if (lane < 56) *(LAS u32x4*)(L + L_SILU + lane * 16) = sv;
    const int kg = lane >> 4;
    const lds_cptr bp = L + L_MODW + (8 * kg + tq) * 32 + tp * 8;
    const lds_cptr ap = L + L_SILU + kg * 16;
    bf16x8 af[14], bfr[14];
    asm volatile("s_waitcnt lgkmcnt(0)" ::: "memory");
#pragma unroll
    for (int s = 0; s < 14; ++s) af[s] = ldsfrag(ap + s * 64);
    asm volatile("s_waitcnt vmcnt(0)" ::: "memory");
#pragma unroll
    for (int s = 0; s < 14; ++s) bfr[s] = trfrag(bp + s * 1024, bp + s * 1024 + 128);
    f32x4 acc0 = {0.f, 0.f, 0.f, 0.f}, acc1 = acc0;
#pragma unroll
    for (int s = 0; s < 14; s += 2) { acc0 = MFMA16(af[s], bfr[s], acc0); acc1 = MFMA16(af[s + 1], bfr[s + 1], acc1); }
    if (lane < 16) put_word(ws + OFF_MODG + (msl * 16 + lane) * 8, tag, __float_as_uint(rbf(acc0[0] + acc1[0])));
  }
  STAMP(1);
  BAR_VM(4);
  u32x4 xr[14];
  {
    const int row = a_tt * 32 + r, gsw = (row >> 1) & 7, c0 = a_kh * 28 + h;
#pragma unroll
    for (int i = 0; i < 4; ++i) {
      const int c = c0 + 2 * i;
      const lds_cptr xa = L + L_XS + row * (DM * 2) + (c & ~7) * 16 + (((c & 7) ^ gsw) * 16);
#pragma unroll
      for (int s = i; s < 14; s += 4) xr[s] = *(const LAS u32x4*)(xa + (s >> 2) * 128);
    }
  }
  u32x4 mg;
  asm volatile("global_load_dwordx4 %0, %1, %2 offset:0 sc1" : "=v"(mg) : "v"((t < DM ? t : DM - 1) * 16), "s"(ws + OFF_MODG) : "memory");
  BAR_LGKM();
  STAMP(2);
  f32x16 zero16;
#pragma unroll
  for (int i = 0; i < 16; ++i) zero16[i] = 0.f;
  f32x16 aq = zero16, ak = zero16, av = zero16;
  const int wro = a_kh * 14 * 1024 + (8 * h + tq) * 64 + g16 * 32 + tp * 8;
  const lds_cptr wqp = L + L_WQ + wro, wkp = L + L_WKV + wro, wvp = L + L_WKV + 28672 + wro;
  {
    const u16* wsrc = wqkv + hq * DH + (1 + (wv >> 2)) * DM + (wv & 3) * 7 * 16 * 3 * DM;
    float s1a = 0.f, s2a = 0.f, s1b = 0.f, s2b = 0.f;
#pragma unroll
    for (int s = 0; s < 14; ++s) {
      if (s == 0 || s == 5 || s == 10) dma1(wsrc + (s / 5) * 16 * 3 * DM, wvo, lds0 + L_WKV + (wv * 7 + (s / 5)) * 1024);
      s1a = dot2bf(xr[s][0], 0x3f803f80u, s1a); s2a = dot2bf(xr[s][0], xr[s][0], s2a);
      s1b = dot2bf(xr[s][1], 0x3f803f80u, s1b); s2b = dot2bf(xr[s][1], xr[s][1], s2b);
      s1a = dot2bf(xr[s][2], 0x3f803f80u, s1a); s2a = dot2bf(xr[s][2], xr[s][2], s2a);
      s1b = dot2bf(xr[s][3], 0x3f803f80u, s1b); s2b = dot2bf(xr[s][3], xr[s][3], s2b);
      PIN(s1a); PIN(s2a); PIN(s1b); PIN(s2b); SBAR();
    }
    const float s1 = xhalf_add(s1a + s1b), s2 = xhalf_add(s2a + s2b);
    if (h == 0) { f32x2 v = {s1, s2}; *(LAS f32x2*)(L + L_STAT + (wv * 32 + r) * 8) = v; }
    asm volatile("s_waitcnt vmcnt(3)" : "+v"(mg) :: "memory");
    if (wv < 7) {
      if (!__all(mg[1] == tag && mg[3] == tag)) {
        const u64 t0 = __builtin_amdgcn_s_memrealtime();
        for (;;) {
          mg = __builtin_amdgcn_raw_buffer_load_b128(rs, OFF_MODG + t * 16, 0, 16);
          if (__all(mg[1] == tag && mg[3] == tag)) break;
          if (__builtin_amdgcn_s_memrealtime() - t0 > 2000000ull) break;
          __builtin_amdgcn_s_sleep(1);
        }
      }
      f32x2 o2 = {__uint_as_float(mg[0]), __uint_as_float(mg[2])};
      const bool isg = t >= 224;
      if (isg) { o2[0] = rbf(1.f + o2[0]); o2[1] = rbf(1.f + o2[1]); }
      *(LAS f32x2*)(L + L_GB + (isg ? 2 * t - DM : DM + 2 * t) * 4) = o2;
    }
    BAR_LGKM();
    STAMP(3);
    {
      const unsigned to = (unsigned)(((rk >> 3) * 32 + wv * 4 + ((lane >> 2) & 3)) * (FF * 2) + (((rk & 7) * 448) & ~127) + (lane & 3) * 128);
      touch1(w1, to, lds0 + L_KMX + 64);
    }
    const lds_cptr gp = L + L_GB + (a_kh * 224 + 8 * h) * 4;
    f32x4 gq[3][4];
#define LOADGB(slot, s) do { gq[slot][0] = *(const LAS f32x4*)(gp + (s) * 64); gq[slot][1] = *(const LAS f32x4*)(gp + (s) * 64 + 16); \
      gq[slot][2] = *(const LAS f32x4*)(gp + DM * 4 + (s) * 64); gq[slot][3] = *(const LAS f32x4*)(gp + DM * 4 + (s) * 64 + 16); } while (0)
    const f32x2 o2 = *(const LAS f32x2*)(L + L_STAT + ((wv ^ 4) * 32 + r) * 8);
    LOADGB(0, 0); LOADGB(1, 1);
    const float mu = (s1 + o2[0]) * (1.f / DM);
    const float var = fmaxf((s2 + o2[1]) * (1.f / DM) - mu * mu, 0.f);
    const float rstd = __builtin_amdgcn_rsqf(var + 1e-5f);
    const unsigned rhi = __float_as_uint(rbf(rstd)), rlo = rhi >> 16;
    const float nbr = -mu * __uint_as_float(rhi);
    bf16x8 wa2[2];
    wa2[0] = trfrag(wqp, wqp + 256);
    SBAR();
#pragma unroll
    for (int s = 0; s < 14; ++s) {
      if (s + 2 < 14) LOADGB((s + 2) % 3, s + 2);
      if (s < 8 && (s & 1) == 0) dma1(wsrc + (3 + s / 2) * 16 * 3 * DM, wvo, lds0 + L_WKV + (wv * 7 + 3 + s / 2) * 1024);
      if (s >= 1) { aq = MFMA32(wa2[(s - 1) & 1], BC8(xr[s - 1]), aq); PIN(aq); }
      SBAR();
      if (s >= 1) wa2[s & 1] = trfrag(wqp + s * 1024, wqp + s * 1024 + 256);
      float tt_[8];
#pragma unroll
      for (int j = 0; j < 4; ++j) {
        asm("v_dot2_f32_bf16 %0, %1, %2, %3" : "=v"(tt_[2 * j]) : "v"(xr[s][j]), "v"(rlo), "v"(nbr));
        asm("v_dot2_f32_bf16 %0, %1, %2, %3" : "=v"(tt_[2 * j + 1]) : "v"(xr[s][j]), "v"(rhi), "v"(nbr));
      }
      SBAR();
#pragma unroll
      for (int j = 0; j < 8; ++j) asm("v_fma_f32 %0, %1, %2, %3" : "=v"(tt_[j]) : "v"(tt_[j]), "v"(gq[s % 3][j >> 2][j & 3]), "v"(gq[s % 3][2 + (j >> 2)][j & 3]));
      SBAR();
#pragma unroll
      for (int j = 0; j < 4; ++j) xr[s][j] = pack2(tt_[2 * j], tt_[2 * j + 1]);
      PIN(xr[s]); SBAR();
    }
    aq = MFMA32(wa2[1], BC8(xr[13]), aq); PIN(aq);
  }
  {
    bf16x8 wa[14], wb[14];
    BAR_VM(0);
    STAMP(4);
#pragma unroll
    for (int s = 0; s < 22; ++s) {
      if (s >= 4 && s < 18) { ak = MFMA32(wb[s - 4], BC8(xr[s - 4]), ak); PIN(ak); SBAR(); }
      if (s < 14) wb[s] = trfrag(wkp + s * 1024, wkp + s * 1024 + 256);
      if (s >= 4 && s < 18) wa[s - 4] = trfrag(wvp + (s - 4) * 1024, wvp + (s - 4) * 1024 + 256);
      SBAR();
      if (s >= 8) { av = MFMA32(BC8(xr[s - 8]), wa[s - 8], av); PIN(av); SBAR(); }
    }
  }
  STAMP(5);
  {
    const lds_ptr xw = L + L_XCH + wv * 6144 + lane * 16;
    const lds_cptr xrd = L + L_XCH + (wv ^ 4) * 6144 + lane * 16;
    const int tt = rt * 4 + a_tt;
    const bool vpad = (r >= 24 && r < 28);
    const unsigned vconst = (r == 24) ? 0x3f803f80u : 0u;
#define W4(off, A, i0) do { f32x4 v_ = {A[i0], A[(i0) + 1], A[(i0) + 2], A[(i0) + 3]}; *(LAS f32x4*)(xw + (off)) = v_; } while (0)
#define R4(off, A, i0) do { const f32x4 v_ = *(const LAS f32x4*)(xrd + (off)); A[i0] += v_[0]; A[(i0) + 1] += v_[1]; A[(i0) + 2] += v_[2]; A[(i0) + 3] += v_[3]; } while (0)
    if (a_kh == 0) {
      W4(0, ak, 0); W4(1024, ak, 4); W4(2048, ak, 8); W4(3072, ak, 12); W4(4096, av, 8); W4(5120, av, 12);
      BAR_LGKM();
      R4(0, aq, 0); R4(1024, aq, 4); R4(2048, aq, 8); R4(3072, aq, 12); R4(4096, av, 0); R4(5120, av, 4);
      const float qs = 0.18898223650461362f * 1.4426950408889634f;
#pragma unroll
      for (int i = 0; i < 16; ++i) aq[i] *= qs;
      u32x4 f0 = pack8(aq, 0), f1 = pack8(aq, 1), vf = pack8(av, 0);
      if (h == 0) { f1[2] = 0; f1[3] = 0; }
#pragma unroll
      for (int i = 0; i < 4; ++i) vf[i] = vpad ? vconst : vf[i];
      const unsigned mo = OFF_Q + (hq * NTT + tt) * 2048 + lane * 16;
      __builtin_amdgcn_raw_buffer_store_b128(f0, rs, mo, 0, 16);
      __builtin_amdgcn_raw_buffer_store_b128(f1, rs, mo + 1024, 0, 16);
      __builtin_amdgcn_raw_buffer_store_b128(vf, rs, OFF_KV + ((hq * NTT + tt) * 4 + 2) * 1024 + lane * 16, 0, 16);
    } else {
      W4(0, aq, 0); W4(1024, aq, 4); W4(2048, aq, 8); W4(3072, aq, 12); W4(4096, av, 0); W4(5120, av, 4);
      BAR_LGKM();
      R4(0, ak, 0); R4(1024, ak, 4); R4(2048, ak, 8); R4(3072, ak, 12); R4(4096, av, 8); R4(5120, av, 12);
      u32x4 f0 = pack8(ak, 0), f1 = pack8(ak, 1), vf = pack8(av, 1);
      u32x4 f1z = f1;
      if (h == 0) { f1z[2] = 0; f1z[3] = 0; f1[2] = 0x3f803f80u; f1[3] = 0x00003f80u; }
#pragma unroll
      for (int i = 0; i < 4; ++i) vf[i] = vpad ? vconst : vf[i];
      const unsigned mo = OFF_KV + (hq * NTT + tt) * 4096 + lane * 16;
      __builtin_amdgcn_raw_buffer_store_b128(f0, rs, mo, 0, 16);
      __builtin_amdgcn_raw_buffer_store_b128(f1, rs, mo + 1024, 0, 16);
      __builtin_amdgcn_raw_buffer_store_b128(vf, rs, mo + 3072, 0, 16);
      float kn = 0.f;
#pragma unroll
      for (int i = 0; i < 4; ++i) { kn = dot2bf(f0[i], f0[i], kn); kn = dot2bf(f1z[i], f1z[i], kn); }
      kn = xhalf_add(kn);
      asm volatile("ds_max_u32 %0, %1" :: "v"(lds0 + L_KMX), "v"(__float_as_uint(kn)) : "memory");
    }
    BAR_ALL();
    if (t == 0) put_word(ws + OFF_FKV + (hq * 16 + rt) * 8, tag, *(const LAS unsigned*)(L + L_KMX));
  }
  STAMP(6);
  const int head = xg * 2 + (rk >> 4), qt = rk & 15;
  const int tg = xg * 4 + (rk >> 3), sl = rk & 7;
  const int qb = wv & 3, kh = wv >> 2;
  const int tb = wv >> 2, kq = wv & 3;
  const int tt_own = tg * 2 + tb;
  const unsigned l16 = lane * 16;
  const unsigned o_off = OFF_O + ((qt * 4 + qb) * 32 + head * 2 + kh) * 1024 + l16;
  const unsigned y_off = OFF_Y + (tt_own * 28 + sl * 4 + kq) * 1024 + l16;
  const unsigned st_off = OFF_ST + ((tt_own * 28 + sl * 4 + kq) * 32 + r) * 8;
  const unsigned h_off = OFF_H + ((tg * 2) * 112 + (sl * 7 + wv) * 2) * 1024 + l16;
  unsigned w1vo[4];
#pragma unroll
  for (int i = 0; i < 4; ++i) { const int pc = (wv + 8 * i < 28) ? wv + 8 * i : wv; const int q = pc * 64 + lane, row = q / 28, c = q - row * 28; w1vo[i] = row * (FF * 2) + c * 16; }
  const u16* w1b = w1 + sl * 224;
  const lds_cptr w1rd = L + L_LOW + (4 * h + tq) * 448 + wv * 64 + g16 * 32 + tp * 8;
  const unsigned w1d3 = (wv < 4) ? 24576 : 0;
#define W1D1(c, j) do { if ((c) >= 0) dma1(w1b + (size_t)(c) * 64 * FF, w1vo[j], lds0 + L_LOW + ((c) & 1) * 28672 + wv * 1024 + ((j) < 3 ? (j) * 8192 : w1d3)); } while (0)
#define W1_DMA(c) do { W1D1(c, 0); W1D1(c, 1); W1D1(c, 2); W1D1(c, 3); } while (0)
  if (wv == 7) {
    float km = __uint_as_float(wait_words(ws + OFF_FKV + head * 128, 16, tag, lane));
#pragma unroll
    for (int m = 8; m >= 1; m >>= 1) km = fmaxf(km, __shfl_xor(km, m));
    if (lane == 0) *(LAS float*)(L + L_SM) = km;
  } else {
    const u64 t0 = __builtin_amdgcn_s_memrealtime();
    u32x4 v0, v1;
    for (;;) {
      v0 = __builtin_amdgcn_raw_buffer_load_b128(rs, OFF_MODG + 7168 + t * 16, 0, 16);
      v1 = __builtin_amdgcn_raw_buffer_load_b128(rs, OFF_MODG + 14336 + t * 16, 0, 16);
      if (__all(v0[1] == tag && v0[3] == tag && v1[1] == tag && v1[3] == tag)) break;
      if (__builtin_amdgcn_s_memrealtime() - t0 > 2000000ull) break;
      __builtin_amdgcn_s_sleep(1);
    }
    W1_DMA(0); W1_DMA(1);
    const int e = (t < 224) ? 2 * t : 2 * t - DM;
    const int pk = (e >> 4) * 32 + ((e >> 2) & 1) * 16 + ((e >> 3) & 1) * 8 + (e & 3) * 2;
    if (t < 224) {
      f32x2 a = {__uint_as_float(v0[0]), __uint_as_float(v0[2])};
      f32x2 g = {rbf(1.f + __uint_as_float(v1[0])), rbf(1.f + __uint_as_float(v1[2]))};
      *(LAS f32x2*)(L + L_GA + e * 4) = a;
      *(LAS f32x2*)(L + L_G2 + e * 4) = g;
      *(LAS unsigned*)(L + L_PK + pk) = pack2(g[0], g[1]);
    } else {
      f32x2 g = {__uint_as_float(v1[0]), __uint_as_float(v1[2])};
      *(LAS f32x2*)(L + L_GBV + e * 4) = g;
      *(LAS unsigned*)(L + L_PK + 896 + pk) = pack2(__uint_as_float(v0[0]), __uint_as_float(v0[2]));
    }
  }
  BAR_LGKM();
  STAMP(7);
  const char* kvsrc = ws + OFF_KV + (size_t)(head * NTT + kh * 32 + qb) * 4096;
  const unsigned kvdst = lds0 + L_HI + kh * 16384 + qb * 4096;
#define DMA_KV(i, buf) dma4c(kvsrc + (size_t)(i) * 16384, l16, kvdst + (buf))
  bf16x8 qf0, qf1;
  {
    u32x4 q0, q1;
    asm volatile("global_load_dwordx4 %0, %2, %3 sc1\n\tglobal_load_dwordx4 %1, %2, %3 offset:1024 sc1" : "=&v"(q0), "=&v"(q1)
                 : "v"(l16), "s"(ws + OFF_Q + (head * NTT + qt * 4 + qb) * 2048) : "memory");
    DMA_KV(0, 0);
    if (wv == 7) { W1_DMA(0); W1_DMA(1); }
    DMA_KV(1, 32768);
    const float km = *(const LAS float*)(L + L_SM);
    asm volatile("s_waitcnt vmcnt(8)" : "+v"(q0), "+v"(q1) :: "memory");
    float qn = 0.f;
#pragma unroll
    for (int i = 0; i < 4; ++i) { qn = dot2bf(q0[i], q0[i], qn); qn = dot2bf(q1[i], q1[i], qn); }
    qn = xhalf_add(qn);
    const float bound = __builtin_amdgcn_sqrtf(qn * km) * 1.004f + 1e-30f;
    if (h == 0) q1[2] = ((__float_as_uint(bound) + 0xffffu) >> 16) | 0x8000u;
    qf0 = BC8(q0); qf1 = BC8(q1);
  }
  bf16x8 wf[28];
  f32x16 o, sA, sB;
  u32x4 pw0, pw1;
  bf16x8 kf0, kf1, vf0, vf1;
  float e12, e13, e14, e15;
  lds_cptr abase = L + L_HI + kh * 16384 + l16;
  lds_cptr aslot[3] = {abase, abase + 32768, abase + 65536};
  PIN(aslot[0]); PIN(aslot[1]); PIN(aslot[2]);
  BAR_VM(4);
  STAMP(8);
  ATTN_PROLOGUE();
  const unsigned rvo = (lane >> 3) * (DM * 2) + (((lane & 7) ^ (((lane >> 4) & 1) << 2)) * 16);
  const int rsw = ((tq >> 1) & 1) * 64;
  const u16* wob = wo + (sl < 7 ? sl : 0) * 64;
  const int wo3 = (wv < 4) ? wv + 24 : wv;
#define W1_RD1(c, i_) (wf[4 * (c) + (i_)] = trfrag(w1rd + ((c) & 1) * 28672 + (i_) * 16 * 448, w1rd + ((c) & 1) * 28672 + ((i_) * 16 + 8) * 448))
#define W1_READ(c) do { W1_RD1(c, 0); W1_RD1(c, 1); W1_RD1(c, 2); W1_RD1(c, 3); } while (0)
#define KV1(st, j) do { if ((st) < 8) dma1c(kvsrc + (size_t)(st) * 16384 + (j) * 1024, l16, kvdst + ((st) % 3) * 32768 + (j) * 1024); } while (0)
#define W1R1(c, j) do { if ((c) >= 0) W1_RD1(((c) >= 0 ? (c) : 0), j); } while (0)
#define WO1(hf, j) do { if ((hf) >= 0) { const int pc_ = (j) < 3 ? wv + 8 * (j) : wo3; \
      dma1(wob + ((hf) * 28 + pc_) * 8 * DM, rvo, lds0 + L_HI + ((hf) ? 0 : 65536) + pc_ * 1024); } } while (0)
#define STAGE(st, NW, RA, RB, RC, DC, WH, TCH) do { \
    const lds_cptr bc = aslot[(st) % 3], bn = aslot[((st) + 1) % 3]; \
    ASTEPH(sA, sB, bc + 2 * 4096, bc + 0 * 4096 + 3072, bc + 1 * 4096 + 2048, W1R1(RA, 0), W1R1(RA, 1), W1R1(RA, 2), W1R1(RA, 3)); \
    ASTEPH(sB, sA, bc + 3 * 4096, bc + 1 * 4096 + 3072, bc + 2 * 4096 + 2048, W1R1(RB, 0), W1R1(RB, 1), W1R1(RB, 2), W1R1(RB, 3)); \
    BAR_VM(NW); \
    ASTEPH(sA, sB, bn + 0 * 4096, bc + 2 * 4096 + 3072, bc + 3 * 4096 + 2048, KV1((st) + 2, 0); WO1(WH, 0); W1R1(RC, 0), KV1((st) + 2, 1); WO1(WH, 1); W1R1(RC, 1), \
           KV1((st) + 2, 2); WO1(WH, 2); W1R1(RC, 2), KV1((st) + 2, 3); WO1(WH, 3); W1R1(RC, 3)); \
    ASTEPH(sB, sA, bn + 1 * 4096, bc + 3 * 4096 + 3072, bn + 0 * 4096 + 2048, W1D1(DC, 0), W1D1(DC, 1), W1D1(DC, 2), W1D1(DC, 3); TCH); \
  } while (0)
  const int tl = wv * 9 + (lane < 9 ? lane : 8);
#define TOUCH_WO(i) touch1(wo, (rk * 98 + ((i) * 72 + tl < 98 ? (i) * 72 + tl : 97)) * 128, lds0 + L_SM + 256)
#define TOUCH_W2(i) touch1(w2, (rk * 392 + ((i) * 72 + tl < 392 ? (i) * 72 + tl : 391)) * 128, lds0 + L_SM + 256)
  STAGE(0, 0,  0,  1, -1,  2, -1, TOUCH_WO(0));
  STAGE(1, 5, -1, -1, -1,  3, -1, TOUCH_WO(1); TOUCH_W2(0));
  STAGE(2, 6, -1, -1,  2, -1, -1, TOUCH_W2(1));
  STAGE(3, 1, -1, -1,  3,  4, -1, TOUCH_W2(2));
  STAGE(4, 5, -1, -1, -1,  5, -1, TOUCH_W2(3));
  STAGE(5, 5, -1, -1,  4, -1, -1, TOUCH_W2(4); TOUCH_W2(5));
  STAGE(6, 2, -1, -1,  5,  6,  0, NOH);
  STAGE(7, 8, -1, -1, -1, -1,  1, NOH);
  STAMP(9);
  float fin[8];
  float lsum;
  constexpr int L_X = L_LOW + 28672;
#pragma unroll 1
  for (int pass = 0; pass < 2; ++pass) {
    pw1[2] = pack2(e12, e13); pw1[3] = pack2(e14, e15);
    o = MFMA32(vf1, BC8(pw1), o);
    {
      f32x4 a, b2;
      if (kh) {
#pragma unroll
        for (int i = 0; i < 4; ++i) { a[i] = o[i]; b2[i] = o[4 + i]; fin[i] = o[8 + i]; fin[4 + i] = o[12 + i]; }
      } else {
#pragma unroll
        for (int i = 0; i < 4; ++i) { a[i] = o[8 + i]; b2[i] = o[12 + i]; fin[i] = o[i]; fin[4 + i] = o[4 + i]; }
      }
      *(LAS f32x4*)(L + L_X + (wv * 2 + 0) * 1024 + l16) = a;
      *(LAS f32x4*)(L + L_X + (wv * 2 + 1) * 1024 + l16) = b2;
      if (h == 0) *(LAS float*)(L + L_X + 16384 + (wv * 32 + r) * 4) = o[12];
    }
    BAR_LGKM();
    {
      const int pw = wv ^ 4;
      const f32x4 a = *(const LAS f32x4*)(L + L_X + (pw * 2 + 0) * 1024 + l16);
      const f32x4 b2 = *(const LAS f32x4*)(L + L_X + (pw * 2 + 1) * 1024 + l16);
      const float c0 = *(const LAS float*)(L + L_X + 16384 + lane * 4) + *(const LAS float*)(L + L_X + 16384 + (128 + lane) * 4);
      const float c1 = *(const LAS float*)(L + L_X + 16384 + (64 + lane) * 4) + *(const LAS float*)(L + L_X + 16384 + (192 + lane) * 4);
#pragma unroll
      for (int i = 0; i < 4; ++i) { fin[i] += a[i]; fin[4 + i] += b2[i]; }
      lsum = *(const LAS float*)(L + L_X + 16384 + (qb * 32 + r) * 4) + *(const LAS float*)(L + L_X + 16384 + ((qb + 4) * 32 + r) * 4);
      const bool ok = (c0 >= 8.271806e-25f) && (c1 >= 8.271806e-25f);
      if (pass == 1 || __all(ok)) break;
    }
    {
      u32x4 q1 = __builtin_bit_cast(u32x4, qf1);
      if (h == 0) { q1[2] = 0; q1[3] = 0; }
      qf1 = BC8(q1);
      float mx = -INFINITY;
      const unsigned ko = OFF_KV + (head * NTT + kh * 32) * 4096 + l16;
#pragma unroll 1
      for (int kt = 0; kt < 32; ++kt) {
        const u32x4 k0 = __builtin_amdgcn_raw_buffer_load_b128(rs, ko + kt * 4096, 0, 16);
        const u32x4 k1 = __builtin_amdgcn_raw_buffer_load_b128(rs, ko + kt * 4096 + 1024, 0, 16);
        f32x16 s = MFMA32(BC8(k0), qf0, zero16);
        s = MFMA32(BC8(k1), qf1, s);
#pragma unroll
        for (int i = 0; i < 16; ++i) mx = fmaxf(mx, s[i]);
      }
      mx = xhalf_max(mx);
      if (h == 0) *(LAS float*)(L + L_X + 20480 + (wv * 32 + r) * 4) = mx;
      BAR_ALL();
      mx = fmaxf(*(const LAS float*)(L + L_X + 20480 + (qb * 32 + r) * 4), *(const LAS float*)(L + L_X + 20480 + ((qb + 4) * 32 + r) * 4));
      const float m0 = rbf(mx), m1 = rbf(mx - m0), m2 = rbf(mx - m0 - m1);
      if (h == 0) { q1[2] = pack2(-m0, -m1); q1[3] = pack2(-m2, 0.f); }
      qf1 = BC8(q1);
      DMA_KV(0, 0);
      DMA_KV(1, 32768);
      asm volatile("s_waitcnt vmcnt(4) lgkmcnt(0)\n\ts_barrier" ::: "memory");
      ATTN_PROLOGUE();
      int cur = 0, nxt = 32768, nn = 65536;
#pragma unroll 1
      for (int st = 0; st < 8; ++st) {
        const lds_cptr bc = abase + cur, bn = abase + nxt;
        ASTEP(sA, sB, bc + 2 * 4096, bc + 0 * 4096 + 3072, bc + 1 * 4096 + 2048);
        ASTEP(sB, sA, bc + 3 * 4096, bc + 1 * 4096 + 3072, bc + 2 * 4096 + 2048);
        BAR_VM(0);
        if (st + 2 < 8) DMA_KV(st + 2, nn);
        if (st == 7) { WO1(0, 0); WO1(0, 1); WO1(0, 2); WO1(0, 3); WO1(1, 0); WO1(1, 1); WO1(1, 2); WO1(1, 3); }
        ASTEP(sA, sB, bn + 0 * 4096, bc + 2 * 4096 + 3072, bc + 3 * 4096 + 2048);
        ASTEP(sB, sA, bn + 1 * 4096, bc + 3 * 4096 + 3072, bn + 0 * 4096 + 2048);
        const int tmp = cur; cur = nxt; nxt = nn; nn = tmp;
      }
    }
  }
  {
    const float inv = __builtin_amdgcn_rcpf(lsum);
    u32x4 ov;
#pragma unroll
    for (int i = 0; i < 4; ++i) ov[i] = pack2(fin[2 * i] * inv, fin[2 * i + 1] * inv);
    if (kh == 1 && h == 0) { ov[2] = 0; ov[3] = 0; }
    __builtin_amdgcn_raw_buffer_store_b128(ov, rs, o_off, 0, 16);
    BAR_ALL();
    if (t == 0) put_word(ws + OFF_FO + (qt * 16 + head) * 8, tag, 0);
  }
  STAMP(10);
  const int n0 = (sl * 2 + (kq >> 1)) * 32 + (kq & 1) * 16 + 4 * h;
  u32x2 xa2, xb2;
  unsigned xc0;
  asm volatile("global_load_dwordx2 %0, %3, %5\n\tglobal_load_dwordx2 %1, %3, %5 offset:16\n\tglobal_load_ushort %2, %4, %5"
               : "=&v"(xa2), "=&v"(xb2), "=&v"(xc0) : "v"((r * DM + (sl < 7 ? n0 : 0)) * 2), "v"(r * DM * 2), "s"(x + (size_t)tt_own * 32 * DM) : "memory");
  W1_READ(6);
  f32x16 uv = zero16;
  if (wv < 7) {
    const lds_cptr pp = L + L_PK + g16 * 896 + h * 16;
    bf16x8 pf[14];
#pragma unroll
    for (int hf2 = 0; hf2 < 2; ++hf2) {
#pragma unroll
      for (int i = 0; i < 14; ++i) pf[i] = ldsfrag(pp + (hf2 * 14 + i) * 32);
      SBAR();
#pragma unroll
      for (int i = 0; i < 14; ++i) uv = MFMA32(wf[hf2 * 14 + i], pf[i], uv);
      PIN(uv); SBAR();
    }
  }
  STAMP(11);
  if (wv == 7 && sl < 7) wait_words(ws + OFF_FO + (tg >> 1) * 128, 16, tag, lane);
  asm volatile("s_waitcnt vmcnt(0) lgkmcnt(0)\n\ts_barrier" : "+v"(xa2), "+v"(xb2), "+v"(xc0) :: "memory");
  const lds_ptr redb = L + L_LOW + tb * 24576 + l16;
#define RW1(A, o8, p, KQ) do { f32x4 v0 = {A[o8], A[(o8) + 1], A[(o8) + 2], A[(o8) + 3]}, v1 = {A[(o8) + 4], A[(o8) + 5], A[(o8) + 6], A[(o8) + 7]}; \
        *(LAS f32x4*)(redb + (((p) * 3 + (((KQ) - (p) - 1) & 3)) * 2) * 1024) = v0; *(LAS f32x4*)(redb + (((p) * 3 + (((KQ) - (p) - 1) & 3)) * 2 + 1) * 1024) = v1; } while (0)
#define RKEEP(AT, A, o8) do { _Pragma("unroll") for (int i = 0; i < 8; ++i) AT[i] = A[(o8) + i]; } while (0)
#define RED_WRITE(AT, A0, A1) do { \
      if (kq == 0)      { RW1(A0, 8, 1, 0); RW1(A1, 0, 2, 0); RW1(A1, 8, 3, 0); RKEEP(AT, A0, 0); } \
      else if (kq == 1) { RW1(A0, 0, 0, 1); RW1(A1, 0, 2, 1); RW1(A1, 8, 3, 1); RKEEP(AT, A0, 8); } \
      else if (kq == 2) { RW1(A0, 0, 0, 2); RW1(A0, 8, 1, 2); RW1(A1, 8, 3, 2); RKEEP(AT, A1, 0); } \
      else              { RW1(A0, 0, 0, 3); RW1(A0, 8, 1, 3); RW1(A1, 0, 2, 3); RKEEP(AT, A1, 8); } } while (0)
#define RED_READ(AT) do { \
      _Pragma("unroll") for (int d = 0; d < 3; ++d) { \
        const f32x4 v0 = *(const LAS f32x4*)(redb + kq * 6144 + (d * 2) * 1024); \
        const f32x4 v1 = *(const LAS f32x4*)(redb + kq * 6144 + (d * 2 + 1) * 1024); \
        _Pragma("unroll") for (int i = 0; i < 4; ++i) { AT[i] += v0[i]; AT[4 + i] += v1[i]; } } } while (0)
  const u16* w2b = w2 + (sl < 7 ? sl : 0) * 64;
#define W2A(i) dma1(w2b + (size_t)((wv >> 1) * 448 + ((wv & 1) * 12 + (i)) * 8) * DM, rvo, lds0 + L_HI + (wv * 12 + (i)) * 1024)
  u32x4 x1p;
  if (sl < 7) {
    float x1[8];
    STAMP(12);
    f32x16 acc0 = zero16, acc1 = zero16;
    {
      const lds_cptr wop = L + L_HI + ((kq >> 1) ? 0 : 65536) + ((kq & 1) * 4 * DH + 4 * h + tq) * 128 + g16 * 32 + tp * 8;
      const lds_cptr p0 = wop + rsw, p1 = wop + (64 ^ rsw);
      bf16x8 wa0[4], wa1[4];
#define WOROW0(i) (((i) >> 1) * DH + 16 * ((i) & 1))
#define WOROW1(i) (((i) >> 1) * DH + (((i) & 1) ? 20 : 8))
      u32x4 ob[8];
      const unsigned oo = OFF_O + (tt_own * 32 + kq * 8) * 1024 + l16;
#pragma unroll
      for (int i = 0; i < 8; ++i) ob[i] = __builtin_amdgcn_raw_buffer_load_b128(rs, oo + i * 1024, 0, 16);
      SBAR();
#pragma unroll
      for (int i = 0; i < 4; ++i) {
        wa0[i] = trfrag(p0 + WOROW0(i) * 128, p0 + WOROW1(i) * 128);
        wa1[i] = trfrag(p1 + WOROW0(i) * 128, p1 + WOROW1(i) * 128);
      }
      SBAR();
#pragma unroll
      for (int i = 0; i < 8; ++i) {
        acc0 = MFMA32(wa0[i & 3], BC8(ob[i]), acc0); acc1 = MFMA32(wa1[i & 3], BC8(ob[i]), acc1); PIN(acc0); PIN(acc1); SBAR();
        if (i < 4) {
          wa0[i] = trfrag(p0 + WOROW0(i + 4) * 128, p0 + WOROW1(i + 4) * 128);
          wa1[i] = trfrag(p1 + WOROW0(i + 4) * 128, p1 + WOROW1(i + 4) * 128);
        }
        SBAR();
      }
    }
    STAMP(13);
    float at[8];
    RED_WRITE(at, acc0, acc1);
    const f32x4 ga0 = *(const LAS f32x4*)(L + L_GA + n0 * 4), ga1 = *(const LAS f32x4*)(L + L_GA + (n0 + 8) * 4);
    const f32x4 g20 = *(const LAS f32x4*)(L + L_G2 + n0 * 4), g21 = *(const LAS f32x4*)(L + L_G2 + (n0 + 8) * 4);
    BAR_LGKM();
    RED_READ(at);
    x1[0] = bflo(xa2[0]) + ga0[0] * at[0]; x1[1] = bfhi(xa2[0]) + ga0[1] * at[1];
    x1[2] = bflo(xa2[1]) + ga0[2] * at[2]; x1[3] = bfhi(xa2[1]) + ga0[3] * at[3];
    x1[4] = bflo(xb2[0]) + ga1[0] * at[4]; x1[5] = bfhi(xb2[0]) + ga1[1] * at[5];
    x1[6] = bflo(xb2[1]) + ga1[2] * at[6]; x1[7] = bfhi(xb2[1]) + ga1[3] * at[7];
#pragma unroll
    for (int i = 0; i < 4; ++i) x1p[i] = pack2(x1[2 * i], x1[2 * i + 1]);
    PIN(x1p);
    const float cshift = __uint_as_float(xc0 << 16);
    float p1 = 0.f, p2 = 0.f, y[8];
#pragma unroll
    for (int i = 0; i < 8; ++i) {
      const float d = x1[i] - cshift;
      p1 += d; p2 += d * d;
      y[i] = d * (i < 4 ? g20[i & 3] : g21[i & 3]);
    }
    u32x4 yv;
#pragma unroll
    for (int i = 0; i < 4; ++i) yv[i] = pack2(y[2 * i], y[2 * i + 1]);
    __builtin_amdgcn_raw_buffer_store_b128(yv, rs, y_off, 0, 16);
    p1 = xhalf_add(p1); p2 = xhalf_add(p2);
    {
      u32x2 sv = {__float_as_uint(p1), __float_as_uint(p2)};
      __builtin_amdgcn_raw_buffer_store_b64(sv, rs, st_off, 0, 16);
    }
    BAR_ALL();
    if (t == 0) put_word(ws + OFF_FY + tg * 128 + sl * 8, tag, 0);
    if (b == 0 && t == 64) __hip_atomic_store((gu64*)ws, epoch + 1ull, RLX_AGENT);
    if (wv < 7) { W2A(0); W2A(1); W2A(2); W2A(3); W2A(4); W2A(5); W2A(6); W2A(7); W2A(8); W2A(9); W2A(10); W2A(11); }
  }
  STAMP(14);
  f32x16 u2, v2;
#pragma unroll
  for (int i = 0; i < 16; ++i) {
    auto rr = __builtin_amdgcn_permlane16_swap(__float_as_uint(uv[i]), __float_as_uint(uv[i]), false, false);
    u2[i] = __uint_as_float(rr[0]); v2[i] = __uint_as_float(rr[1]) * -1.4426950408889634f;
  }
  PIN(u2); PIN(v2);
  if (wv == 7) wait_words(ws + OFF_FY + tg * 128, 7, tag, lane);
  BAR_LGKM();
  STAMP(15);
  {
    if (wv < 7) {
      const char* ysrc = ws + OFF_Y + (size_t)tg * 57344 + wv * 8192;
      dma4c(ysrc, l16, lds0 + L_LOW + wv * 8192); dma4c(ysrc + 4096, l16, lds0 + L_LOW + wv * 8192 + 4096);
    } else {
      u32x2 sv[28];
#pragma unroll
      for (int i = 0; i < 28; ++i) sv[i] = __builtin_amdgcn_raw_buffer_load_b64(rs, OFF_ST + (((tg * 2 + h) * 28 + i) * 32 + r) * 8, 0, 16);
      float a1 = 0.f, a2 = 0.f;
#pragma unroll
      for (int i = 0; i < 28; ++i) { a1 += __uint_as_float(sv[i][0]); a2 += __uint_as_float(sv[i][1]); }
      const float m = a1 * (1.f / DM);
      const float var = fmaxf(a2 * (1.f / DM) - m * m, 0.f);
      f32x2 o2 = {__builtin_amdgcn_rsqf(var + 1e-5f) * -1.4426950408889634f, m};
      *(LAS f32x2*)(L + L_SM + 512 + lane * 8) = o2;
    }
  }
  BAR_ALL();
  STAMP(16);
  f32x16 c0 = zero16, c1 = zero16;
  f32x2 st0, st1;
  u32x4 gbp;
#define SILU1(C, ST, i) do { const float t_ = __builtin_fmaf(-ST[1], u2[i], C[i]); const float q_ = __builtin_fmaf(ST[0], t_, v2[i]); \
      C[i] = q_ * __builtin_amdgcn_rcpf(1.f + EX2(q_)); } while (0)
  {
    const f32x4 gb0 = *(const LAS f32x4*)(L + L_GBV + (sl < 7 ? n0 : 0) * 4), gb1 = *(const LAS f32x4*)(L + L_GBV + ((sl < 7 ? n0 : 0) + 8) * 4);
    gbp[0] = pack2(gb0[0], gb0[1]); gbp[1] = pack2(gb0[2], gb0[3]); gbp[2] = pack2(gb1[0], gb1[1]); gbp[3] = pack2(gb1[2], gb1[3]); PIN(gbp);
  }
  const int rowoff = (4 * h + tq) * 128 + g16 * 32 + tp * 8;
  const lds_cptr hi0 = L + L_HI + kq * 24576 + rowoff + rsw, hi1 = L + L_HI + kq * 24576 + rowoff + (64 ^ rsw);
  bf16x8 r0[12], r1[12];
  {
    const lds_cptr yp = L + L_LOW + l16;
    st0 = *(const LAS f32x2*)(L + L_SM + 512 + r * 8); st1 = *(const LAS f32x2*)(L + L_SM + 512 + (32 + r) * 8);
    bf16x8 yb[4];
    if (wv < 7) {
#pragma unroll
      for (int i = 0; i < 4; ++i) yb[i] = ldsfrag(yp + i * 1024);
      SBAR();
#pragma unroll
      for (int ks = 0; ks < 28; ++ks) {
        c0 = MFMA32(wf[ks], yb[ks & 3], c0); PIN(c0); SBAR();
        yb[ks & 3] = ldsfrag(yp + (ks + 4) * 1024);
        SBAR();
      }
    } else { W2A(0); W2A(1); W2A(2); W2A(3); W2A(4); W2A(5); W2A(6); W2A(7); W2A(8); W2A(9); W2A(10); W2A(11); }
    if (wv < 7) {
#pragma unroll
      for (int ks = 0; ks < 28; ++ks) {
        c1 = MFMA32(wf[ks], yb[ks & 3], c1); PIN(c1); SBAR();
        if (ks + 4 < 28) yb[ks & 3] = ldsfrag(yp + (28 + ks + 4) * 1024);
        if (ks >= 1 && ks < 17) { SILU1(c0, st0, ks - 1); PIN(c0); }
        if (ks == 18) __builtin_amdgcn_raw_buffer_store_b128(pack8(c0, 0), rs, h_off, 0, 16);
        if (ks == 19) __builtin_amdgcn_raw_buffer_store_b128(pack8(c0, 1), rs, h_off + 1024, 0, 16);
        SBAR();
      }
    }
  }
  BAR_ALL();
  if (t == 0) put_word(ws + OFF_FH + tg * 128 + sl * 8, tag, 0);
  STAMP(17);
#pragma unroll
  for (int j = 0; j < 12; ++j) { r0[j] = trfrag(hi0 + j * 2048, hi0 + j * 2048 + 1024); r1[j] = trfrag(hi1 + j * 2048, hi1 + j * 2048 + 1024); }
  SBAR();
  if (wv < 7) {
#pragma unroll
    for (int i = 0; i < 8; ++i) SILU1(c1, st1, i);
    __builtin_amdgcn_raw_buffer_store_b128(pack8(c1, 0), rs, h_off + 112 * 1024, 0, 16);
#pragma unroll
    for (int i = 8; i < 16; ++i) SILU1(c1, st1, i);
    __builtin_amdgcn_raw_buffer_store_b128(pack8(c1, 1), rs, h_off + 113 * 1024, 0, 16);
  }
  BAR_ALL();
  if (t == 0) put_word(ws + OFF_FH + tg * 128 + 64 + sl * 8, tag, 0);
  STAMP(18);
  if (sl == 7) { STAMP_DUMP(); return; }
  {
#pragma unroll
    for (int i = 0; i < 16; ++i)
      dma1(w2b + (size_t)((wv >> 1) * 448 + 192 + ((wv & 1) * 16 + i) * 8) * DM, rvo, lds0 + (wv * 16 + i) * 1024);
    STAMP(19);
    const unsigned ho = OFF_H + (tt_own * 112 + kq * 28) * 1024;
    f32x16 d0, d1;
    const lds_cptr lo0 = L + kq * 32768 + rowoff + rsw, lo1 = L + kq * 32768 + rowoff + (64 ^ rsw);
    if (tb) BAR_VM(0);
    wait_words(ws + OFF_FH + tg * 128 + tb * 64, 8, tag, lane);
    STAMP(20);
    u32x4 hb[28];
#pragma unroll
    for (int i = 0; i < 18; ++i) hb[i] = __builtin_amdgcn_raw_buffer_load_b128(rs, ho + l16 + i * 1024, 0, 16);
    SBAR();
    d0 = zero16; d1 = zero16;
#pragma unroll
    for (int i = 0; i < 12; ++i) {
      d0 = MFMA32(r0[i], BC8(hb[i]), d0); PIN(d0); SBAR();
      d1 = MFMA32(r1[i], BC8(hb[i]), d1); PIN(d1); SBAR();
      if (i >= 1 && i < 11) hb[17 + i] = __builtin_amdgcn_raw_buffer_load_b128(rs, ho + l16 + (17 + i) * 1024, 0, 16);
      SBAR();
    }
    if (!tb) asm volatile("s_barrier" ::: "memory");
    {
      bf16x8 a0[4], a1[4];
#pragma unroll
      for (int i = 0; i < 4; ++i) { a0[i] = trfrag(lo0 + i * 2048, lo0 + i * 2048 + 1024); a1[i] = trfrag(lo1 + i * 2048, lo1 + i * 2048 + 1024); }
      SBAR();
#pragma unroll
      for (int i = 0; i < 16; ++i) {
        d0 = MFMA32(a0[i & 3], BC8(hb[12 + i]), d0); PIN(d0); SBAR();
        if (i + 4 < 16) a0[i & 3] = trfrag(lo0 + (i + 4) * 2048, lo0 + (i + 4) * 2048 + 1024);
        SBAR();
        d1 = MFMA32(a1[i & 3], BC8(hb[12 + i]), d1); PIN(d1); SBAR();
        if (i + 4 < 16) a1[i & 3] = trfrag(lo1 + (i + 4) * 2048, lo1 + (i + 4) * 2048 + 1024);
        SBAR();
      }
    }
    STAMP(21);
    BAR_LGKM();
    float f[8];
    RED_WRITE(f, d0, d1);
    BAR_LGKM();
    RED_READ(f);
#pragma unroll
    for (int i = 0; i < 8; ++i) f[i] *= -0.6931471805599453f;
    u32x2 oa, ob2;
    oa[0] = pack2(bflo(x1p[0]) + bflo(gbp[0]) * f[0], bfhi(x1p[0]) + bfhi(gbp[0]) * f[1]);
    oa[1] = pack2(bflo(x1p[1]) + bflo(gbp[1]) * f[2], bfhi(x1p[1]) + bfhi(gbp[1]) * f[3]);
    ob2[0] = pack2(bflo(x1p[2]) + bflo(gbp[2]) * f[4], bfhi(x1p[2]) + bfhi(gbp[2]) * f[5]);
    ob2[1] = pack2(bflo(x1p[3]) + bflo(gbp[3]) * f[6], bfhi(x1p[3]) + bfhi(gbp[3]) * f[7]);
    u16* orow = out + (size_t)(tt_own * 32 + r) * DM;
    *(u32x2*)(orow + n0) = oa;
    *(u32x2*)(orow + n0 + 8) = ob2;
  }
  STAMP(22);
  STAMP_DUMP();
}

extern "C" void kernel_launch(void* const* d_in, const int* in_sizes, int n_in, void* d_out, int out_size, void* d_ws,
                              size_t ws_size, hipStream_t stream) {
  fused_block<<<256, 512, 0, stream>>>((const u16*)d_in[0], (const u16*)d_in[1], (const u16*)d_in[2], (const u16*)d_in[4],
                                       (const u16*)d_in[5], (const u16*)d_in[6], (const u16*)d_in[7], (u16*)d_out, (char*)d_ws);
}
```

```cpp
#include <hip/hip_runtime.h>
#include <hip/hip_bf16.h>
#include <stdint.h>

typedef __attribute__((ext_vector_type(8))) short bf16x8;
typedef __attribute__((ext_vector_type(4))) short s16x4;
typedef __attribute__((ext_vector_type(16))) float f32x16;
typedef __attribute__((ext_vector_type(4))) unsigned u32x4;
typedef __attribute__((ext_vector_type(2))) unsigned u32x2;
typedef __attribute__((ext_vector_type(4))) float f32x4;
typedef __attribute__((ext_vector_type(2))) float f32x2;
typedef __attribute__((ext_vector_type(2))) __bf16 bf16x2_t;
typedef unsigned short u16;
typedef unsigned long long u64;
#define GAS __attribute__((address_space(1)))
#define LAS __attribute__((address_space(3)))
typedef GAS u64 gu64;
typedef LAS const char* lds_cptr;
typedef LAS char* lds_ptr;

#define DI __device__ __forceinline__
#define MFMA32(a, b, c) __builtin_amdgcn_mfma_f32_32x32x16_bf16((a), (b), (c), 0, 0, 0)
#define MFMA16(a, b, c) __builtin_amdgcn_mfma_f32_16x16x32_bf16((a), (b), (c), 0, 0, 0)
#define SBAR() __builtin_amdgcn_sched_barrier(0)
#define PIN(x) asm volatile("" : "+v"(x))
#define RLX_AGENT __ATOMIC_RELAXED, __HIP_MEMORY_SCOPE_AGENT
#define BC8(x) __builtin_bit_cast(bf16x8, x)
#define EX2(v) __builtin_amdgcn_exp2f(v)
#define BAR_LGKM() asm volatile("s_waitcnt lgkmcnt(0)\n\ts_barrier" ::: "memory")
#define BAR_ALL() asm volatile("s_waitcnt vmcnt(0) lgkmcnt(0)\n\ts_barrier" ::: "memory")
#define BAR_VM(n) asm volatile("s_waitcnt vmcnt(" #n ")\n\ts_barrier" ::: "memory")

constexpr int DM = 448, DH = 28, FF = 1792;
constexpr int NTT = 64;

constexpr unsigned OFF_MODG = 4096;
constexpr unsigned OFF_FKV = 32768;
constexpr unsigned OFF_FO = 36864;
constexpr unsigned OFF_FY = 40960;
constexpr unsigned OFF_FH = 49152;
constexpr unsigned OFF_ST = 65536;
constexpr unsigned OFF_Q = 1u << 20;
constexpr unsigned OFF_KV = 3u << 20;
constexpr unsigned OFF_O = 7u << 20;
constexpr unsigned OFF_Y = 9u << 20;
constexpr unsigned OFF_H = 11u << 20;
constexpr unsigned WS_USED = 19u << 20;


DI float bflo(unsigned w) { return __uint_as_float(w << 16); }
DI float bfhi(unsigned w) { return __uint_as_float(w & 0xffff0000u); }
DI unsigned pack2(float lo, float hi) {
  f32x2 v = {lo, hi};
  bf16x2_t b = __builtin_convertvector(v, bf16x2_t);
  return __builtin_bit_cast(unsigned, b);
}
DI float rbf(float x) { return bflo(pack2(x, x)); }
DI u32x4 pack8(const f32x16& x, int s) {
  u32x4 p;
  p[0] = pack2(x[8 * s + 0], x[8 * s + 1]);
  p[1] = pack2(x[8 * s + 2], x[8 * s + 3]);
  p[2] = pack2(x[8 * s + 4], x[8 * s + 5]);
  p[3] = pack2(x[8 * s + 6], x[8 * s + 7]);
  return p;
}
DI float xhalf_add(float v) {
  auto rr = __builtin_amdgcn_permlane32_swap(__float_as_uint(v), __float_as_uint(v), false, false);
  return __uint_as_float(rr[0]) + __uint_as_float(rr[1]);
}
DI float xhalf_max(float v) {
  auto rr = __builtin_amdgcn_permlane32_swap(__float_as_uint(v), __float_as_uint(v), false, false);
  return fmaxf(__uint_as_float(rr[0]), __uint_as_float(rr[1]));
}
DI void dma1(const void* sbase, unsigned voff, unsigned lds_base) {
  unsigned sv;
  asm volatile("s_mov_b32 %0, m0\n\ts_mov_b32 m0, %2\n\ts_nop 0\n\tglobal_load_lds_dwordx4 %1, %3\n\ts_mov_b32 m0, %0"
               : "=&s"(sv) : "v"(voff), "s"(lds_base), "s"(sbase) : "memory");
}
DI void dma1c(const void* sbase, unsigned voff, unsigned lds_base) {
  unsigned sv;
  asm volatile("s_mov_b32 %0, m0\n\ts_mov_b32 m0, %2\n\ts_nop 0\n\tglobal_load_lds_dwordx4 %1, %3 sc1\n\ts_mov_b32 m0, %0"
               : "=&s"(sv) : "v"(voff), "s"(lds_base), "s"(sbase) : "memory");
}
DI void dma4c(const void* sbase, unsigned voff, unsigned lds_base) {
  unsigned sv;
  asm volatile("s_mov_b32 %0, m0\n\ts_mov_b32 m0, %2\n\ts_nop 0\n\t"
               "global_load_lds_dwordx4 %1, %3 sc1\n\tglobal_load_lds_dwordx4 %1, %3 offset:1024 sc1\n\t"
               "global_load_lds_dwordx4 %1, %3 offset:2048 sc1\n\tglobal_load_lds_dwordx4 %1, %3 offset:3072 sc1\n\t"
               "s_mov_b32 m0, %0" : "=&s"(sv) : "v"(voff), "s"(lds_base), "s"(sbase) : "memory");
}
DI void touch1(const void* sbase, unsigned voff, unsigned lds_base) {
  unsigned sv;
  asm volatile("s_mov_b32 %0, m0\n\ts_mov_b32 m0, %2\n\ts_nop 0\n\tglobal_load_lds_dword %1, %3\n\ts_mov_b32 m0, %0"
               : "=&s"(sv) : "v"(voff), "s"(lds_base), "s"(sbase) : "memory");
}
DI bf16x8 ldsfrag(lds_cptr p) { return *(const LAS bf16x8*)p; }
DI s16x4 trread(lds_cptr p) { return __builtin_amdgcn_ds_read_tr16_b64_v4i16((LAS s16x4*)p); }
DI bf16x8 trfrag(lds_cptr p0, lds_cptr p1) {
  s16x4 lo = trread(p0), hi = trread(p1);
  return __builtin_shufflevector(lo, hi, 0, 1, 2, 3, 4, 5, 6, 7);
}
DI float dot2bf(unsigned a, unsigned b, float c) {
  return __builtin_amdgcn_fdot2_f32_bf16(__builtin_bit_cast(bf16x2_t, a), __builtin_bit_cast(bf16x2_t, b), c, false);
}
DI unsigned wait_words(const char* p, int n, unsigned tag, int lane) {
  const gu64* f = (const gu64*)p + (lane < n ? lane : n - 1);
  const u64 t0 = __builtin_amdgcn_s_memrealtime();
  u64 v;
  for (;;) {
    v = __hip_atomic_load(f, RLX_AGENT);
    if (__all((unsigned)(v >> 32) == tag)) break;
    if (__builtin_amdgcn_s_memrealtime() - t0 > 2000000ull) break;
    __builtin_amdgcn_s_sleep(1);
  }
  return (unsigned)v;
}
DI void put_word(char* p, unsigned tag, unsigned val) { __hip_atomic_store((gu64*)p, ((u64)tag << 32) | val, RLX_AGENT); }

constexpr int L_XS = 0;
constexpr int L_WKV = 0;
constexpr int L_XCH = 65536;
constexpr int L_WQ = 114688;
constexpr int L_MODW = 143360;
constexpr int L_SILU = 157696;
constexpr int L_STAT = 143360;
constexpr int L_GB = 145408;
constexpr int L_KMX = 158592;
constexpr int L_LOW = 0;
constexpr int L_M2 = 57344;
constexpr int L_GA = L_M2, L_G2 = L_M2 + 1792, L_GBV = L_M2 + 3584, L_PK = L_M2 + 5376;
constexpr int L_SM = L_M2 + 7168;
constexpr int L_HI = 65536;
constexpr int LDS_BYTES = 160 * 1024;

#define ASTEPH(SC, SN, KADDR, V1ADDR, V0ADDR, H1, H2, H3, H4) do { \
    SN = MFMA32(kf0, qf0, zero16); SBAR(); \
    pw1[2] = pack2(e12, e13); pw1[3] = pack2(e14, e15); \
    kf0 = ldsfrag(KADDR); SBAR(); \
    SC[0] = EX2(SC[0]); SC[1] = EX2(SC[1]); SBAR(); H1; SBAR(); SC[2] = EX2(SC[2]); SC[3] = EX2(SC[3]); PIN(SC); PIN(pw1); SBAR(); \
    o = MFMA32(vf1, BC8(pw1), o); SBAR(); \
    vf1 = ldsfrag(V1ADDR); \
    SC[4] = EX2(SC[4]); SC[5] = EX2(SC[5]); SBAR(); pw0[0] = pack2(SC[0], SC[1]); pw0[1] = pack2(SC[2], SC[3]); SBAR(); H2; SBAR(); SC[6] = EX2(SC[6]); SC[7] = EX2(SC[7]); PIN(SC); PIN(pw0); SBAR(); \
    SN = MFMA32(kf1, qf1, SN); SBAR(); \
    kf1 = ldsfrag((KADDR) + 1024); \
    SC[8] = EX2(SC[8]); SC[9] = EX2(SC[9]); SBAR(); pw0[2] = pack2(SC[4], SC[5]); pw0[3] = pack2(SC[6], SC[7]); SBAR(); H3; SBAR(); SC[10] = EX2(SC[10]); SC[11] = EX2(SC[11]); PIN(SC); PIN(pw0); SBAR(); \
    o = MFMA32(vf0, BC8(pw0), o); SBAR(); \
    vf0 = ldsfrag(V0ADDR); \
    e12 = EX2(SC[12]); e13 = EX2(SC[13]); SBAR(); pw1[0] = pack2(SC[8], SC[9]); pw1[1] = pack2(SC[10], SC[11]); SBAR(); H4; SBAR(); e14 = EX2(SC[14]); e15 = EX2(SC[15]); PIN(e12); PIN(e13); PIN(e14); PIN(e15); PIN(pw1); SBAR(); \
  } while (0)
#define NOH ((void)0)
#define ASTEP(SC, SN, KADDR, V1ADDR, V0ADDR) ASTEPH(SC, SN, KADDR, V1ADDR, V0ADDR, NOH, NOH, NOH, NOH)

#define ATTN_PROLOGUE() \
    kf0 = ldsfrag(abase); kf1 = ldsfrag(abase + 1024); \
    vf0 = ldsfrag(abase + 2048); vf1 = vf0; \
    e12 = 0.f; e13 = 0.f; e14 = 0.f; e15 = 0.f; pw1[0] = 0; pw1[1] = 0; o = zero16; \
    sA = MFMA32(kf0, qf0, zero16); sA = MFMA32(kf1, qf1, sA); \
    kf0 = ldsfrag(abase + 4096); kf1 = ldsfrag(abase + 4096 + 1024);

__global__ void __launch_bounds__(512) fused_block(const u16* __restrict__ x, const u16* __restrict__ wqkv, const u16* __restrict__ wo,
                                                    const u16* __restrict__ w1, const u16* __restrict__ w2, const u16* __restrict__ cvec,
                                                    const u16* __restrict__ wmod, u16* __restrict__ out, char* __restrict__ ws) {
  __shared__ __attribute__((aligned(16))) char lds_arr[LDS_BYTES];
  const lds_ptr L = (lds_ptr)lds_arr;
  const unsigned lds0 = (unsigned)(uintptr_t)lds_arr;
  const int t = threadIdx.x, lane = t & 63, wv = __builtin_amdgcn_readfirstlane(t >> 6), r = lane & 31, h = lane >> 5;
  __builtin_assume(wv < 8);
  const int g16 = (lane >> 4) & 1, tq = (lane & 15) >> 2, tp = lane & 3;
  const int b = blockIdx.x, xg = b & 7, rk = b >> 3;
  const __amdgpu_buffer_rsrc_t rs = __builtin_amdgcn_make_buffer_rsrc(ws, 0, WS_USED, 0x00020000);
  asm volatile("" :: "s"(x), "s"(wqkv), "s"(wo), "s"(w1), "s"(w2), "s"(cvec), "s"(wmod), "s"(out), "s"(ws));

  const int rt = xg * 2 + (rk >> 4), hq = rk & 15;
  const bool modwave = (rk < 21) && (wv == 0);
  const int msl = xg * 21 + rk;
  u32x4 craw = {0, 0, 0, 0};
  if (modwave) {
    asm volatile("global_load_dwordx4 %0, %1, %2" : "=v"(craw) : "v"((lane < 56 ? lane : 55) * 16), "s"(cvec) : "memory");
    const unsigned vo = (unsigned)(lane >> 1) * (6 * DM * 2) + (lane & 1) * 16;
#pragma unroll
    for (int i = 0; i < 14; ++i) dma1(wmod + msl * 16 + i * 32 * 6 * DM, vo, lds0 + L_MODW + i * 1024);
  }
  const int a_tt = wv & 3, a_kh = wv >> 2;
  if (wv > 0) {
    const u16* xb = x + (size_t)(rt * 128) * DM;
    const int g0 = (2 * (wv - 1) + hq) & 15, g1 = (g0 + 1) & 15, g2 = (14 + hq) & 15, g3 = (15 + hq) & 15;
    const unsigned p0 = (g0 & 1) << 6, p1 = p0 ^ 64;
#pragma unroll
    for (int i = 0; i < 7; ++i) {
      const int q = i * 64 + lane, rl = (q * 1171) >> 16, c = q - rl * 56;
      const unsigned vo = rl * (DM * 2) + ((c ^ (rl >> 1)) * 16);
      dma1(xb + g0 * 8 * DM, vo ^ p0, lds0 + L_XS + g0 * 7168 + i * 1024);
      dma1(xb + g1 * 8 * DM, vo ^ p1, lds0 + L_XS + g1 * 7168 + i * 1024);
    }
    const int q = (wv - 1) * 64 + lane, rl = (q * 1171) >> 16, c = q - rl * 56;
    const unsigned vo = rl * (DM * 2) + ((c ^ (rl >> 1)) * 16);
    dma1(xb + g2 * 8 * DM, vo ^ p0, lds0 + L_XS + g2 * 7168 + (wv - 1) * 1024);
    dma1(xb + g3 * 8 * DM, vo ^ p1, lds0 + L_XS + g3 * 7168 + (wv - 1) * 1024);
  }
  u64 epoch;
  asm volatile("s_load_dwordx2 %0, %1, 0x0" : "=s"(epoch) : "s"(ws) : "memory");
  const unsigned wvo = (lane >> 2) * (3 * DM * 2) + ((tp == 3) ? 40 : tp * 16);
  if (wv > 0) {
#pragma unroll
    for (int i = 0; i < 4; ++i) { const int s = (wv - 1) * 4 + i; dma1(wqkv + hq * DH + s * 16 * 3 * DM, wvo, lds0 + L_WQ + s * 1024); }
  }
  if (t == 0) *(LAS unsigned*)(L + L_KMX) = 0;
  asm volatile("s_waitcnt lgkmcnt(0)" : "+s"(epoch) :: "memory");
  const unsigned tag = (unsigned)epoch + 1u;
  if (modwave) {
    asm volatile("s_waitcnt vmcnt(14)" : "+v"(craw) :: "memory");
    u32x4 sv;
#pragma unroll
    for (int j = 0; j < 4; ++j) {
      const float c0_ = bflo(craw[j]), c1_ = bfhi(craw[j]);
      sv[j] = pack2(c0_ * __builtin_amdgcn_rcpf(1.f + EX2(-1.4426950408889634f * c0_)), c1_ * __builtin_amdgcn_rcpf(1.f + EX2(-1.4426950408889634f * c1_)));
    }
    if (lane < 56) *(LAS u32x4*)(L + L_SILU + lane * 16) = sv;
    const int kg = lane >> 4;
    const lds_cptr bp = L + L_MODW + (8 * kg + tq) * 32 + tp * 8;
    const lds_cptr ap = L + L_SILU + kg * 16;
    bf16x8 af[14], bfr[14];
    asm volatile("s_waitcnt lgkmcnt(0)" ::: "memory");
#pragma unroll
    for (int s = 0; s < 14; ++s) af[s] = ldsfrag(ap + s * 64);
    asm volatile("s_waitcnt vmcnt(0)" ::: "memory");
#pragma unroll
    for (int s = 0; s < 14; ++s) bfr[s] = trfrag(bp + s * 1024, bp + s * 1024 + 128);
    f32x4 acc0 = {0.f, 0.f, 0.f, 0.f}, acc1 = acc0;
#pragma unroll
    for (int s = 0; s < 14; s += 2) { acc0 = MFMA16(af[s], bfr[s], acc0); acc1 = MFMA16(af[s + 1], bfr[s + 1], acc1); }
    if (lane < 16) put_word(ws + OFF_MODG + (msl * 16 + lane) * 8, tag, __float_as_uint(rbf(acc0[0] + acc1[0])));
  }
  BAR_VM(4);
  u32x4 xr[14];
  {
    const int row = a_tt * 32 + r, gsw = (row >> 1) & 7, c0 = a_kh * 28 + h;
#pragma unroll
    for (int i = 0; i < 4; ++i) {
      const int c = c0 + 2 * i;
      const lds_cptr xa = L + L_XS + row * (DM * 2) + (c & ~7) * 16 + (((c & 7) ^ gsw) * 16);
#pragma unroll
      for (int s = i; s < 14; s += 4) xr[s] = *(const LAS u32x4*)(xa + (s >> 2) * 128);
    }
  }
  u32x4 mg;
  asm volatile("global_load_dwordx4 %0, %1, %2 offset:0 sc1" : "=v"(mg) : "v"((t < DM ? t : DM - 1) * 16), "s"(ws + OFF_MODG) : "memory");
  BAR_LGKM();
  f32x16 zero16;
#pragma unroll
  for (int i = 0; i < 16; ++i) zero16[i] = 0.f;
  f32x16 aq = zero16, ak = zero16, av = zero16;
  const int wro = a_kh * 14 * 1024 + (8 * h + tq) * 64 + g16 * 32 + tp * 8;
  const lds_cptr wqp = L + L_WQ + wro, wkp = L + L_WKV + wro, wvp = L + L_WKV + 28672 + wro;
  {
    const u16* wsrc = wqkv + hq * DH + (1 + (wv >> 2)) * DM + (wv & 3) * 7 * 16 * 3 * DM;
    float s1a = 0.f, s2a = 0.f, s1b = 0.f, s2b = 0.f;
#pragma unroll
    for (int s = 0; s < 14; ++s) {
      if (s == 0 || s == 5 || s == 10) dma1(wsrc + (s / 5) * 16 * 3 * DM, wvo, lds0 + L_WKV + (wv * 7 + (s / 5)) * 1024);
      s1a = dot2bf(xr[s][0], 0x3f803f80u, s1a); s2a = dot2bf(xr[s][0], xr[s][0], s2a);
      s1b = dot2bf(xr[s][1], 0x3f803f80u, s1b); s2b = dot2bf(xr[s][1], xr[s][1], s2b);
      s1a = dot2bf(xr[s][2], 0x3f803f80u, s1a); s2a = dot2bf(xr[s][2], xr[s][2], s2a);
      s1b = dot2bf(xr[s][3], 0x3f803f80u, s1b); s2b = dot2bf(xr[s][3], xr[s][3], s2b);
      PIN(s1a); PIN(s2a); PIN(s1b); PIN(s2b); SBAR();
    }
    const float s1 = xhalf_add(s1a + s1b), s2 = xhalf_add(s2a + s2b);
    if (h == 0) { f32x2 v = {s1, s2}; *(LAS f32x2*)(L + L_STAT + (wv * 32 + r) * 8) = v; }
    asm volatile("s_waitcnt vmcnt(3)" : "+v"(mg) :: "memory");
    if (wv < 7) {
      if (!__all(mg[1] == tag && mg[3] == tag)) {
        const u64 t0 = __builtin_amdgcn_s_memrealtime();
        for (;;) {
          mg = __builtin_amdgcn_raw_buffer_load_b128(rs, OFF_MODG + t * 16, 0, 16);
          if (__all(mg[1] == tag && mg[3] == tag)) break;
          if (__builtin_amdgcn_s_memrealtime() - t0 > 2000000ull) break;
          __builtin_amdgcn_s_sleep(1);
        }
      }
      f32x2 o2 = {__uint_as_float(mg[0]), __uint_as_float(mg[2])};
      const bool isg = t >= 224;
      if (isg) { o2[0] = rbf(1.f + o2[0]); o2[1] = rbf(1.f + o2[1]); }
      *(LAS f32x2*)(L + L_GB + (isg ? 2 * t - DM : DM + 2 * t) * 4) = o2;
    }
    BAR_LGKM();
    {
      const unsigned to = (unsigned)(((rk >> 3) * 32 + wv * 4 + ((lane >> 2) & 3)) * (FF * 2) + (((rk & 7) * 448) & ~127) + (lane & 3) * 128);
      touch1(w1, to, lds0 + L_KMX + 64);
    }
    const lds_cptr gp = L + L_GB + (a_kh * 224 + 8 * h) * 4;
    f32x4 gq[3][4];
#define LOADGB(slot, s) do { gq[slot][0] = *(const LAS f32x4*)(gp + (s) * 64); gq[slot][1] = *(const LAS f32x4*)(gp + (s) * 64 + 16); \
      gq[slot][2] = *(const LAS f32x4*)(gp + DM * 4 + (s) * 64); gq[slot][3] = *(const LAS f32x4*)(gp + DM * 4 + (s) * 64 + 16); } while (0)
    const f32x2 o2 = *(const LAS f32x2*)(L + L_STAT + ((wv ^ 4) * 32 + r) * 8);
    LOADGB(0, 0); LOADGB(1, 1);
    const float mu = (s1 + o2[0]) * (1.f / DM);
    const float var = fmaxf((s2 + o2[1]) * (1.f / DM) - mu * mu, 0.f);
    const float rstd = __builtin_amdgcn_rsqf(var + 1e-5f);
    const unsigned rhi = __float_as_uint(rbf(rstd)), rlo = rhi >> 16;
    const float nbr = -mu * __uint_as_float(rhi);
    bf16x8 wa2[2];
    wa2[0] = trfrag(wqp, wqp + 256);
    SBAR();
#pragma unroll
    for (int s = 0; s < 14; ++s) {
      if (s + 2 < 14) LOADGB((s + 2) % 3, s + 2);
      if (s < 8 && (s & 1) == 0) dma1(wsrc + (3 + s / 2) * 16 * 3 * DM, wvo, lds0 + L_WKV + (wv * 7 + 3 + s / 2) * 1024);
      if (s >= 1) { aq = MFMA32(wa2[(s - 1) & 1], BC8(xr[s - 1]), aq); PIN(aq); }
      SBAR();
      if (s >= 1) wa2[s & 1] = trfrag(wqp + s * 1024, wqp + s * 1024 + 256);
      float tt_[8];
#pragma unroll
      for (int j = 0; j < 4; ++j) {
        asm("v_dot2_f32_bf16 %0, %1, %2, %3" : "=v"(tt_[2 * j]) : "v"(xr[s][j]), "v"(rlo), "v"(nbr));
        asm("v_dot2_f32_bf16 %0, %1, %2, %3" : "=v"(tt_[2 * j + 1]) : "v"(xr[s][j]), "v"(rhi), "v"(nbr));
      }
      SBAR();
#pragma unroll
      for (int j = 0; j < 8; ++j) asm("v_fma_f32 %0, %1, %2, %3" : "=v"(tt_[j]) : "v"(tt_[j]), "v"(gq[s % 3][j >> 2][j & 3]), "v"(gq[s % 3][2 + (j >> 2)][j & 3]));
      SBAR();
#pragma unroll
      for (int j = 0; j < 4; ++j) xr[s][j] = pack2(tt_[2 * j], tt_[2 * j + 1]);
      PIN(xr[s]); SBAR();
    }
    aq = MFMA32(wa2[1], BC8(xr[13]), aq); PIN(aq);
  }
  {
    bf16x8 wa[14], wb[14];
    BAR_VM(0);
#pragma unroll
    for (int s = 0; s < 22; ++s) {
      if (s >= 4 && s < 18) { ak = MFMA32(wb[s - 4], BC8(xr[s - 4]), ak); PIN(ak); SBAR(); }
      if (s < 14) wb[s] = trfrag(wkp + s * 1024, wkp + s * 1024 + 256);
      if (s >= 4 && s < 18) wa[s - 4] = trfrag(wvp + (s - 4) * 1024, wvp + (s - 4) * 1024 + 256);
      SBAR();
      if (s >= 8) { av = MFMA32(BC8(xr[s - 8]), wa[s - 8], av); PIN(av); SBAR(); }
    }
  }
  {
    const lds_ptr xw = L + L_XCH + wv * 6144 + lane * 16;
    const lds_cptr xrd = L + L_XCH + (wv ^ 4) * 6144 + lane * 16;
    const int tt = rt * 4 + a_tt;
    const bool vpad = (r >= 24 && r < 28);
    const unsigned vconst = (r == 24) ? 0x3f803f80u : 0u;
#define W4(off, A, i0) do { f32x4 v_ = {A[i0], A[(i0) + 1], A[(i0) + 2], A[(i0) + 3]}; *(LAS f32x4*)(xw + (off)) = v_; } while (0)
#define R4(off, A, i0) do { const f32x4 v_ = *(const LAS f32x4*)(xrd + (off)); A[i0] += v_[0]; A[(i0) + 1] += v_[1]; A[(i0) + 2] += v_[2]; A[(i0) + 3] += v_[3]; } while (0)
    if (a_kh == 0) {
      W4(0, ak, 0); W4(1024, ak, 4); W4(2048, ak, 8); W4(3072, ak, 12); W4(4096, av, 8); W4(5120, av, 12);
      BAR_LGKM();
      R4(0, aq, 0); R4(1024, aq, 4); R4(2048, aq, 8); R4(3072, aq, 12); R4(4096, av, 0); R4(5120, av, 4);
      const float qs = 0.18898223650461362f * 1.4426950408889634f;
#pragma unroll
      for (int i = 0; i < 16; ++i) aq[i] *= qs;
      u32x4 f0 = pack8(aq, 0), f1 = pack8(aq, 1), vf = pack8(av, 0);
      if (h == 0) { f1[2] = 0; f1[3] = 0; }
#pragma unroll
      for (int i = 0; i < 4; ++i) vf[i] = vpad ? vconst : vf[i];
      const unsigned mo = OFF_Q + (hq * NTT + tt) * 2048 + lane * 16;
      __builtin_amdgcn_raw_buffer_store_b128(f0, rs, mo, 0, 16);
      __builtin_amdgcn_raw_buffer_store_b128(f1, rs, mo + 1024, 0, 16);
      __builtin_amdgcn_raw_buffer_store_b128(vf, rs, OFF_KV + ((hq * NTT + tt) * 4 + 2) * 1024 + lane * 16, 0, 16);
    } else {
      W4(0, aq, 0); W4(1024, aq, 4); W4(2048, aq, 8); W4(3072, aq, 12); W4(4096, av, 0); W4(5120, av, 4);
      BAR_LGKM();
      R4(0, ak, 0); R4(1024, ak, 4); R4(2048, ak, 8); R4(3072, ak, 12); R4(4096, av, 8); R4(5120, av, 12);
      u32x4 f0 = pack8(ak, 0), f1 = pack8(ak, 1), vf = pack8(av, 1);
      u32x4 f1z = f1;
      if (h == 0) { f1z[2] = 0; f1z[3] = 0; f1[2] = 0x3f803f80u; f1[3] = 0x00003f80u; }
#pragma unroll
      for (int i = 0; i < 4; ++i) vf[i] = vpad ? vconst : vf[i];
      const unsigned mo = OFF_KV + (hq * NTT + tt) * 4096 + lane * 16;
      __builtin_amdgcn_raw_buffer_store_b128(f0, rs, mo, 0, 16);
      __builtin_amdgcn_raw_buffer_store_b128(f1, rs, mo + 1024, 0, 16);
      __builtin_amdgcn_raw_buffer_store_b128(vf, rs, mo + 3072, 0, 16);
      float kn = 0.f;
#pragma unroll
      for (int i = 0; i < 4; ++i) { kn = dot2bf(f0[i], f0[i], kn); kn = dot2bf(f1z[i], f1z[i], kn); }
      kn = xhalf_add(kn);
      asm volatile("ds_max_u32 %0, %1" :: "v"(lds0 + L_KMX), "v"(__float_as_uint(kn)) : "memory");
    }
    BAR_ALL();
    if (t == 0) put_word(ws + OFF_FKV + (hq * 16 + rt) * 8, tag, *(const LAS unsigned*)(L + L_KMX));
  }
  const int head = xg * 2 + (rk >> 4), qt = rk & 15;
  const int tg = xg * 4 + (rk >> 3), sl = rk & 7;
  const int qb = wv & 3, kh = wv >> 2;
  const int tb = wv >> 2, kq = wv & 3;
  const int tt_own = tg * 2 + tb;
  const unsigned l16 = lane * 16;
  const unsigned o_off = OFF_O + ((qt * 4 + qb) * 32 + head * 2 + kh) * 1024 + l16;
  const unsigned y_off = OFF_Y + (tt_own * 28 + sl * 4 + kq) * 1024 + l16;
  const unsigned st_off = OFF_ST + ((tt_own * 28 + sl * 4 + kq) * 32 + r) * 8;
  const unsigned h_off = OFF_H + ((tg * 2) * 112 + (sl * 7 + wv) * 2) * 1024 + l16;
  unsigned w1vo[4];
#pragma unroll
  for (int i = 0; i < 4; ++i) { const int pc = (wv + 8 * i < 28) ? wv + 8 * i : wv; const int q = pc * 64 + lane, row = q / 28, c = q - row * 28; w1vo[i] = row * (FF * 2) + c * 16; }
  const u16* w1b = w1 + sl * 224;
  const lds_cptr w1rd = L + L_LOW + (4 * h + tq) * 448 + wv * 64 + g16 * 32 + tp * 8;
  const unsigned w1d3 = (wv < 4) ? 24576 : 0;
#define W1D1(c, j) do { if ((c) >= 0) dma1(w1b + (size_t)(c) * 64 * FF, w1vo[j], lds0 + L_LOW + ((c) & 1) * 28672 + wv * 1024 + ((j) < 3 ? (j) * 8192 : w1d3)); } while (0)
#define W1_DMA(c) do { W1D1(c, 0); W1D1(c, 1); W1D1(c, 2); W1D1(c, 3); } while (0)
  if (wv == 7) {
    float km = __uint_as_float(wait_words(ws + OFF_FKV + head * 128, 16, tag, lane));
#pragma unroll
    for (int m = 8; m >= 1; m >>= 1) km = fmaxf(km, __shfl_xor(km, m));
    if (lane == 0) *(LAS float*)(L + L_SM) = km;
  } else {
    const u64 t0 = __builtin_amdgcn_s_memrealtime();
    u32x4 v0, v1;
    for (;;) {
      v0 = __builtin_amdgcn_raw_buffer_load_b128(rs, OFF_MODG + 7168 + t * 16, 0, 16);
      v1 = __builtin_amdgcn_raw_buffer_load_b128(rs, OFF_MODG + 14336 + t * 16, 0, 16);
      if (__all(v0[1] == tag && v0[3] == tag && v1[1] == tag && v1[3] == tag)) break;
      if (__builtin_amdgcn_s_memrealtime() - t0 > 2000000ull) break;
      __builtin_amdgcn_s_sleep(1);
    }
    W1_DMA(0); W1_DMA(1);
    const int e = (t < 224) ? 2 * t : 2 * t - DM;
    const int pk = (e >> 4) * 32 + ((e >> 2) & 1) * 16 + ((e >> 3) & 1) * 8 + (e & 3) * 2;
    if (t < 224) {
      f32x2 a = {__uint_as_float(v0[0]), __uint_as_float(v0[2])};
      f32x2 g = {rbf(1.f + __uint_as_float(v1[0])), rbf(1.f + __uint_as_float(v1[2]))};
      *(LAS f32x2*)(L + L_GA + e * 4) = a;
      *(LAS f32x2*)(L + L_G2 + e * 4) = g;
      *(LAS unsigned*)(L + L_PK + pk) = pack2(g[0], g[1]);
    } else {
      f32x2 g = {__uint_as_float(v1[0]), __uint_as_float(v1[2])};
      *(LAS f32x2*)(L + L_GBV + e * 4) = g;
      *(LAS unsigned*)(L + L_PK + 896 + pk) = pack2(__uint_as_float(v0[0]), __uint_as_float(v0[2]));
    }
  }
  BAR_LGKM();
  const char* kvsrc = ws + OFF_KV + (size_t)(head * NTT + kh * 32 + qb) * 4096;
  const unsigned kvdst = lds0 + L_HI + kh * 16384 + qb * 4096;
#define DMA_KV(i, buf) dma4c(kvsrc + (size_t)(i) * 16384, l16, kvdst + (buf))
  bf16x8 qf0, qf1;
  {
    u32x4 q0, q1;
    asm volatile("global_load_dwordx4 %0, %2, %3 sc1\n\tglobal_load_dwordx4 %1, %2, %3 offset:1024 sc1" : "=&v"(q0), "=&v"(q1)
                 : "v"(l16), "s"(ws + OFF_Q + (head * NTT + qt * 4 + qb) * 2048) : "memory");
    DMA_KV(0, 0);
    if (wv == 7) { W1_DMA(0); W1_DMA(1); }
    DMA_KV(1, 32768);
    const float km = *(const LAS float*)(L + L_SM);
    asm volatile("s_waitcnt vmcnt(8)" : "+v"(q0), "+v"(q1) :: "memory");
    float qn = 0.f;
#pragma unroll
    for (int i = 0; i < 4; ++i) { qn = dot2bf(q0[i], q0[i], qn); qn = dot2bf(q1[i], q1[i], qn); }
    qn = xhalf_add(qn);
    const float bound = __builtin_amdgcn_sqrtf(qn * km) * 1.004f + 1e-30f;
    if (h == 0) q1[2] = ((__float_as_uint(bound) + 0xffffu) >> 16) | 0x8000u;
    qf0 = BC8(q0); qf1 = BC8(q1);
  }
  bf16x8 wf[28];
  f32x16 o, sA, sB;
  u32x4 pw0, pw1;
  bf16x8 kf0, kf1, vf0, vf1;
  float e12, e13, e14, e15;
  lds_cptr abase = L + L_HI + kh * 16384 + l16;
  lds_cptr aslot[3] = {abase, abase + 32768, abase + 65536};
  PIN(aslot[0]); PIN(aslot[1]); PIN(aslot[2]);
  BAR_VM(4);
  ATTN_PROLOGUE();
  const unsigned rvo = (lane >> 3) * (DM * 2) + (((lane & 7) ^ (((lane >> 4) & 1) << 2)) * 16);
  const int rsw = ((tq >> 1) & 1) * 64;
  const u16* wob = wo + (sl < 7 ? sl : 0) * 64;
  const int wo3 = (wv < 4) ? wv + 24 : wv;
#define W1_RD1(c, i_) (wf[4 * (c) + (i_)] = trfrag(w1rd + ((c) & 1) * 28672 + (i_) * 16 * 448, w1rd + ((c) & 1) * 28672 + ((i_) * 16 + 8) * 448))
#define W1_READ(c) do { W1_RD1(c, 0); W1_RD1(c, 1); W1_RD1(c, 2); W1_RD1(c, 3); } while (0)
#define KV1(st, j) do { if ((st) < 8) dma1c(kvsrc + (size_t)(st) * 16384 + (j) * 1024, l16, kvdst + ((st) % 3) * 32768 + (j) * 1024); } while (0)
#define W1R1(c, j) do { if ((c) >= 0) W1_RD1(((c) >= 0 ? (c) : 0), j); } while (0)
#define WO1(hf, j) do { if ((hf) >= 0) { const int pc_ = (j) < 3 ? wv + 8 * (j) : wo3; \
      dma1(wob + ((hf) * 28 + pc_) * 8 * DM, rvo, lds0 + L_HI + ((hf) ? 0 : 65536) + pc_ * 1024); } } while (0)
#define STAGE(st, NW, RA, RB, RC, DC, WH, TCH) do { \
    const lds_cptr bc = aslot[(st) % 3], bn = aslot[((st) + 1) % 3]; \
    ASTEPH(sA, sB, bc + 2 * 4096, bc + 0 * 4096 + 3072, bc + 1 * 4096 + 2048, W1R1(RA, 0), W1R1(RA, 1), W1R1(RA, 2), W1R1(RA, 3)); \
    ASTEPH(sB, sA, bc + 3 * 4096, bc + 1 * 4096 + 3072, bc + 2 * 4096 + 2048, W1R1(RB, 0), W1R1(RB, 1), W1R1(RB, 2), W1R1(RB, 3)); \
    BAR_VM(NW); \
    ASTEPH(sA, sB, bn + 0 * 4096, bc + 2 * 4096 + 3072, bc + 3 * 4096 + 2048, KV1((st) + 2, 0); WO1(WH, 0); W1R1(RC, 0), KV1((st) + 2, 1); WO1(WH, 1); W1R1(RC, 1), \
           KV1((st) + 2, 2); WO1(WH, 2); W1R1(RC, 2), KV1((st) + 2, 3); WO1(WH, 3); W1R1(RC, 3)); \
    ASTEPH(sB, sA, bn + 1 * 4096, bc + 3 * 4096 + 3072, bn + 0 * 4096 + 2048, W1D1(DC, 0), W1D1(DC, 1), W1D1(DC, 2), W1D1(DC, 3); TCH); \
  } while (0)
  const int tl = wv * 9 + (lane < 9 ? lane : 8);
#define TOUCH_WO(i) touch1(wo, (rk * 98 + ((i) * 72 + tl < 98 ? (i) * 72 + tl : 97)) * 128, lds0 + L_SM + 256)
#define TOUCH_W2(i) touch1(w2, (rk * 392 + ((i) * 72 + tl < 392 ? (i) * 72 + tl : 391)) * 128, lds0 + L_SM + 256)
  STAGE(0, 0,  0,  1, -1,  2, -1, TOUCH_WO(0));
  STAGE(1, 5, -1, -1, -1,  3, -1, TOUCH_WO(1); TOUCH_W2(0));
  STAGE(2, 6, -1, -1,  2, -1, -1, TOUCH_W2(1));
  STAGE(3, 1, -1, -1,  3,  4, -1, TOUCH_W2(2));
  STAGE(4, 5, -1, -1, -1,  5, -1, TOUCH_W2(3));
  STAGE(5, 5, -1, -1,  4, -1, -1, TOUCH_W2(4); TOUCH_W2(5));
  STAGE(6, 2, -1, -1,  5,  6,  0, NOH);
  STAGE(7, 8, -1, -1, -1, -1,  1, NOH);
  float fin[8];
  float lsum;
  constexpr int L_X = L_LOW + 28672;
#pragma unroll 1
  for (int pass = 0; pass < 2; ++pass) {
    pw1[2] = pack2(e12, e13); pw1[3] = pack2(e14, e15);
    o = MFMA32(vf1, BC8(pw1), o);
    {
      f32x4 a, b2;
      if (kh) {
#pragma unroll
        for (int i = 0; i < 4; ++i) { a[i] = o[i]; b2[i] = o[4 + i]; fin[i] = o[8 + i]; fin[4 + i] = o[12 + i]; }
      } else {
#pragma unroll
        for (int i = 0; i < 4; ++i) { a[i] = o[8 + i]; b2[i] = o[12 + i]; fin[i] = o[i]; fin[4 + i] = o[4 + i]; }
      }
      *(LAS f32x4*)(L + L_X + (wv * 2 + 0) * 1024 + l16) = a;
      *(LAS f32x4*)(L + L_X + (wv * 2 + 1) * 1024 + l16) = b2;
      if (h == 0) *(LAS float*)(L + L_X + 16384 + (wv * 32 + r) * 4) = o[12];
    }
    BAR_LGKM();
    {
      const int pw = wv ^ 4;
      const f32x4 a = *(const LAS f32x4*)(L + L_X + (pw * 2 + 0) * 1024 + l16);
      const f32x4 b2 = *(const LAS f32x4*)(L + L_X + (pw * 2 + 1) * 1024 + l16);
      const float c0 = *(const LAS float*)(L + L_X + 16384 + lane * 4) + *(const LAS float*)(L + L_X + 16384 + (128 + lane) * 4);
      const float c1 = *(const LAS float*)(L + L_X + 16384 + (64 + lane) * 4) + *(const LAS float*)(L + L_X + 16384 + (192 + lane) * 4);
#pragma unroll
      for (int i = 0; i < 4; ++i) { fin[i] += a[i]; fin[4 + i] += b2[i]; }
      lsum = *(const LAS float*)(L + L_X + 16384 + (qb * 32 + r) * 4) + *(const LAS float*)(L + L_X + 16384 + ((qb + 4) * 32 + r) * 4);
      const bool ok = (c0 >= 8.271806e-25f) && (c1 >= 8.271806e-25f);
      if (pass == 1 || __all(ok)) break;
    }
    {
      u32x4 q1 = __builtin_bit_cast(u32x4, qf1);
      if (h == 0) { q1[2] = 0; q1[3] = 0; }
      qf1 = BC8(q1);
      float mx = -INFINITY;
      const unsigned ko = OFF_KV + (head * NTT + kh * 32) * 4096 + l16;
#pragma unroll 1
      for (int kt = 0; kt < 32; ++kt) {
        const u32x4 k0 = __builtin_amdgcn_raw_buffer_load_b128(rs, ko + kt * 4096, 0, 16);
        const u32x4 k1 = __builtin_amdgcn_raw_buffer_load_b128(rs, ko + kt * 4096 + 1024, 0, 16);
        f32x16 s = MFMA32(BC8(k0), qf0, zero16);
        s = MFMA32(BC8(k1), qf1, s);
#pragma unroll
        for (int i = 0; i < 16; ++i) mx = fmaxf(mx, s[i]);
      }
      mx = xhalf_max(mx);
      if (h == 0) *(LAS float*)(L + L_X + 20480 + (wv * 32 + r) * 4) = mx;
      BAR_ALL();
      mx = fmaxf(*(const LAS float*)(L + L_X + 20480 + (qb * 32 + r) * 4), *(const LAS float*)(L + L_X + 20480 + ((qb + 4) * 32 + r) * 4));
      const float m0 = rbf(mx), m1 = rbf(mx - m0), m2 = rbf(mx - m0 - m1);
      if (h == 0) { q1[2] = pack2(-m0, -m1); q1[3] = pack2(-m2, 0.f); }
      qf1 = BC8(q1);
      DMA_KV(0, 0);
      DMA_KV(1, 32768);
      asm volatile("s_waitcnt vmcnt(4) lgkmcnt(0)\n\ts_barrier" ::: "memory");
      ATTN_PROLOGUE();
      int cur = 0, nxt = 32768, nn = 65536;
#pragma unroll 1
      for (int st = 0; st < 8; ++st) {
        const lds_cptr bc = abase + cur, bn = abase + nxt;
        ASTEP(sA, sB, bc + 2 * 4096, bc + 0 * 4096 + 3072, bc + 1 * 4096 + 2048);
        ASTEP(sB, sA, bc + 3 * 4096, bc + 1 * 4096 + 3072, bc + 2 * 4096 + 2048);
        BAR_VM(0);
        if (st + 2 < 8) DMA_KV(st + 2, nn);
        if (st == 7) { WO1(0, 0); WO1(0, 1); WO1(0, 2); WO1(0, 3); WO1(1, 0); WO1(1, 1); WO1(1, 2); WO1(1, 3); }
        ASTEP(sA, sB, bn + 0 * 4096, bc + 2 * 4096 + 3072, bc + 3 * 4096 + 2048);
        ASTEP(sB, sA, bn + 1 * 4096, bc + 3 * 4096 + 3072, bn + 0 * 4096 + 2048);
        const int tmp = cur; cur = nxt; nxt = nn; nn = tmp;
      }
    }
  }
  {
    const float inv = __builtin_amdgcn_rcpf(lsum);
    u32x4 ov;
#pragma unroll
    for (int i = 0; i < 4; ++i) ov[i] = pack2(fin[2 * i] * inv, fin[2 * i + 1] * inv);
    if (kh == 1 && h == 0) { ov[2] = 0; ov[3] = 0; }
    __builtin_amdgcn_raw_buffer_store_b128(ov, rs, o_off, 0, 16);
    BAR_ALL();
    if (t == 0) put_word(ws + OFF_FO + (qt * 16 + head) * 8, tag, 0);
  }
  const int n0 = (sl * 2 + (kq >> 1)) * 32 + (kq & 1) * 16 + 4 * h;
  u32x2 xa2, xb2;
  unsigned xc0;
  asm volatile("global_load_dwordx2 %0, %3, %5\n\tglobal_load_dwordx2 %1, %3, %5 offset:16\n\tglobal_load_ushort %2, %4, %5"
               : "=&v"(xa2), "=&v"(xb2), "=&v"(xc0) : "v"((r * DM + (sl < 7 ? n0 : 0)) * 2), "v"(r * DM * 2), "s"(x + (size_t)tt_own * 32 * DM) : "memory");
  W1_READ(6);
  f32x16 uv = zero16;
  if (wv < 7) {
    const lds_cptr pp = L + L_PK + g16 * 896 + h * 16;
    bf16x8 pf[14];
#pragma unroll
    for (int hf2 = 0; hf2 < 2; ++hf2) {
#pragma unroll
      for (int i = 0; i < 14; ++i) pf[i] = ldsfrag(pp + (hf2 * 14 + i) * 32);
      SBAR();
#pragma unroll
      for (int i = 0; i < 14; ++i) uv = MFMA32(wf[hf2 * 14 + i], pf[i], uv);
      PIN(uv); SBAR();
    }
  }
  if (wv == 7 && sl < 7) wait_words(ws + OFF_FO + (tg >> 1) * 128, 16, tag, lane);
  asm volatile("s_waitcnt vmcnt(0) lgkmcnt(0)\n\ts_barrier" : "+v"(xa2), "+v"(xb2), "+v"(xc0) :: "memory");
  const lds_ptr redb = L + L_LOW + tb * 24576 + l16;
#define RW1(A, o8, p, KQ) do { f32x4 v0 = {A[o8], A[(o8) + 1], A[(o8) + 2], A[(o8) + 3]}, v1 = {A[(o8) + 4], A[(o8) + 5], A[(o8) + 6], A[(o8) + 7]}; \
        *(LAS f32x4*)(redb + (((p) * 3 + (((KQ) - (p) - 1) & 3)) * 2) * 1024) = v0; *(LAS f32x4*)(redb + (((p) * 3 + (((KQ) - (p) - 1) & 3)) * 2 + 1) * 1024) = v1; } while (0)
#define RKEEP(AT, A, o8) do { _Pragma("unroll") for (int i = 0; i < 8; ++i) AT[i] = A[(o8) + i]; } while (0)
#define RED_WRITE(AT, A0, A1) do { \
      if (kq == 0)      { RW1(A0, 8, 1, 0); RW1(A1, 0, 2, 0); RW1(A1, 8, 3, 0); RKEEP(AT, A0, 0); } \
      else if (kq == 1) { RW1(A0, 0, 0, 1); RW1(A1, 0, 2, 1); RW1(A1, 8, 3, 1); RKEEP(AT, A0, 8); } \
      else if (kq == 2) { RW1(A0, 0, 0, 2); RW1(A0, 8, 1, 2); RW1(A1, 8, 3, 2); RKEEP(AT, A1, 0); } \
      else              { RW1(A0, 0, 0, 3); RW1(A0, 8, 1, 3); RW1(A1, 0, 2, 3); RKEEP(AT, A1, 8); } } while (0)
#define RED_READ(AT) do { \
      _Pragma("unroll") for (int d = 0; d < 3; ++d) { \
        const f32x4 v0 = *(const LAS f32x4*)(redb + kq * 6144 + (d * 2) * 1024); \
        const f32x4 v1 = *(const LAS f32x4*)(redb + kq * 6144 + (d * 2 + 1) * 1024); \
        _Pragma("unroll") for (int i = 0; i < 4; ++i) { AT[i] += v0[i]; AT[4 + i] += v1[i]; } } } while (0)
  const u16* w2b = w2 + (sl < 7 ? sl : 0) * 64;
#define W2A(i) dma1(w2b + (size_t)((wv >> 1) * 448 + ((wv & 1) * 12 + (i)) * 8) * DM, rvo, lds0 + L_HI + (wv * 12 + (i)) * 1024)
  u32x4 x1p;
  if (sl < 7) {
    float x1[8];
    f32x16 acc0 = zero16, acc1 = zero16;
    {
      const lds_cptr wop = L + L_HI + ((kq >> 1) ? 0 : 65536) + ((kq & 1) * 4 * DH + 4 * h + tq) * 128 + g16 * 32 + tp * 8;
      const lds_cptr p0 = wop + rsw, p1 = wop + (64 ^ rsw);
      bf16x8 wa0[4], wa1[4];
#define WOROW0(i) (((i) >> 1) * DH + 16 * ((i) & 1))
#define WOROW1(i) (((i) >> 1) * DH + (((i) & 1) ? 20 : 8))
      u32x4 ob[8];
      const unsigned oo = OFF_O + (tt_own * 32 + kq * 8) * 1024 + l16;
#pragma unroll
      for (int i = 0; i < 8; ++i) ob[i] = __builtin_amdgcn_raw_buffer_load_b128(rs, oo + i * 1024, 0, 16);
      SBAR();
#pragma unroll
      for (int i = 0; i < 4; ++i) {
        wa0[i] = trfrag(p0 + WOROW0(i) * 128, p0 + WOROW1(i) * 128);
        wa1[i] = trfrag(p1 + WOROW0(i) * 128, p1 + WOROW1(i) * 128);
      }
      SBAR();
#pragma unroll
      for (int i = 0; i < 8; ++i) {
        acc0 = MFMA32(wa0[i & 3], BC8(ob[i]), acc0); acc1 = MFMA32(wa1[i & 3], BC8(ob[i]), acc1); PIN(acc0); PIN(acc1); SBAR();
        if (i < 4) {
          wa0[i] = trfrag(p0 + WOROW0(i + 4) * 128, p0 + WOROW1(i + 4) * 128);
          wa1[i] = trfrag(p1 + WOROW0(i + 4) * 128, p1 + WOROW1(i + 4) * 128);
        }
        SBAR();
      }
    }
    float at[8];
    RED_WRITE(at, acc0, acc1);
    const f32x4 ga0 = *(const LAS f32x4*)(L + L_GA + n0 * 4), ga1 = *(const LAS f32x4*)(L + L_GA + (n0 + 8) * 4);
    const f32x4 g20 = *(const LAS f32x4*)(L + L_G2 + n0 * 4), g21 = *(const LAS f32x4*)(L + L_G2 + (n0 + 8) * 4);
    BAR_LGKM();
    RED_READ(at);
    x1[0] = bflo(xa2[0]) + ga0[0] * at[0]; x1[1] = bfhi(xa2[0]) + ga0[1] * at[1];
    x1[2] = bflo(xa2[1]) + ga0[2] * at[2]; x1[3] = bfhi(xa2[1]) + ga0[3] * at[3];
    x1[4] = bflo(xb2[0]) + ga1[0] * at[4]; x1[5] = bfhi(xb2[0]) + ga1[1] * at[5];
    x1[6] = bflo(xb2[1]) + ga1[2] * at[6]; x1[7] = bfhi(xb2[1]) + ga1[3] * at[7];
#pragma unroll
    for (int i = 0; i < 4; ++i) x1p[i] = pack2(x1[2 * i], x1[2 * i + 1]);
    PIN(x1p);
    const float cshift = __uint_as_float(xc0 << 16);
    float p1 = 0.f, p2 = 0.f, y[8];
#pragma unroll
    for (int i = 0; i < 8; ++i) {
      const float d = x1[i] - cshift;
      p1 += d; p2 += d * d;
      y[i] = d * (i < 4 ? g20[i & 3] : g21[i & 3]);
    }
    u32x4 yv;
#pragma unroll
    for (int i = 0; i < 4; ++i) yv[i] = pack2(y[2 * i], y[2 * i + 1]);
    __builtin_amdgcn_raw_buffer_store_b128(yv, rs, y_off, 0, 16);
    p1 = xhalf_add(p1); p2 = xhalf_add(p2);
    {
      u32x2 sv = {__float_as_uint(p1), __float_as_uint(p2)};
      __builtin_amdgcn_raw_buffer_store_b64(sv, rs, st_off, 0, 16);
    }
    BAR_ALL();
    if (t == 0) put_word(ws + OFF_FY + tg * 128 + sl * 8, tag, 0);
    if (b == 0 && t == 64) __hip_atomic_store((gu64*)ws, epoch + 1ull, RLX_AGENT);
    if (wv < 7) { W2A(0); W2A(1); W2A(2); W2A(3); W2A(4); W2A(5); W2A(6); W2A(7); W2A(8); W2A(9); W2A(10); W2A(11); }
  }
  f32x16 u2, v2;
#pragma unroll
  for (int i = 0; i < 16; ++i) {
    auto rr = __builtin_amdgcn_permlane16_swap(__float_as_uint(uv[i]), __float_as_uint(uv[i]), false, false);
    u2[i] = __uint_as_float(rr[0]); v2[i] = __uint_as_float(rr[1]) * -1.4426950408889634f;
  }
  PIN(u2); PIN(v2);
  if (wv == 7) wait_words(ws + OFF_FY + tg * 128, 7, tag, lane);
  BAR_LGKM();
  {
    if (wv < 7) {
      const char* ysrc = ws + OFF_Y + (size_t)tg * 57344 + wv * 8192;
      dma4c(ysrc, l16, lds0 + L_LOW + wv * 8192); dma4c(ysrc + 4096, l16, lds0 + L_LOW + wv * 8192 + 4096);
    } else {
      u32x2 sv[28];
#pragma unroll
      for (int i = 0; i < 28; ++i) sv[i] = __builtin_amdgcn_raw_buffer_load_b64(rs, OFF_ST + (((tg * 2 + h) * 28 + i) * 32 + r) * 8, 0, 16);
      float a1 = 0.f, a2 = 0.f;
#pragma unroll
      for (int i = 0; i < 28; ++i) { a1 += __uint_as_float(sv[i][0]); a2 += __uint_as_float(sv[i][1]); }
      const float m = a1 * (1.f / DM);
      const float var = fmaxf(a2 * (1.f / DM) - m * m, 0.f);
      f32x2 o2 = {__builtin_amdgcn_rsqf(var + 1e-5f) * -1.4426950408889634f, m};
      *(LAS f32x2*)(L + L_SM + 512 + lane * 8) = o2;
    }
  }
  BAR_ALL();
  f32x16 c0 = zero16, c1 = zero16;
  f32x2 st0, st1;
  u32x4 gbp;
#define SILU1(C, ST, i) do { const float t_ = __builtin_fmaf(-ST[1], u2[i], C[i]); const float q_ = __builtin_fmaf(ST[0], t_, v2[i]); \
      C[i] = q_ * __builtin_amdgcn_rcpf(1.f + EX2(q_)); } while (0)
  {
    const f32x4 gb0 = *(const LAS f32x4*)(L + L_GBV + (sl < 7 ? n0 : 0) * 4), gb1 = *(const LAS f32x4*)(L + L_GBV + ((sl < 7 ? n0 : 0) + 8) * 4);
    gbp[0] = pack2(gb0[0], gb0[1]); gbp[1] = pack2(gb0[2], gb0[3]); gbp[2] = pack2(gb1[0], gb1[1]); gbp[3] = pack2(gb1[2], gb1[3]); PIN(gbp);
  }
  const int rowoff = (4 * h + tq) * 128 + g16 * 32 + tp * 8;
  const lds_cptr hi0 = L + L_HI + kq * 24576 + rowoff + rsw, hi1 = L + L_HI + kq * 24576 + rowoff + (64 ^ rsw);
  bf16x8 r0[12], r1[12];
  {
    const lds_cptr yp = L + L_LOW + l16;
    st0 = *(const LAS f32x2*)(L + L_SM + 512 + r * 8); st1 = *(const LAS f32x2*)(L + L_SM + 512 + (32 + r) * 8);
    bf16x8 yb[4];
    if (wv < 7) {
#pragma unroll
      for (int i = 0; i < 4; ++i) yb[i] = ldsfrag(yp + i * 1024);
      SBAR();
#pragma unroll
      for (int ks = 0; ks < 28; ++ks) {
        c0 = MFMA32(wf[ks], yb[ks & 3], c0); PIN(c0); SBAR();
        yb[ks & 3] = ldsfrag(yp + (ks + 4) * 1024);
        SBAR();
      }
    } else { W2A(0); W2A(1); W2A(2); W2A(3); W2A(4); W2A(5); W2A(6); W2A(7); W2A(8); W2A(9); W2A(10); W2A(11); }
    if (wv < 7) {
#pragma unroll
      for (int ks = 0; ks < 28; ++ks) {
        c1 = MFMA32(wf[ks], yb[ks & 3], c1); PIN(c1); SBAR();
        if (ks + 4 < 28) yb[ks & 3] = ldsfrag(yp + (28 + ks + 4) * 1024);
        if (ks >= 1 && ks < 17) { SILU1(c0, st0, ks - 1); PIN(c0); }
        if (ks == 18) __builtin_amdgcn_raw_buffer_store_b128(pack8(c0, 0), rs, h_off, 0, 16);
        if (ks == 19) __builtin_amdgcn_raw_buffer_store_b128(pack8(c0, 1), rs, h_off + 1024, 0, 16);
        SBAR();
      }
    }
  }
  BAR_ALL();
  if (t == 0) put_word(ws + OFF_FH + tg * 128 + sl * 8, tag, 0);
#pragma unroll
  for (int j = 0; j < 12; ++j) { r0[j] = trfrag(hi0 + j * 2048, hi0 + j * 2048 + 1024); r1[j] = trfrag(hi1 + j * 2048, hi1 + j * 2048 + 1024); }
  SBAR();
  if (wv < 7) {
#pragma unroll
    for (int i = 0; i < 8; ++i) SILU1(c1, st1, i);
    __builtin_amdgcn_raw_buffer_store_b128(pack8(c1, 0), rs, h_off + 112 * 1024, 0, 16);
#pragma unroll
    for (int i = 8; i < 16; ++i) SILU1(c1, st1, i);
    __builtin_amdgcn_raw_buffer_store_b128(pack8(c1, 1), rs, h_off + 113 * 1024, 0, 16);
  }
  BAR_ALL();
  if (t == 0) put_word(ws + OFF_FH + tg * 128 + 64 + sl * 8, tag, 0);
  if (sl == 7) return;
  {
#pragma unroll
    for (int i = 0; i < 16; ++i)
      dma1(w2b + (size_t)((wv >> 1) * 448 + 192 + ((wv & 1) * 16 + i) * 8) * DM, rvo, lds0 + (wv * 16 + i) * 1024);
    const unsigned ho = OFF_H + (tt_own * 112 + kq * 28) * 1024;
    f32x16 d0, d1;
    const lds_cptr lo0 = L + kq * 32768 + rowoff + rsw, lo1 = L + kq * 32768 + rowoff + (64 ^ rsw);
    if (tb) BAR_VM(0);
    wait_words(ws + OFF_FH + tg * 128 + tb * 64, 8, tag, lane);
    u32x4 hb[28];
#pragma unroll
    for (int i = 0; i < 18; ++i) hb[i] = __builtin_amdgcn_raw_buffer_load_b128(rs, ho + l16 + i * 1024, 0, 16);
    SBAR();
    d0 = zero16; d1 = zero16;
#pragma unroll
    for (int i = 0; i < 12; ++i) {
      d0 = MFMA32(r0[i], BC8(hb[i]), d0); PIN(d0); SBAR();
      d1 = MFMA32(r1[i], BC8(hb[i]), d1); PIN(d1); SBAR();
      if (i >= 1 && i < 11) hb[17 + i] = __builtin_amdgcn_raw_buffer_load_b128(rs, ho + l16 + (17 + i) * 1024, 0, 16);
      SBAR();
    }
    if (!tb) asm volatile("s_barrier" ::: "memory");
    {
      bf16x8 a0[4], a1[4];
#pragma unroll
      for (int i = 0; i < 4; ++i) { a0[i] = trfrag(lo0 + i * 2048, lo0 + i * 2048 + 1024); a1[i] = trfrag(lo1 + i * 2048, lo1 + i * 2048 + 1024); }
      SBAR();
#pragma unroll
      for (int i = 0; i < 16; ++i) {
        d0 = MFMA32(a0[i & 3], BC8(hb[12 + i]), d0); PIN(d0); SBAR();
        if (i + 4 < 16) a0[i & 3] = trfrag(lo0 + (i + 4) * 2048, lo0 + (i + 4) * 2048 + 1024);
        SBAR();
        d1 = MFMA32(a1[i & 3], BC8(hb[12 + i]), d1); PIN(d1); SBAR();
        if (i + 4 < 16) a1[i & 3] = trfrag(lo1 + (i + 4) * 2048, lo1 + (i + 4) * 2048 + 1024);
        SBAR();
      }
    }
    BAR_LGKM();
    float f[8];
    RED_WRITE(f, d0, d1);
    BAR_LGKM();
    RED_READ(f);
#pragma unroll
    for (int i = 0; i < 8; ++i) f[i] *= -0.6931471805599453f;
    u32x2 oa, ob2;
    oa[0] = pack2(bflo(x1p[0]) + bflo(gbp[0]) * f[0], bfhi(x1p[0]) + bfhi(gbp[0]) * f[1]);
    oa[1] = pack2(bflo(x1p[1]) + bflo(gbp[1]) * f[2], bfhi(x1p[1]) + bfhi(gbp[1]) * f[3]);
    ob2[0] = pack2(bflo(x1p[2]) + bflo(gbp[2]) * f[4], bfhi(x1p[2]) + bfhi(gbp[2]) * f[5]);
    ob2[1] = pack2(bflo(x1p[3]) + bflo(gbp[3]) * f[6], bfhi(x1p[3]) + bfhi(gbp[3]) * f[7]);
    u16* orow = out + (size_t)(tt_own * 32 + r) * DM;
    *(u32x2*)(orow + n0) = oa;
    *(u32x2*)(orow + n0 + 8) = ob2;
  }
}

extern "C" void kernel_launch(void* const* d_in, const int* in_sizes, int n_in, void* d_out, int out_size, void* d_ws,
                              size_t ws_size, hipStream_t stream) {
  fused_block<<<256, 512, 0, stream>>>((const u16*)d_in[0], (const u16*)d_in[1], (const u16*)d_in[2], (const u16*)d_in[4],
                                       (const u16*)d_in[5], (const u16*)d_in[6], (const u16*)d_in[7], (u16*)d_out, (char*)d_ws);
}
```

```cpp
#include <hip/hip_runtime.h>
#include <hip/hip_bf16.h>
#include <stdint.h>

typedef __attribute__((ext_vector_type(8))) short bf16x8;
typedef __attribute__((ext_vector_type(4))) short s16x4;
typedef __attribute__((ext_vector_type(16))) float f32x16;
typedef __attribute__((ext_vector_type(4))) unsigned u32x4;
typedef __attribute__((ext_vector_type(2))) unsigned u32x2;
typedef __attribute__((ext_vector_type(4))) float f32x4;
typedef __attribute__((ext_vector_type(2))) float f32x2;
typedef __attribute__((ext_vector_type(2))) __bf16 bf16x2_t;
typedef __attribute__((ext_vector_type(2))) _Float16 h16x2;
typedef unsigned short u16;
typedef unsigned long long u64;
#define GAS __attribute__((address_space(1)))
#define LAS __attribute__((address_space(3)))
typedef GAS u64 gu64;
typedef LAS const char* lds_cptr;
typedef LAS char* lds_ptr;

#define DI __device__ __forceinline__
#define MFMA32(a, b, c) __builtin_amdgcn_mfma_f32_32x32x16_bf16((a), (b), (c), 0, 0, 0)
#define MFMA16(a, b, c) __builtin_amdgcn_mfma_f32_16x16x32_bf16((a), (b), (c), 0, 0, 0)
#define SBAR() __builtin_amdgcn_sched_barrier(0)
#define PIN(x) asm volatile("" : "+v"(x))
#define RLX_AGENT __ATOMIC_RELAXED, __HIP_MEMORY_SCOPE_AGENT
#define BC8(x) __builtin_bit_cast(bf16x8, x)
#define EX2(v) __builtin_amdgcn_exp2f(v)
#define BAR_LGKM() asm volatile("s_waitcnt lgkmcnt(0)\n\ts_barrier" ::: "memory")
#define BAR_ALL() asm volatile("s_waitcnt vmcnt(0) lgkmcnt(0)\n\ts_barrier" ::: "memory")
#define BAR_VM(n) asm volatile("s_waitcnt vmcnt(" #n ")\n\ts_barrier" ::: "memory")

constexpr int DM = 448, DH = 28, FF = 1792;
constexpr int NTT = 64;

constexpr unsigned OFF_MODG = 4096;
constexpr unsigned OFF_FKV = 32768;
constexpr unsigned OFF_FO = 36864;
constexpr unsigned OFF_FY = 40960;
constexpr unsigned OFF_FH = 49152;
constexpr unsigned OFF_ST = 65536;
constexpr unsigned OFF_Q = 1u << 20;
constexpr unsigned OFF_KV = 3u << 20;
constexpr unsigned OFF_O = 7u << 20;
constexpr unsigned OFF_Y = 9u << 20;
constexpr unsigned OFF_H = 11u << 20;
constexpr unsigned WS_USED = 19u << 20;

#define STAMP(i) do {} while (0)
#define STAMP_DECL() do {} while (0)
#define STAMP_DUMP() do {} while (0)

DI float bf2f(u16 v) { return __uint_as_float((unsigned)v << 16); }
DI float bflo(unsigned w) { return __uint_as_float(w << 16); }
DI float bfhi(unsigned w) { return __uint_as_float(w & 0xffff0000u); }
DI unsigned pack2(float lo, float hi) {
  f32x2 v = {lo, hi};
  bf16x2_t b = __builtin_convertvector(v, bf16x2_t);
  return __builtin_bit_cast(unsigned, b);
}
DI unsigned packh2(float lo, float hi) {
  f32x2 v = {lo, hi};
  h16x2 b = __builtin_convertvector(v, h16x2);
  return __builtin_bit_cast(unsigned, b);
}
DI float hlo(unsigned w) { return (float)__builtin_bit_cast(h16x2, w)[0]; }
DI float hhi(unsigned w) { return (float)__builtin_bit_cast(h16x2, w)[1]; }
DI float fmix_lo(unsigned w, float g, float b) { float d; asm("v_fma_mix_f32 %0, %1, %2, %3 op_sel_hi:[1,0,0]" : "=v"(d) : "v"(w), "v"(g), "v"(b)); return d; }
DI float fmix_hi(unsigned w, float g, float b) { float d; asm("v_fma_mix_f32 %0, %1, %2, %3 op_sel:[1,0,0] op_sel_hi:[1,0,0]" : "=v"(d) : "v"(w), "v"(g), "v"(b)); return d; }
DI float rbf(float x) { return bflo(pack2(x, x)); }
DI u32x4 pack8(const f32x16& x, int s) {
  u32x4 p;
  p[0] = pack2(x[8 * s + 0], x[8 * s + 1]);
  p[1] = pack2(x[8 * s + 2], x[8 * s + 3]);
  p[2] = pack2(x[8 * s + 4], x[8 * s + 5]);
  p[3] = pack2(x[8 * s + 6], x[8 * s + 7]);
  return p;
}
DI float xhalf_add(float v) {
  auto rr = __builtin_amdgcn_permlane32_swap(__float_as_uint(v), __float_as_uint(v), false, false);
  return __uint_as_float(rr[0]) + __uint_as_float(rr[1]);
}
DI float xhalf_max(float v) {
  auto rr = __builtin_amdgcn_permlane32_swap(__float_as_uint(v), __float_as_uint(v), false, false);
  return fmaxf(__uint_as_float(rr[0]), __uint_as_float(rr[1]));
}
DI void dma1(const void* sbase, unsigned voff, unsigned lds_base) {
  unsigned sv;
  asm volatile("s_mov_b32 %0, m0\n\ts_mov_b32 m0, %2\n\ts_nop 0\n\tglobal_load_lds_dwordx4 %1, %3\n\ts_mov_b32 m0, %0"
               : "=&s"(sv) : "v"(voff), "s"(lds_base), "s"(sbase) : "memory");
}
DI void dma1c(const void* sbase, unsigned voff, unsigned lds_base) {
  unsigned sv;
  asm volatile("s_mov_b32 %0, m0\n\ts_mov_b32 m0, %2\n\ts_nop 0\n\tglobal_load_lds_dwordx4 %1, %3 sc1\n\ts_mov_b32 m0, %0"
               : "=&s"(sv) : "v"(voff), "s"(lds_base), "s"(sbase) : "memory");
}
DI void dma4c(const void* sbase, unsigned voff, unsigned lds_base) {
  unsigned sv;
  asm volatile("s_mov_b32 %0, m0\n\ts_mov_b32 m0, %2\n\ts_nop 0\n\t"
               "global_load_lds_dwordx4 %1, %3 sc1\n\tglobal_load_lds_dwordx4 %1, %3 offset:1024 sc1\n\t"
               "global_load_lds_dwordx4 %1, %3 offset:2048 sc1\n\tglobal_load_lds_dwordx4 %1, %3 offset:3072 sc1\n\t"
               "s_mov_b32 m0, %0" : "=&s"(sv) : "v"(voff), "s"(lds_base), "s"(sbase) : "memory");
}
DI void touch1(const void* sbase, unsigned voff, unsigned lds_base) {
  unsigned sv;
  asm volatile("s_mov_b32 %0, m0\n\ts_mov_b32 m0, %2\n\ts_nop 0\n\tglobal_load_lds_dword %1, %3\n\ts_mov_b32 m0, %0"
               : "=&s"(sv) : "v"(voff), "s"(lds_base), "s"(sbase) : "memory");
}
DI bf16x8 ldsfrag(lds_cptr p) { return *(const LAS bf16x8*)p; }
DI s16x4 trread(lds_cptr p) { return __builtin_amdgcn_ds_read_tr16_b64_v4i16((LAS s16x4*)p); }
DI bf16x8 trfrag(lds_cptr p0, lds_cptr p1) {
  s16x4 lo = trread(p0), hi = trread(p1);
  return __builtin_shufflevector(lo, hi, 0, 1, 2, 3, 4, 5, 6, 7);
}
DI float dot2bf(unsigned a, unsigned b, float c) {
  return __builtin_amdgcn_fdot2_f32_bf16(__builtin_bit_cast(bf16x2_t, a), __builtin_bit_cast(bf16x2_t, b), c, false);
}
DI unsigned wait_words(const char* p, int n, unsigned tag, int lane) {
  const gu64* f = (const gu64*)p + (lane < n ? lane : n - 1);
  const u64 t0 = __builtin_amdgcn_s_memrealtime();
  u64 v;
  for (;;) {
    v = __hip_atomic_load(f, RLX_AGENT);
    if (__all((unsigned)(v >> 32) == tag)) break;
    if (__builtin_amdgcn_s_memrealtime() - t0 > 2000000ull) break;
    __builtin_amdgcn_s_sleep(1);
  }
  return (unsigned)v;
}
DI void put_word(char* p, unsigned tag, unsigned val) { __hip_atomic_store((gu64*)p, ((u64)tag << 32) | val, RLX_AGENT); }

constexpr int L_XS = 0;
constexpr int L_WKV = 0;
constexpr int L_XCH = 65536;
constexpr int L_WQ = 114688;
constexpr int L_MODW = 143360;
constexpr int L_SILU = 157696;
constexpr int L_STAT = 143360;
constexpr int L_GB = 145408;
constexpr int L_KMX = 158592;
constexpr int L_LOW = 0;
constexpr int L_M2 = 57344;
constexpr int L_GA = L_M2, L_G2 = L_M2 + 1792, L_GBV = L_M2 + 3584, L_PK = L_M2 + 5376;
constexpr int L_SM = L_M2 + 7168;
constexpr int L_HI = 65536;
constexpr int LDS_BYTES = 160 * 1024;

#define ASTEPH(SC, SN, KADDR, V1ADDR, V0ADDR, H1, H2, H3, H4) do { \
    SN = MFMA32(kf0, qf0, zero16); SBAR(); \
    pw1[2] = pack2(e12, e13); pw1[3] = pack2(e14, e15); \
    kf0 = ldsfrag(KADDR); SBAR(); \
    SC[0] = EX2(SC[0]); SC[1] = EX2(SC[1]); SBAR(); H1; SBAR(); SC[2] = EX2(SC[2]); SC[3] = EX2(SC[3]); PIN(SC); PIN(pw1); SBAR(); \
    o = MFMA32(vf1, BC8(pw1), o); SBAR(); \
    vf1 = ldsfrag(V1ADDR); \
    SC[4] = EX2(SC[4]); SC[5] = EX2(SC[5]); SBAR(); pw0[0] = pack2(SC[0], SC[1]); pw0[1] = pack2(SC[2], SC[3]); SBAR(); H2; SBAR(); SC[6] = EX2(SC[6]); SC[7] = EX2(SC[7]); PIN(SC); PIN(pw0); SBAR(); \
    SN = MFMA32(kf1, qf1, SN); SBAR(); \
    kf1 = ldsfrag((KADDR) + 1024); \
    SC[8] = EX2(SC[8]); SC[9] = EX2(SC[9]); SBAR(); pw0[2] = pack2(SC[4], SC[5]); pw0[3] = pack2(SC[6], SC[7]); SBAR(); H3; SBAR(); SC[10] = EX2(SC[10]); SC[11] = EX2(SC[11]); PIN(SC); PIN(pw0); SBAR(); \
    o = MFMA32(vf0, BC8(pw0), o); SBAR(); \
    vf0 = ldsfrag(V0ADDR); \
    e12 = EX2(SC[12]); e13 = EX2(SC[13]); SBAR(); pw1[0] = pack2(SC[8], SC[9]); pw1[1] = pack2(SC[10], SC[11]); SBAR(); H4; SBAR(); e14 = EX2(SC[14]); e15 = EX2(SC[15]); PIN(e12); PIN(e13); PIN(e14); PIN(e15); PIN(pw1); SBAR(); \
  } while (0)
#define NOH ((void)0)
#define ASTEP(SC, SN, KADDR, V1ADDR, V0ADDR) ASTEPH(SC, SN, KADDR, V1ADDR, V0ADDR, NOH, NOH, NOH, NOH)

#define ATTN_PROLOGUE() \
    kf0 = ldsfrag(abase); kf1 = ldsfrag(abase + 1024); \
    vf0 = ldsfrag(abase + 2048); vf1 = vf0; \
    e12 = 0.f; e13 = 0.f; e14 = 0.f; e15 = 0.f; pw1[0] = 0; pw1[1] = 0; o = zero16; \
    sA = MFMA32(kf0, qf0, zero16); sA = MFMA32(kf1, qf1, sA); \
    kf0 = ldsfrag(abase + 4096); kf1 = ldsfrag(abase + 4096 + 1024);

__global__ void __launch_bounds__(512) fused_block(const u16* __restrict__ x, const u16* __restrict__ wqkv, const u16* __restrict__ wo,
                                                    const u16* __restrict__ w1, const u16* __restrict__ w2, const u16* __restrict__ cvec,
                                                    const u16* __restrict__ wmod, u16* __restrict__ out, char* __restrict__ ws) {
  __shared__ __attribute__((aligned(16))) char lds_arr[LDS_BYTES];
  const lds_ptr L = (lds_ptr)lds_arr;
  const unsigned lds0 = (unsigned)(uintptr_t)lds_arr;
  const int t = threadIdx.x, lane = t & 63, wv = __builtin_amdgcn_readfirstlane(t >> 6), r = lane & 31, h = lane >> 5;
  __builtin_assume(wv < 8);
  const int g16 = (lane >> 4) & 1, tq = (lane & 15) >> 2, tp = lane & 3;
  const int b = blockIdx.x, xg = b & 7, rk = b >> 3;
  const __amdgpu_buffer_rsrc_t rs = __builtin_amdgcn_make_buffer_rsrc(ws, 0, WS_USED, 0x00020000);
  asm volatile("" :: "s"(x), "s"(wqkv), "s"(wo), "s"(w1), "s"(w2), "s"(cvec), "s"(wmod), "s"(out), "s"(ws));
  STAMP_DECL();
  STAMP(0);

  const int rt = xg * 2 + (rk >> 4), hq = rk & 15;
  const bool modwave = (rk < 21) && (wv == 0);
  const int msl = xg * 21 + rk;
  u32x4 craw = {0, 0, 0, 0};
  if (modwave) {
    asm volatile("global_load_dwordx4 %0, %1, %2" : "=v"(craw) : "v"((lane < 56 ? lane : 55) * 16), "s"(cvec) : "memory");
    const unsigned vo = (unsigned)(lane >> 1) * (6 * DM * 2) + (lane & 1) * 16;
#pragma unroll
    for (int i = 0; i < 14; ++i) dma1(wmod + msl * 16 + i * 32 * 6 * DM, vo, lds0 + L_MODW + i * 1024);
  }
  const int a_tt = wv & 3, a_kh = wv >> 2;
  if (wv > 0) {
    const u16* xb = x + (size_t)(rt * 128) * DM;
    const int g0 = (2 * (wv - 1) + hq) & 15, g1 = (g0 + 1) & 15, g2 = (14 + hq) & 15, g3 = (15 + hq) & 15;
    const unsigned p0 = (g0 & 1) << 6, p1 = p0 ^ 64;
#pragma unroll
    for (int i = 0; i < 7; ++i) {
      const int q = i * 64 + lane, rl = (q * 1171) >> 16, c = q - rl * 56;
      const unsigned vo = rl * (DM * 2) + ((c ^ (rl >> 1)) * 16);
      dma1(xb + g0 * 8 * DM, vo ^ p0, lds0 + L_XS + g0 * 7168 + i * 1024);
      dma1(xb + g1 * 8 * DM, vo ^ p1, lds0 + L_XS + g1 * 7168 + i * 1024);
    }
    const int q = (wv - 1) * 64 + lane, rl = (q * 1171) >> 16, c = q - rl * 56;
    const unsigned vo = rl * (DM * 2) + ((c ^ (rl >> 1)) * 16);
    dma1(xb + g2 * 8 * DM, vo ^ p0, lds0 + L_XS + g2 * 7168 + (wv - 1) * 1024);
    dma1(xb + g3 * 8 * DM, vo ^ p1, lds0 + L_XS + g3 * 7168 + (wv - 1) * 1024);
  }
  u64 epoch;
  asm volatile("s_load_dwordx2 %0, %1, 0x0" : "=s"(epoch) : "s"(ws) : "memory");
  const unsigned wvo = (lane >> 2) * (3 * DM * 2) + ((tp == 3) ? 40 : tp * 16);
  if (wv > 0) {
#pragma unroll
    for (int i = 0; i < 4; ++i) { const int s = (wv - 1) * 4 + i; dma1(wqkv + hq * DH + s * 16 * 3 * DM, wvo, lds0 + L_WQ + s * 1024); }
  }
  if (t == 0) *(LAS unsigned*)(L + L_KMX) = 0;
  asm volatile("s_waitcnt lgkmcnt(0)" : "+s"(epoch) :: "memory");
  const unsigned tag = (unsigned)epoch + 1u;
  if (modwave) {
    asm volatile("s_waitcnt vmcnt(14)" : "+v"(craw) :: "memory");
    u32x4 sv;
#pragma unroll
    for (int j = 0; j < 4; ++j) {
      const float c0_ = bflo(craw[j]), c1_ = bfhi(craw[j]);
      sv[j] = pack2(c0_ * __builtin_amdgcn_rcpf(1.f + EX2(-1.4426950408889634f * c0_)), c1_ * __builtin_amdgcn_rcpf(1.f + EX2(-1.4426950408889634f * c1_)));
    }
    if (lane < 56) *(LAS u32x4*)(L + L_SILU + lane * 16) = sv;
    const int kg = lane >> 4;
    const lds_cptr bp = L + L_MODW + (8 * kg + tq) * 32 + tp * 8;
    const lds_cptr ap = L + L_SILU + kg * 16;
    bf16x8 af[14], bfr[14];
    asm volatile("s_waitcnt lgkmcnt(0)" ::: "memory");
#pragma unroll
    for (int s = 0; s < 14; ++s) af[s] = ldsfrag(ap + s * 64);
    asm volatile("s_waitcnt vmcnt(0)" ::: "memory");
#pragma unroll
    for (int s = 0; s < 14; ++s) bfr[s] = trfrag(bp + s * 1024, bp + s * 1024 + 128);
    f32x4 acc0 = {0.f, 0.f, 0.f, 0.f}, acc1 = acc0;
#pragma unroll
    for (int s = 0; s < 14; s += 2) { acc0 = MFMA16(af[s], bfr[s], acc0); acc1 = MFMA16(af[s + 1], bfr[s + 1], acc1); }
    if (lane < 16) put_word(ws + OFF_MODG + (msl * 16 + lane) * 8, tag, __float_as_uint(rbf(acc0[0] + acc1[0])));
  }
  STAMP(1);
  BAR_VM(4);
  u32x4 xr[14];
  {
    const int row = a_tt * 32 + r, gsw = (row >> 1) & 7, c0 = a_kh * 28 + h;
#pragma unroll
    for (int i = 0; i < 4; ++i) {
      const int c = c0 + 2 * i;
      const lds_cptr xa = L + L_XS + row * (DM * 2) + (c & ~7) * 16 + (((c & 7) ^ gsw) * 16);
#pragma unroll
      for (int s = i; s < 14; s += 4) xr[s] = *(const LAS u32x4*)(xa + (s >> 2) * 128);
    }
  }
  u32x4 mg;
  asm volatile("global_load_dwordx4 %0, %1, %2 offset:0 sc1" : "=v"(mg) : "v"((t < DM ? t : DM - 1) * 16), "s"(ws + OFF_MODG) : "memory");
  BAR_LGKM();
  STAMP(2);
  f32x16 zero16;
#pragma unroll
  for (int i = 0; i < 16; ++i) zero16[i] = 0.f;
  f32x16 aq = zero16, ak = zero16, av = zero16;
  const int wro = a_kh * 14 * 1024 + (8 * h + tq) * 64 + g16 * 32 + tp * 8;
  const lds_cptr wqp = L + L_WQ + wro, wkp = L + L_WKV + wro, wvp = L + L_WKV + 28672 + wro;
  {
    const u16* wsrc = wqkv + hq * DH + (1 + (wv >> 2)) * DM + (wv & 3) * 7 * 16 * 3 * DM;
    float s1a = 0.f, s2a = 0.f, s1b = 0.f, s2b = 0.f;
#pragma unroll
    for (int s = 0; s < 14; ++s) {
      if (s == 0 || s == 5 || s == 10) dma1(wsrc + (s / 5) * 16 * 3 * DM, wvo, lds0 + L_WKV + (wv * 7 + (s / 5)) * 1024);
      s1a = dot2bf(xr[s][0], 0x3f803f80u, s1a); s2a = dot2bf(xr[s][0], xr[s][0], s2a);
      s1b = dot2bf(xr[s][1], 0x3f803f80u, s1b); s2b = dot2bf(xr[s][1], xr[s][1], s2b);
      s1a = dot2bf(xr[s][2], 0x3f803f80u, s1a); s2a = dot2bf(xr[s][2], xr[s][2], s2a);
      s1b = dot2bf(xr[s][3], 0x3f803f80u, s1b); s2b = dot2bf(xr[s][3], xr[s][3], s2b);
      PIN(s1a); PIN(s2a); PIN(s1b); PIN(s2b); SBAR();
    }
    const float s1 = xhalf_add(s1a + s1b), s2 = xhalf_add(s2a + s2b);
    if (h == 0) { f32x2 v = {s1, s2}; *(LAS f32x2*)(L + L_STAT + (wv * 32 + r) * 8) = v; }
    asm volatile("s_waitcnt vmcnt(3)" : "+v"(mg) :: "memory");
    if (wv < 7) {
      if (!__all(mg[1] == tag && mg[3] == tag)) {
        const u64 t0 = __builtin_amdgcn_s_memrealtime();
        for (;;) {
          mg = __builtin_amdgcn_raw_buffer_load_b128(rs, OFF_MODG + t * 16, 0, 16);
          if (__all(mg[1] == tag && mg[3] == tag)) break;
          if (__builtin_amdgcn_s_memrealtime() - t0 > 2000000ull) break;
          __builtin_amdgcn_s_sleep(1);
        }
      }
      f32x2 o2 = {__uint_as_float(mg[0]), __uint_as_float(mg[2])};
      const bool isg = t >= 224;
      if (isg) { o2[0] = rbf(1.f + o2[0]); o2[1] = rbf(1.f + o2[1]); }
      *(LAS f32x2*)(L + L_GB + (isg ? 2 * t - DM : DM + 2 * t) * 4) = o2;
    }
    BAR_LGKM();
    STAMP(3);
    {
      const unsigned to = (unsigned)(((rk >> 3) * 32 + wv * 4 + ((lane >> 2) & 3)) * (FF * 2) + (((rk & 7) * 448) & ~127) + (lane & 3) * 128);
      touch1(w1, to, lds0 + L_KMX + 64);
    }
    const lds_cptr gp = L + L_GB + (a_kh * 224 + 8 * h) * 4;
    f32x4 gq[3][4];
#define LOADGB(slot, s) do { gq[slot][0] = *(const LAS f32x4*)(gp + (s) * 64); gq[slot][1] = *(const LAS f32x4*)(gp + (s) * 64 + 16); \
      gq[slot][2] = *(const LAS f32x4*)(gp + DM * 4 + (s) * 64); gq[slot][3] = *(const LAS f32x4*)(gp + DM * 4 + (s) * 64 + 16); } while (0)
    const f32x2 o2 = *(const LAS f32x2*)(L + L_STAT + ((wv ^ 4) * 32 + r) * 8);
    LOADGB(0, 0); LOADGB(1, 1);
    const float mu = (s1 + o2[0]) * (1.f / DM);
    const float var = fmaxf((s2 + o2[1]) * (1.f / DM) - mu * mu, 0.f);
    const float rstd = __builtin_amdgcn_rsqf(var + 1e-5f);
    const unsigned rhi = __float_as_uint(rbf(rstd)), rlo = rhi >> 16;
    const float nbr = -mu * __uint_as_float(rhi);
    bf16x8 wa2[2];
    wa2[0] = trfrag(wqp, wqp + 256);
    SBAR();
#pragma unroll
    for (int s = 0; s < 14; ++s) {
      if (s + 2 < 14) LOADGB((s + 2) % 3, s + 2);
      if (s < 8 && (s & 1) == 0) dma1(wsrc + (3 + s / 2) * 16 * 3 * DM, wvo, lds0 + L_WKV + (wv * 7 + 3 + s / 2) * 1024);
      if (s >= 1) { aq = MFMA32(wa2[(s - 1) & 1], BC8(xr[s - 1]), aq); PIN(aq); }
      SBAR();
      if (s >= 1) wa2[s & 1] = trfrag(wqp + s * 1024, wqp + s * 1024 + 256);
      float tt_[8];
#pragma unroll
      for (int j = 0; j < 4; ++j) {
        asm("v_dot2_f32_bf16 %0, %1, %2, %3" : "=v"(tt_[2 * j]) : "v"(xr[s][j]), "v"(rlo), "v"(nbr));
        asm("v_dot2_f32_bf16 %0, %1, %2, %3" : "=v"(tt_[2 * j + 1]) : "v"(xr[s][j]), "v"(rhi), "v"(nbr));
      }
      SBAR();
#pragma unroll
      for (int j = 0; j < 8; ++j) asm("v_fma_f32 %0, %1, %2, %3" : "=v"(tt_[j]) : "v"(tt_[j]), "v"(gq[s % 3][j >> 2][j & 3]), "v"(gq[s % 3][2 + (j >> 2)][j & 3]));
      SBAR();
#pragma unroll
      for (int j = 0; j < 4; ++j) xr[s][j] = pack2(tt_[2 * j], tt_[2 * j + 1]);
      PIN(xr[s]); SBAR();
    }
    aq = MFMA32(wa2[1], BC8(xr[13]), aq); PIN(aq);
  }
  {
    bf16x8 wa[14], wb[14];
    BAR_VM(0);
    STAMP(4);
#pragma unroll
    for (int s = 0; s < 22; ++s) {
      if (s >= 4 && s < 18) { ak = MFMA32(wb[s - 4], BC8(xr[s - 4]), ak); PIN(ak); SBAR(); }
      if (s < 14) wb[s] = trfrag(wkp + s * 1024, wkp + s * 1024 + 256);
      if (s >= 4 && s < 18) wa[s - 4] = trfrag(wvp + (s - 4) * 1024, wvp + (s - 4) * 1024 + 256);
      SBAR();
      if (s >= 8) { av = MFMA32(BC8(xr[s - 8]), wa[s - 8], av); PIN(av); SBAR(); }
    }
  }
  STAMP(5);
  {
    const lds_ptr xw = L + L_XCH + wv * 6144 + lane * 16;
    const lds_cptr xrd = L + L_XCH + (wv ^ 4) * 6144 + lane * 16;
    const int tt = rt * 4 + a_tt;
    const bool vpad = (r >= 24 && r < 28);
    const unsigned vconst = (r == 24) ? 0x3f803f80u : 0u;
#define W4(off, A, i0) do { f32x4 v_ = {A[i0], A[(i0) + 1], A[(i0) + 2], A[(i0) + 3]}; *(LAS f32x4*)(xw + (off)) = v_; } while (0)
#define R4(off, A, i0) do { const f32x4 v_ = *(const LAS f32x4*)(xrd + (off)); A[i0] += v_[0]; A[(i0) + 1] += v_[1]; A[(i0) + 2] += v_[2]; A[(i0) + 3] += v_[3]; } while (0)
    if (a_kh == 0) {
      W4(0, ak, 0); W4(1024, ak, 4); W4(2048, ak, 8); W4(3072, ak, 12); W4(4096, av, 8); W4(5120, av, 12);
      BAR_LGKM();
      R4(0, aq, 0); R4(1024, aq, 4); R4(2048, aq, 8); R4(3072, aq, 12); R4(4096, av, 0); R4(5120, av, 4);
      const float qs = 0.18898223650461362f * 1.4426950408889634f;
#pragma unroll
      for (int i = 0; i < 16; ++i) aq[i] *= qs;
      u32x4 f0 = pack8(aq, 0), f1 = pack8(aq, 1), vf = pack8(av, 0);
      if (h == 0) { f1[2] = 0; f1[3] = 0; }
#pragma unroll
      for (int i = 0; i < 4; ++i) vf[i] = vpad ? vconst : vf[i];
      const unsigned mo = OFF_Q + (hq * NTT + tt) * 2048 + lane * 16;
      __builtin_amdgcn_raw_buffer_store_b128(f0, rs, mo, 0, 16);
      __builtin_amdgcn_raw_buffer_store_b128(f1, rs, mo + 1024, 0, 16);
      __builtin_amdgcn_raw_buffer_store_b128(vf, rs, OFF_KV + ((hq * NTT + tt) * 4 + 2) * 1024 + lane * 16, 0, 16);
    } else {
      W4(0, aq, 0); W4(1024, aq, 4); W4(2048, aq, 8); W4(3072, aq, 12); W4(4096, av, 0); W4(5120, av, 4);
      BAR_LGKM();
      R4(0, ak, 0); R4(1024, ak, 4); R4(2048, ak, 8); R4(3072, ak, 12); R4(4096, av, 8); R4(5120, av, 12);
      u32x4 f0 = pack8(ak, 0), f1 = pack8(ak, 1), vf = pack8(av, 1);
      u32x4 f1z = f1;
      if (h == 0) { f1z[2] = 0; f1z[3] = 0; f1[2] = 0x3f803f80u; f1[3] = 0x00003f80u; }
#pragma unroll
      for (int i = 0; i < 4; ++i) vf[i] = vpad ? vconst : vf[i];
      const unsigned mo = OFF_KV + (hq * NTT + tt) * 4096 + lane * 16;
      __builtin_amdgcn_raw_buffer_store_b128(f0, rs, mo, 0, 16);
      __builtin_amdgcn_raw_buffer_store_b128(f1, rs, mo + 1024, 0, 16);
      __builtin_amdgcn_raw_buffer_store_b128(vf, rs, mo + 3072, 0, 16);
      float kn = 0.f;
#pragma unroll
      for (int i = 0; i < 4; ++i) { kn = dot2bf(f0[i], f0[i], kn); kn = dot2bf(f1z[i], f1z[i], kn); }
      kn = xhalf_add(kn);
      asm volatile("ds_max_u32 %0, %1" :: "v"(lds0 + L_KMX), "v"(__float_as_uint(kn)) : "memory");
    }
    BAR_ALL();
    if (t == 0) put_word(ws + OFF_FKV + (hq * 16 + rt) * 8, tag, *(const LAS unsigned*)(L + L_KMX));
  }
  STAMP(6);
  const int head = xg * 2 + (rk >> 4), qt = rk & 15;
  const int tg = xg * 4 + (rk >> 3), sl = rk & 7;
  const int qb = wv & 3, kh = wv >> 2;
  const int tb = wv >> 2, kq = wv & 3;
  const int tt_own = tg * 2 + tb;
  const unsigned l16 = lane * 16;
  const unsigned o_off = OFF_O + ((qt * 4 + qb) * 32 + head * 2 + kh) * 1024 + l16;
  const unsigned y_off = OFF_Y + (tt_own * 28 + sl * 4 + kq) * 1024 + l16;
  const unsigned st_off = OFF_ST + ((tt_own * 28 + sl * 4 + kq) * 32 + r) * 8;
  const unsigned h_off = OFF_H + ((tg * 2) * 112 + (sl * 7 + wv) * 2) * 1024 + l16;
  unsigned w1vo[4];
#pragma unroll
  for (int i = 0; i < 4; ++i) { const int pc = (wv + 8 * i < 28) ? wv + 8 * i : wv; const int q = pc * 64 + lane, row = q / 28, c = q - row * 28; w1vo[i] = row * (FF * 2) + c * 16; }
  const u16* w1b = w1 + sl * 224;
  const lds_cptr w1rd = L + L_LOW + (4 * h + tq) * 448 + wv * 64 + g16 * 32 + tp * 8;
  const unsigned w1d3 = (wv < 4) ? 24576 : 0;
#define W1D1(c, j) do { if ((c) >= 0) dma1(w1b + (size_t)(c) * 64 * FF, w1vo[j], lds0 + L_LOW + ((c) & 1) * 28672 + wv * 1024 + ((j) < 3 ? (j) * 8192 : w1d3)); } while (0)
#define W1_DMA(c) do { W1D1(c, 0); W1D1(c, 1); W1D1(c, 2); W1D1(c, 3); } while (0)
  if (wv == 7) {
    float km = __uint_as_float(wait_words(ws + OFF_FKV + head * 128, 16, tag, lane));
#pragma unroll
    for (int m = 8; m >= 1; m >>= 1) km = fmaxf(km, __shfl_xor(km, m));
    if (lane == 0) *(LAS float*)(L + L_SM) = km;
  } else {
    const u64 t0 = __builtin_amdgcn_s_memrealtime();
    u32x4 v0, v1;
    for (;;) {
      v0 = __builtin_amdgcn_raw_buffer_load_b128(rs, OFF_MODG + 7168 + t * 16, 0, 16);
      v1 = __builtin_amdgcn_raw_buffer_load_b128(rs, OFF_MODG + 14336 + t * 16, 0, 16);
      if (__all(v0[1] == tag && v0[3] == tag && v1[1] == tag && v1[3] == tag)) break;
      if (__builtin_amdgcn_s_memrealtime() - t0 > 2000000ull) break;
      __builtin_amdgcn_s_sleep(1);
    }
    W1_DMA(0); W1_DMA(1);
    const int e = (t < 224) ? 2 * t : 2 * t - DM;
    const int pk = (e >> 4) * 32 + ((e >> 2) & 1) * 16 + ((e >> 3) & 1) * 8 + (e & 3) * 2;
    if (t < 224) {
      f32x2 a = {__uint_as_float(v0[0]), __uint_as_float(v0[2])};
      f32x2 g = {rbf(1.f + __uint_as_float(v1[0])), rbf(1.f + __uint_as_float(v1[2]))};
      *(LAS f32x2*)(L + L_GA + e * 4) = a;
      *(LAS f32x2*)(L + L_G2 + e * 4) = g;
      *(LAS unsigned*)(L + L_PK + pk) = pack2(g[0], g[1]);
    } else {
      f32x2 g = {__uint_as_float(v1[0]), __uint_as_float(v1[2])};
      *(LAS f32x2*)(L + L_GBV + e * 4) = g;
      *(LAS unsigned*)(L + L_PK + 896 + pk) = pack2(__uint_as_float(v0[0]), __uint_as_float(v0[2]));
    }
  }
  BAR_LGKM();
  STAMP(7);
  const char* kvsrc = ws + OFF_KV + (size_t)(head * NTT + kh * 32 + qb) * 4096;
  const unsigned kvdst = lds0 + L_HI + kh * 16384 + qb * 4096;
#define DMA_KV(i, buf) dma4c(kvsrc + (size_t)(i) * 16384, l16, kvdst + (buf))
  bf16x8 qf0, qf1;
  {
    u32x4 q0, q1;
    asm volatile("global_load_dwordx4 %0, %2, %3 sc1\n\tglobal_load_dwordx4 %1, %2, %3 offset:1024 sc1" : "=&v"(q0), "=&v"(q1)
                 : "v"(l16), "s"(ws + OFF_Q + (head * NTT + qt * 4 + qb) * 2048) : "memory");
    DMA_KV(0, 0);
    if (wv == 7) { W1_DMA(0); W1_DMA(1); }
    DMA_KV(1, 32768);
    const float km = *(const LAS float*)(L + L_SM);
    asm volatile("s_waitcnt vmcnt(8)" : "+v"(q0), "+v"(q1) :: "memory");
    float qn = 0.f;
#pragma unroll
    for (int i = 0; i < 4; ++i) { qn = dot2bf(q0[i], q0[i], qn); qn = dot2bf(q1[i], q1[i], qn); }
    qn = xhalf_add(qn);
    const float bound = __builtin_amdgcn_sqrtf(qn * km) * 1.004f + 1e-30f;
    if (h == 0) q1[2] = ((__float_as_uint(bound) + 0xffffu) >> 16) | 0x8000u;
    qf0 = BC8(q0); qf1 = BC8(q1);
  }
  bf16x8 wf[28];
  f32x16 o, sA, sB;
  u32x4 pw0, pw1;
  bf16x8 kf0, kf1, vf0, vf1;
  float e12, e13, e14, e15;
  lds_cptr abase = L + L_HI + kh * 16384 + l16;
  lds_cptr aslot[3] = {abase, abase + 32768, abase + 65536};
  PIN(aslot[0]); PIN(aslot[1]); PIN(aslot[2]);
  BAR_VM(4);
  STAMP(8);
  ATTN_PROLOGUE();
  const unsigned rvo = (lane >> 3) * (DM * 2) + (((lane & 7) ^ (((lane >> 4) & 1) << 2)) * 16);
  const int rsw = ((tq >> 1) & 1) * 64;
  const u16* wob = wo + (sl < 7 ? sl : 0) * 64;
  const int wo3 = (wv < 4) ? wv + 24 : wv;
#define W1_RD1(c, i_) (wf[4 * (c) + (i_)] = trfrag(w1rd + ((c) & 1) * 28672 + (i_) * 16 * 448, w1rd + ((c) & 1) * 28672 + ((i_) * 16 + 8) * 448))
#define W1_READ(c) do { W1_RD1(c, 0); W1_RD1(c, 1); W1_RD1(c, 2); W1_RD1(c, 3); } while (0)
#define KV1(st, j) do { if ((st) < 8) dma1c(kvsrc + (size_t)(st) * 16384 + (j) * 1024, l16, kvdst + ((st) % 3) * 32768 + (j) * 1024); } while (0)
#define W1R1(c, j) do { if ((c) >= 0) W1_RD1(((c) >= 0 ? (c) : 0), j); } while (0)
#define WO1(hf, j) do { if ((hf) >= 0) { const int pc_ = (j) < 3 ? wv + 8 * (j) : wo3; \
      dma1(wob + ((hf) * 28 + pc_) * 8 * DM, rvo, lds0 + L_HI + ((hf) ? 0 : 65536) + pc_ * 1024); } } while (0)
#define STAGE(st, NW, RA, RB, RC, DC, WH, TCH) do { \
    const lds_cptr bc = aslot[(st) % 3], bn = aslot[((st) + 1) % 3]; \
    ASTEPH(sA, sB, bc + 2 * 4096, bc + 0 * 4096 + 3072, bc + 1 * 4096 + 2048, W1R1(RA, 0), W1R1(RA, 1), W1R1(RA, 2), W1R1(RA, 3)); \
    ASTEPH(sB, sA, bc + 3 * 4096, bc + 1 * 4096 + 3072, bc + 2 * 4096 + 2048, W1R1(RB, 0), W1R1(RB, 1), W1R1(RB, 2), W1R1(RB, 3)); \
    BAR_VM(NW); \
    ASTEPH(sA, sB, bn + 0 * 4096, bc + 2 * 4096 + 3072, bc + 3 * 4096 + 2048, KV1((st) + 2, 0); WO1(WH, 0); W1R1(RC, 0), KV1((st) + 2, 1); WO1(WH, 1); W1R1(RC, 1), \
           KV1((st) + 2, 2); WO1(WH, 2); W1R1(RC, 2), KV1((st) + 2, 3); WO1(WH, 3); W1R1(RC, 3)); \
    ASTEPH(sB, sA, bn + 1 * 4096, bc + 3 * 4096 + 3072, bn + 0 * 4096 + 2048, W1D1(DC, 0), W1D1(DC, 1), W1D1(DC, 2), W1D1(DC, 3); TCH); \
  } while (0)
  const int tl = wv * 9 + (lane < 9 ? lane : 8);
#define TOUCH_WO(i) touch1(wo, (rk * 98 + ((i) * 72 + tl < 98 ? (i) * 72 + tl : 97)) * 128, lds0 + L_SM + 256)
#define TOUCH_W2(i) do { const int li_ = (rk & 7) * 168 + ((i) * 72 + tl < 168 ? (i) * 72 + tl : 167); touch1(w2, ((rk >> 3) * 3136 + li_) * 128, lds0 + L_SM + 256); } while (0)
  STAGE(0, 0,  0,  1, -1,  2, -1, TOUCH_WO(0));
  STAGE(1, 5, -1, -1, -1,  3, -1, TOUCH_WO(1));
  STAGE(2, 5, -1, -1,  2, -1, -1, TOUCH_W2(0));
  STAGE(3, 1, -1, -1,  3,  4, -1, NOH);
  STAGE(4, 4, -1, -1, -1,  5, -1, NOH);
  STAGE(5, 4, -1, -1,  4, -1, -1, TOUCH_W2(1); TOUCH_W2(2));
  STAGE(6, 2, -1, -1,  5,  6,  0, NOH);
  STAGE(7, 8, -1, -1, -1, -1,  1, NOH);
  STAMP(9);
  float fin[8];
  float lsum;
  constexpr int L_X = L_LOW + 28672;
#pragma unroll 1
  for (int pass = 0; pass < 2; ++pass) {
    pw1[2] = pack2(e12, e13); pw1[3] = pack2(e14, e15);
    o = MFMA32(vf1, BC8(pw1), o);
    {
      f32x4 a, b2;
      if (kh) {
#pragma unroll
        for (int i = 0; i < 4; ++i) { a[i] = o[i]; b2[i] = o[4 + i]; fin[i] = o[8 + i]; fin[4 + i] = o[12 + i]; }
      } else {
#pragma unroll
        for (int i = 0; i < 4; ++i) { a[i] = o[8 + i]; b2[i] = o[12 + i]; fin[i] = o[i]; fin[4 + i] = o[4 + i]; }
      }
      *(LAS f32x4*)(L + L_X + (wv * 2 + 0) * 1024 + l16) = a;
      *(LAS f32x4*)(L + L_X + (wv * 2 + 1) * 1024 + l16) = b2;
      if (h == 0) *(LAS float*)(L + L_X + 16384 + (wv * 32 + r) * 4) = o[12];
    }
    BAR_LGKM();
    {
      const int pw = wv ^ 4;
      const f32x4 a = *(const LAS f32x4*)(L + L_X + (pw * 2 + 0) * 1024 + l16);
      const f32x4 b2 = *(const LAS f32x4*)(L + L_X + (pw * 2 + 1) * 1024 + l16);
      const float c0 = *(const LAS float*)(L + L_X + 16384 + lane * 4) + *(const LAS float*)(L + L_X + 16384 + (128 + lane) * 4);
      const float c1 = *(const LAS float*)(L + L_X + 16384 + (64 + lane) * 4) + *(const LAS float*)(L + L_X + 16384 + (192 + lane) * 4);
#pragma unroll
      for (int i = 0; i < 4; ++i) { fin[i] += a[i]; fin[4 + i] += b2[i]; }
      lsum = *(const LAS float*)(L + L_X + 16384 + (qb * 32 + r) * 4) + *(const LAS float*)(L + L_X + 16384 + ((qb + 4) * 32 + r) * 4);
      const bool ok = (c0 >= 8.271806e-25f) && (c1 >= 8.271806e-25f);
      if (pass == 1 || __all(ok)) break;
    }
    {
      u32x4 q1 = __builtin_bit_cast(u32x4, qf1);
      if (h == 0) { q1[2] = 0; q1[3] = 0; }
      qf1 = BC8(q1);
      float mx = -INFINITY;
      const unsigned ko = OFF_KV + (head * NTT + kh * 32) * 4096 + l16;
#pragma unroll 1
      for (int kt = 0; kt < 32; ++kt) {
        const u32x4 k0 = __builtin_amdgcn_raw_buffer_load_b128(rs, ko + kt * 4096, 0, 16);
        const u32x4 k1 = __builtin_amdgcn_raw_buffer_load_b128(rs, ko + kt * 4096 + 1024, 0, 16);
        f32x16 s = MFMA32(BC8(k0), qf0, zero16);
        s = MFMA32(BC8(k1), qf1, s);
#pragma unroll
        for (int i = 0; i < 16; ++i) mx = fmaxf(mx, s[i]);
      }
      mx = xhalf_max(mx);
      if (h == 0) *(LAS float*)(L + L_X + 20480 + (wv * 32 + r) * 4) = mx;
      BAR_ALL();
      mx = fmaxf(*(const LAS float*)(L + L_X + 20480 + (qb * 32 + r) * 4), *(const LAS float*)(L + L_X + 20480 + ((qb + 4) * 32 + r) * 4));
      const float m0 = rbf(mx), m1 = rbf(mx - m0), m2 = rbf(mx - m0 - m1);
      if (h == 0) { q1[2] = pack2(-m0, -m1); q1[3] = pack2(-m2, 0.f); }
      qf1 = BC8(q1);
      DMA_KV(0, 0);
      DMA_KV(1, 32768);
      asm volatile("s_waitcnt vmcnt(4) lgkmcnt(0)\n\ts_barrier" ::: "memory");
      ATTN_PROLOGUE();
      int cur = 0, nxt = 32768, nn = 65536;
#pragma unroll 1
      for (int st = 0; st < 8; ++st) {
        const lds_cptr bc = abase + cur, bn = abase + nxt;
        ASTEP(sA, sB, bc + 2 * 4096, bc + 0 * 4096 + 3072, bc + 1 * 4096 + 2048);
        ASTEP(sB, sA, bc + 3 * 4096, bc + 1 * 4096 + 3072, bc + 2 * 4096 + 2048);
        BAR_VM(0);
        if (st + 2 < 8) DMA_KV(st + 2, nn);
        if (st == 7) { WO1(0, 0); WO1(0, 1); WO1(0, 2); WO1(0, 3); WO1(1, 0); WO1(1, 1); WO1(1, 2); WO1(1, 3); }
        ASTEP(sA, sB, bn + 0 * 4096, bc + 2 * 4096 + 3072, bc + 3 * 4096 + 2048);
        ASTEP(sB, sA, bn + 1 * 4096, bc + 3 * 4096 + 3072, bn + 0 * 4096 + 2048);
        const int tmp = cur; cur = nxt; nxt = nn; nn = tmp;
      }
    }
  }
  {
    const float inv = __builtin_amdgcn_rcpf(lsum);
    u32x4 ov;
#pragma unroll
    for (int i = 0; i < 4; ++i) ov[i] = pack2(fin[2 * i] * inv, fin[2 * i + 1] * inv);
    if (kh == 1 && h == 0) { ov[2] = 0; ov[3] = 0; }
    __builtin_amdgcn_raw_buffer_store_b128(ov, rs, o_off, 0, 16);
    BAR_ALL();
    if (t == 0) put_word(ws + OFF_FO + (qt * 16 + head) * 8, tag, 0);
  }
  STAMP(10);
  const int n0 = (sl * 2 + (kq >> 1)) * 32 + (kq & 1) * 16 + 4 * h;
  u32x2 xa2, xb2;
  unsigned xc0;
  asm volatile("global_load_dwordx2 %0, %3, %5\n\tglobal_load_dwordx2 %1, %3, %5 offset:16\n\tglobal_load_ushort %2, %4, %5"
               : "=&v"(xa2), "=&v"(xb2), "=&v"(xc0) : "v"((r * DM + (sl < 7 ? n0 : 0)) * 2), "v"(r * DM * 2), "s"(x + (size_t)tt_own * 32 * DM) : "memory");
  W1_READ(6);
  f32x16 uv = zero16;
  if (wv < 7) {
    const lds_cptr pp = L + L_PK + g16 * 896 + h * 16;
    bf16x8 pf[14];
#pragma unroll
    for (int hf2 = 0; hf2 < 2; ++hf2) {
#pragma unroll
      for (int i = 0; i < 14; ++i) pf[i] = ldsfrag(pp + (hf2 * 14 + i) * 32);
      SBAR();
#pragma unroll
      for (int i = 0; i < 14; ++i) uv = MFMA32(wf[hf2 * 14 + i], pf[i], uv);
      PIN(uv); SBAR();
    }
  }
  STAMP(11);
  if (wv == 7 && sl < 7) wait_words(ws + OFF_FO + (tg >> 1) * 128, 16, tag, lane);
  asm volatile("s_waitcnt vmcnt(0) lgkmcnt(0)\n\ts_barrier" : "+v"(xa2), "+v"(xb2), "+v"(xc0) :: "memory");
  const lds_ptr redb = L + L_LOW + tb * 24576 + l16;
#define RW1(A, o8, p, KQ) do { f32x4 v0 = {A[o8], A[(o8) + 1], A[(o8) + 2], A[(o8) + 3]}, v1 = {A[(o8) + 4], A[(o8) + 5], A[(o8) + 6], A[(o8) + 7]}; \
        *(LAS f32x4*)(redb + (((p) * 3 + (((KQ) - (p) - 1) & 3)) * 2) * 1024) = v0; *(LAS f32x4*)(redb + (((p) * 3 + (((KQ) - (p) - 1) & 3)) * 2 + 1) * 1024) = v1; } while (0)
#define RKEEP(AT, A, o8) do { _Pragma("unroll") for (int i = 0; i < 8; ++i) AT[i] = A[(o8) + i]; } while (0)
#define RED_WRITE(AT, A0, A1) do { \
      if (kq == 0)      { RW1(A0, 8, 1, 0); RW1(A1, 0, 2, 0); RW1(A1, 8, 3, 0); RKEEP(AT, A0, 0); } \
      else if (kq == 1) { RW1(A0, 0, 0, 1); RW1(A1, 0, 2, 1); RW1(A1, 8, 3, 1); RKEEP(AT, A0, 8); } \
      else if (kq == 2) { RW1(A0, 0, 0, 2); RW1(A0, 8, 1, 2); RW1(A1, 8, 3, 2); RKEEP(AT, A1, 0); } \
      else              { RW1(A0, 0, 0, 3); RW1(A0, 8, 1, 3); RW1(A1, 0, 2, 3); RKEEP(AT, A1, 8); } } while (0)
#define RED_READ(AT) do { \
      _Pragma("unroll") for (int d = 0; d < 3; ++d) { \
        const f32x4 v0 = *(const LAS f32x4*)(redb + kq * 6144 + (d * 2) * 1024); \
        const f32x4 v1 = *(const LAS f32x4*)(redb + kq * 6144 + (d * 2 + 1) * 1024); \
        _Pragma("unroll") for (int i = 0; i < 4; ++i) { AT[i] += v0[i]; AT[4 + i] += v1[i]; } } } while (0)
  const u16* w2b = w2 + (sl < 7 ? sl : 0) * 64;
#define W2A(i) dma1(w2b + (size_t)((wv >> 1) * 448 + ((wv & 1) * 12 + (i)) * 8) * DM, rvo, lds0 + L_HI + (wv * 12 + (i)) * 1024)
  u32x4 x1p;
  if (sl < 7) {
    float x1[8];
    STAMP(12);
    f32x16 acc0 = zero16, acc1 = zero16;
    {
      const lds_cptr wop = L + L_HI + ((kq >> 1) ? 0 : 65536) + ((kq & 1) * 4 * DH + 4 * h + tq) * 128 + g16 * 32 + tp * 8;
      const lds_cptr p0 = wop + rsw, p1 = wop + (64 ^ rsw);
      bf16x8 wa0[4], wa1[4];
#define WOROW0(i) (((i) >> 1) * DH + 16 * ((i) & 1))
#define WOROW1(i) (((i) >> 1) * DH + (((i) & 1) ? 20 : 8))
      u32x4 ob[8];
      const unsigned oo = OFF_O + (tt_own * 32 + kq * 8) * 1024 + l16;
#pragma unroll
      for (int i = 0; i < 8; ++i) ob[i] = __builtin_amdgcn_raw_buffer_load_b128(rs, oo + i * 1024, 0, 16);
      SBAR();
#pragma unroll
      for (int i = 0; i < 4; ++i) {
        wa0[i] = trfrag(p0 + WOROW0(i) * 128, p0 + WOROW1(i) * 128);
        wa1[i] = trfrag(p1 + WOROW0(i) * 128, p1 + WOROW1(i) * 128);
      }
      SBAR();
#pragma unroll
      for (int i = 0; i < 8; ++i) {
        acc0 = MFMA32(wa0[i & 3], BC8(ob[i]), acc0); acc1 = MFMA32(wa1[i & 3], BC8(ob[i]), acc1); PIN(acc0); PIN(acc1); SBAR();
        if (i < 4) {
          wa0[i] = trfrag(p0 + WOROW0(i + 4) * 128, p0 + WOROW1(i + 4) * 128);
          wa1[i] = trfrag(p1 + WOROW0(i + 4) * 128, p1 + WOROW1(i + 4) * 128);
        }
        SBAR();
      }
    }
    STAMP(13);
    float at[8];
    RED_WRITE(at, acc0, acc1);
    const f32x4 ga0 = *(const LAS f32x4*)(L + L_GA + n0 * 4), ga1 = *(const LAS f32x4*)(L + L_GA + (n0 + 8) * 4);
    const f32x4 g20 = *(const LAS f32x4*)(L + L_G2 + n0 * 4), g21 = *(const LAS f32x4*)(L + L_G2 + (n0 + 8) * 4);
    BAR_LGKM();
    RED_READ(at);
    x1[0] = bflo(xa2[0]) + ga0[0] * at[0]; x1[1] = bfhi(xa2[0]) + ga0[1] * at[1];
    x1[2] = bflo(xa2[1]) + ga0[2] * at[2]; x1[3] = bfhi(xa2[1]) + ga0[3] * at[3];
    x1[4] = bflo(xb2[0]) + ga1[0] * at[4]; x1[5] = bfhi(xb2[0]) + ga1[1] * at[5];
    x1[6] = bflo(xb2[1]) + ga1[2] * at[6]; x1[7] = bfhi(xb2[1]) + ga1[3] * at[7];
#pragma unroll
    for (int i = 0; i < 4; ++i) x1p[i] = pack2(x1[2 * i], x1[2 * i + 1]);
    PIN(x1p);
    const float cshift = __uint_as_float(xc0 << 16);
    float p1 = 0.f, p2 = 0.f, y[8];
#pragma unroll
    for (int i = 0; i < 8; ++i) {
      const float d = x1[i] - cshift;
      p1 += d; p2 += d * d;
      y[i] = d * (i < 4 ? g20[i & 3] : g21[i & 3]);
    }
    u32x4 yv;
#pragma unroll
    for (int i = 0; i < 4; ++i) yv[i] = pack2(y[2 * i], y[2 * i + 1]);
    __builtin_amdgcn_raw_buffer_store_b128(yv, rs, y_off, 0, 16);
    p1 = xhalf_add(p1); p2 = xhalf_add(p2);
    {
      u32x2 sv = {__float_as_uint(p1), __float_as_uint(p2)};
      __builtin_amdgcn_raw_buffer_store_b64(sv, rs, st_off, 0, 16);
    }
    BAR_ALL();
    if (t == 0) put_word(ws + OFF_FY + tg * 128 + sl * 8, tag, 0);
    if (b == 0 && t == 64) __hip_atomic_store((gu64*)ws, epoch + 1ull, RLX_AGENT);
    if (wv < 7) { W2A(0); W2A(1); W2A(2); W2A(3); W2A(4); W2A(5); W2A(6); W2A(7); W2A(8); W2A(9); W2A(10); W2A(11); }
  }
  STAMP(14);
  f32x16 u2, v2;
#pragma unroll
  for (int i = 0; i < 16; ++i) {
    auto rr = __builtin_amdgcn_permlane16_swap(__float_as_uint(uv[i]), __float_as_uint(uv[i]), false, false);
    u2[i] = __uint_as_float(rr[0]); v2[i] = __uint_as_float(rr[1]) * -1.4426950408889634f;
  }
  PIN(u2); PIN(v2);
  if (wv == 7) wait_words(ws + OFF_FY + tg * 128, 7, tag, lane);
  BAR_LGKM();
  STAMP(15);
  {
    if (wv < 7) {
      const char* ysrc = ws + OFF_Y + (size_t)tg * 57344 + wv * 8192;
      dma4c(ysrc, l16, lds0 + L_LOW + wv * 8192); dma4c(ysrc + 4096, l16, lds0 + L_LOW + wv * 8192 + 4096);
    } else {
      u32x2 sv[28];
#pragma unroll
      for (int i = 0; i < 28; ++i) sv[i] = __builtin_amdgcn_raw_buffer_load_b64(rs, OFF_ST + (((tg * 2 + h) * 28 + i) * 32 + r) * 8, 0, 16);
      float a1 = 0.f, a2 = 0.f;
#pragma unroll
      for (int i = 0; i < 28; ++i) { a1 += __uint_as_float(sv[i][0]); a2 += __uint_as_float(sv[i][1]); }
      const float m = a1 * (1.f / DM);
      const float var = fmaxf(a2 * (1.f / DM) - m * m, 0.f);
      f32x2 o2 = {__builtin_amdgcn_rsqf(var + 1e-5f) * -1.4426950408889634f, m};
      *(LAS f32x2*)(L + L_SM + 512 + lane * 8) = o2;
    }
  }
  BAR_ALL();
  STAMP(16);
  f32x16 c0 = zero16, c1 = zero16;
  f32x2 st0, st1;
  u32x4 gbp;
#define SILU1(C, ST, i) do { const float t_ = __builtin_fmaf(-ST[1], u2[i], C[i]); const float q_ = __builtin_fmaf(ST[0], t_, v2[i]); \
      C[i] = q_ * __builtin_amdgcn_rcpf(1.f + EX2(q_)); } while (0)
  {
    const f32x4 gb0 = *(const LAS f32x4*)(L + L_GBV + (sl < 7 ? n0 : 0) * 4), gb1 = *(const LAS f32x4*)(L + L_GBV + ((sl < 7 ? n0 : 0) + 8) * 4);
    gbp[0] = pack2(gb0[0], gb0[1]); gbp[1] = pack2(gb0[2], gb0[3]); gbp[2] = pack2(gb1[0], gb1[1]); gbp[3] = pack2(gb1[2], gb1[3]); PIN(gbp);
  }
  const int rowoff = (4 * h + tq) * 128 + g16 * 32 + tp * 8;
  const lds_cptr hi0 = L + L_HI + kq * 24576 + rowoff + rsw, hi1 = L + L_HI + kq * 24576 + rowoff + (64 ^ rsw);
  bf16x8 r0[12], r1[12];
  {
    const lds_cptr yp = L + L_LOW + l16;
    st0 = *(const LAS f32x2*)(L + L_SM + 512 + r * 8); st1 = *(const LAS f32x2*)(L + L_SM + 512 + (32 + r) * 8);
    bf16x8 yb[4];
    if (wv < 7) {
#pragma unroll
      for (int i = 0; i < 4; ++i) yb[i] = ldsfrag(yp + i * 1024);
      SBAR();
#pragma unroll
      for (int ks = 0; ks < 28; ++ks) {
        c0 = MFMA32(wf[ks], yb[ks & 3], c0); PIN(c0); SBAR();
        yb[ks & 3] = ldsfrag(yp + (ks + 4) * 1024);
        SBAR();
      }
    } else {
      W2A(0); W2A(1); W2A(2); W2A(3); W2A(4); W2A(5); W2A(6); W2A(7); W2A(8); W2A(9); W2A(10); W2A(11);
      const unsigned to = (192 + (rk >> 3) * 64 + lane) * (DM * 2);
#pragma unroll
      for (int q4 = 0; q4 < 4; ++q4) touch1(w2b + q4 * 448 * DM, to, lds0 + L_SM + 256);
    }
    if (wv < 7) {
#pragma unroll
      for (int ks = 0; ks < 28; ++ks) {
        c1 = MFMA32(wf[ks], yb[ks & 3], c1); PIN(c1); SBAR();
        if (ks + 4 < 28) yb[ks & 3] = ldsfrag(yp + (28 + ks + 4) * 1024);
        if (ks >= 1 && ks < 17) { SILU1(c0, st0, ks - 1); PIN(c0); }
        if (ks == 18) __builtin_amdgcn_raw_buffer_store_b128(pack8(c0, 0), rs, h_off, 0, 16);
        if (ks == 19) __builtin_amdgcn_raw_buffer_store_b128(pack8(c0, 1), rs, h_off + 1024, 0, 16);
        SBAR();
      }
    }
  }
  BAR_ALL();
  if (t == 0) put_word(ws + OFF_FH + tg * 128 + sl * 8, tag, 0);
  STAMP(17);
#pragma unroll
  for (int j = 0; j < 12; ++j) { r0[j] = trfrag(hi0 + j * 2048, hi0 + j * 2048 + 1024); r1[j] = trfrag(hi1 + j * 2048, hi1 + j * 2048 + 1024); }
  SBAR();
  if (wv < 7) {
#pragma unroll
    for (int i = 0; i < 8; ++i) SILU1(c1, st1, i);
    __builtin_amdgcn_raw_buffer_store_b128(pack8(c1, 0), rs, h_off + 112 * 1024, 0, 16);
#pragma unroll
    for (int i = 8; i < 16; ++i) SILU1(c1, st1, i);
    __builtin_amdgcn_raw_buffer_store_b128(pack8(c1, 1), rs, h_off + 113 * 1024, 0, 16);
  }
  BAR_ALL();
  if (t == 0) put_word(ws + OFF_FH + tg * 128 + 64 + sl * 8, tag, 0);
  STAMP(18);
  if (sl == 7) { STAMP_DUMP(); return; }
  {
#pragma unroll
    for (int i = 0; i < 16; ++i)
      dma1(w2b + (size_t)((wv >> 1) * 448 + 192 + ((wv & 1) * 16 + i) * 8) * DM, rvo, lds0 + (wv * 16 + i) * 1024);
    STAMP(19);
    const unsigned ho = OFF_H + (tt_own * 112 + kq * 28) * 1024;
    f32x16 d0, d1;
    const lds_cptr lo0 = L + kq * 32768 + rowoff + rsw, lo1 = L + kq * 32768 + rowoff + (64 ^ rsw);
    if (tb) BAR_VM(0);
    wait_words(ws + OFF_FH + tg * 128 + tb * 64, 8, tag, lane);
    STAMP(20);
    u32x4 hb[28];
#pragma unroll
    for (int i = 0; i < 18; ++i) hb[i] = __builtin_amdgcn_raw_buffer_load_b128(rs, ho + l16 + i * 1024, 0, 16);
    SBAR();
    d0 = zero16; d1 = zero16;
#pragma unroll
    for (int i = 0; i < 12; ++i) {
      d0 = MFMA32(r0[i], BC8(hb[i]), d0); PIN(d0); SBAR();
      d1 = MFMA32(r1[i], BC8(hb[i]), d1); PIN(d1); SBAR();
      if (i >= 1 && i < 11) hb[17 + i] = __builtin_amdgcn_raw_buffer_load_b128(rs, ho + l16 + (17 + i) * 1024, 0, 16);
      SBAR();
    }
    if (!tb) asm volatile("s_barrier" ::: "memory");
    {
      bf16x8 a0[4], a1[4];
#pragma unroll
      for (int i = 0; i < 4; ++i) { a0[i] = trfrag(lo0 + i * 2048, lo0 + i * 2048 + 1024); a1[i] = trfrag(lo1 + i * 2048, lo1 + i * 2048 + 1024); }
      SBAR();
#pragma unroll
      for (int i = 0; i < 16; ++i) {
        d0 = MFMA32(a0[i & 3], BC8(hb[12 + i]), d0); PIN(d0); SBAR();
        if (i + 4 < 16) a0[i & 3] = trfrag(lo0 + (i + 4) * 2048, lo0 + (i + 4) * 2048 + 1024);
        SBAR();
        d1 = MFMA32(a1[i & 3], BC8(hb[12 + i]), d1); PIN(d1); SBAR();
        if (i + 4 < 16) a1[i & 3] = trfrag(lo1 + (i + 4) * 2048, lo1 + (i + 4) * 2048 + 1024);
        SBAR();
      }
    }
    STAMP(21);
    BAR_LGKM();
    float f[8];
    RED_WRITE(f, d0, d1);
    BAR_LGKM();
    RED_READ(f);
#pragma unroll
    for (int i = 0; i < 8; ++i) f[i] *= -0.6931471805599453f;
    u32x2 oa, ob2;
    oa[0] = pack2(bflo(x1p[0]) + bflo(gbp[0]) * f[0], bfhi(x1p[0]) + bfhi(gbp[0]) * f[1]);
    oa[1] = pack2(bflo(x1p[1]) + bflo(gbp[1]) * f[2], bfhi(x1p[1]) + bfhi(gbp[1]) * f[3]);
    ob2[0] = pack2(bflo(x1p[2]) + bflo(gbp[2]) * f[4], bfhi(x1p[2]) + bfhi(gbp[2]) * f[5]);
    ob2[1] = pack2(bflo(x1p[3]) + bflo(gbp[3]) * f[6], bfhi(x1p[3]) + bfhi(gbp[3]) * f[7]);
    u16* orow = out + (size_t)(tt_own * 32 + r) * DM;
    *(u32x2*)(orow + n0) = oa;
    *(u32x2*)(orow + n0 + 8) = ob2;
  }
  STAMP(22);
  STAMP_DUMP();
}

extern "C" void kernel_launch(void* const* d_in, const int* in_sizes, int n_in, void* d_out, int out_size, void* d_ws,
                              size_t ws_size, hipStream_t stream) {
  fused_block<<<256, 512, 0, stream>>>((const u16*)d_in[0], (const u16*)d_in[1], (const u16*)d_in[2], (const u16*)d_in[4],
                                       (const u16*)d_in[5], (const u16*)d_in[6], (const u16*)d_in[7], (u16*)d_out, (char*)d_ws);
}
```

```cpp
#include <hip/hip_runtime.h>
#include <hip/hip_bf16.h>
#include <stdint.h>

typedef __attribute__((ext_vector_type(8))) short bf16x8;
typedef __attribute__((ext_vector_type(4))) short s16x4;
typedef __attribute__((ext_vector_type(16))) float f32x16;
typedef __attribute__((ext_vector_type(4))) unsigned u32x4;
typedef __attribute__((ext_vector_type(2))) unsigned u32x2;
typedef __attribute__((ext_vector_type(4))) float f32x4;
typedef __attribute__((ext_vector_type(2))) float f32x2;
typedef __attribute__((ext_vector_type(2))) __bf16 bf16x2_t;
typedef unsigned short u16;
typedef unsigned long long u64;
#define GAS __attribute__((address_space(1)))
#define LAS __attribute__((address_space(3)))
typedef GAS u64 gu64;
typedef LAS const char* lds_cptr;
typedef LAS char* lds_ptr;

#define DI __device__ __forceinline__
#define MFMA32(a, b, c) __builtin_amdgcn_mfma_f32_32x32x16_bf16((a), (b), (c), 0, 0, 0)
#define MFMA16(a, b, c) __builtin_amdgcn_mfma_f32_16x16x32_bf16((a), (b), (c), 0, 0, 0)
#define SBAR() __builtin_amdgcn_sched_barrier(0)
#define PIN(x) asm volatile("" : "+v"(x))
#define RLX_AGENT __ATOMIC_RELAXED, __HIP_MEMORY_SCOPE_AGENT
#define BC8(x) __builtin_bit_cast(bf16x8, x)
#define EX2(v) __builtin_amdgcn_exp2f(v)
#define BAR_LGKM() asm volatile("s_waitcnt lgkmcnt(0)\n\ts_barrier" ::: "memory")
#define BAR_ALL() asm volatile("s_waitcnt vmcnt(0) lgkmcnt(0)\n\ts_barrier" ::: "memory")
#define BAR_VM(n) asm volatile("s_waitcnt vmcnt(" #n ")\n\ts_barrier" ::: "memory")

constexpr int DM = 448, DH = 28, FF = 1792;
constexpr int NTT = 64;

constexpr unsigned OFF_MODG = 4096;
constexpr unsigned OFF_FKV = 32768;
constexpr unsigned OFF_FO = 36864;
constexpr unsigned OFF_FY = 40960;
constexpr unsigned OFF_FH = 49152;
constexpr unsigned OFF_ST = 65536;
constexpr unsigned OFF_Q = 1u << 20;
constexpr unsigned OFF_KV = 3u << 20;
constexpr unsigned OFF_O = 7u << 20;
constexpr unsigned OFF_Y = 9u << 20;
constexpr unsigned OFF_H = 11u << 20;
constexpr unsigned WS_USED = 19u << 20;


DI float bflo(unsigned w) { return __uint_as_float(w << 16); }
DI float bfhi(unsigned w) { return __uint_as_float(w & 0xffff0000u); }
DI unsigned pack2(float lo, float hi) {
  f32x2 v = {lo, hi};
  bf16x2_t b = __builtin_convertvector(v, bf16x2_t);
  return __builtin_bit_cast(unsigned, b);
}
DI float rbf(float x) { return bflo(pack2(x, x)); }
DI u32x4 pack8(const f32x16& x, int s) {
  u32x4 p;
  p[0] = pack2(x[8 * s + 0], x[8 * s + 1]);
  p[1] = pack2(x[8 * s + 2], x[8 * s + 3]);
  p[2] = pack2(x[8 * s + 4], x[8 * s + 5]);
  p[3] = pack2(x[8 * s + 6], x[8 * s + 7]);
  return p;
}
DI float xhalf_add(float v) {
  auto rr = __builtin_amdgcn_permlane32_swap(__float_as_uint(v), __float_as_uint(v), false, false);
  return __uint_as_float(rr[0]) + __uint_as_float(rr[1]);
}
DI float xhalf_max(float v) {
  auto rr = __builtin_amdgcn_permlane32_swap(__float_as_uint(v), __float_as_uint(v), false, false);
  return fmaxf(__uint_as_float(rr[0]), __uint_as_float(rr[1]));
}
DI void dma1(const void* sbase, unsigned voff, unsigned lds_base) {
  unsigned sv;
  asm volatile("s_mov_b32 %0, m0\n\ts_mov_b32 m0, %2\n\ts_nop 0\n\tglobal_load_lds_dwordx4 %1, %3\n\ts_mov_b32 m0, %0"
               : "=&s"(sv) : "v"(voff), "s"(lds_base), "s"(sbase) : "memory");
}
DI void dma1c(const void* sbase, unsigned voff, unsigned lds_base) {
  unsigned sv;
  asm volatile("s_mov_b32 %0, m0\n\ts_mov_b32 m0, %2\n\ts_nop 0\n\tglobal_load_lds_dwordx4 %1, %3 sc1\n\ts_mov_b32 m0, %0"
               : "=&s"(sv) : "v"(voff), "s"(lds_base), "s"(sbase) : "memory");
}
DI void dma4c(const void* sbase, unsigned voff, unsigned lds_base) {
  unsigned sv;
  asm volatile("s_mov_b32 %0, m0\n\ts_mov_b32 m0, %2\n\ts_nop 0\n\t"
               "global_load_lds_dwordx4 %1, %3 sc1\n\tglobal_load_lds_dwordx4 %1, %3 offset:1024 sc1\n\t"
               "global_load_lds_dwordx4 %1, %3 offset:2048 sc1\n\tglobal_load_lds_dwordx4 %1, %3 offset:3072 sc1\n\t"
               "s_mov_b32 m0, %0" : "=&s"(sv) : "v"(voff), "s"(lds_base), "s"(sbase) : "memory");
}
DI void touch1(const void* sbase, unsigned voff, unsigned lds_base) {
  unsigned sv;
  asm volatile("s_mov_b32 %0, m0\n\ts_mov_b32 m0, %2\n\ts_nop 0\n\tglobal_load_lds_dword %1, %3\n\ts_mov_b32 m0, %0"
               : "=&s"(sv) : "v"(voff), "s"(lds_base), "s"(sbase) : "memory");
}
DI bf16x8 ldsfrag(lds_cptr p) { return *(const LAS bf16x8*)p; }
DI s16x4 trread(lds_cptr p) { return __builtin_amdgcn_ds_read_tr16_b64_v4i16((LAS s16x4*)p); }
DI bf16x8 trfrag(lds_cptr p0, lds_cptr p1) {
  s16x4 lo = trread(p0), hi = trread(p1);
  return __builtin_shufflevector(lo, hi, 0, 1, 2, 3, 4, 5, 6, 7);
}
DI float dot2bf(unsigned a, unsigned b, float c) {
  return __builtin_amdgcn_fdot2_f32_bf16(__builtin_bit_cast(bf16x2_t, a), __builtin_bit_cast(bf16x2_t, b), c, false);
}
DI unsigned wait_words(const char* p, int n, unsigned tag, int lane) {
  const gu64* f = (const gu64*)p + (lane < n ? lane : n - 1);
  const u64 t0 = __builtin_amdgcn_s_memrealtime();
  u64 v;
  for (;;) {
    v = __hip_atomic_load(f, RLX_AGENT);
    if (__all((unsigned)(v >> 32) == tag)) break;
    if (__builtin_amdgcn_s_memrealtime() - t0 > 2000000ull) break;
    __builtin_amdgcn_s_sleep(1);
  }
  return (unsigned)v;
}
DI void put_word(char* p, unsigned tag, unsigned val) { __hip_atomic_store((gu64*)p, ((u64)tag << 32) | val, RLX_AGENT); }

constexpr int L_XS = 0;
constexpr int L_WKV = 0;
constexpr int L_XCH = 65536;
constexpr int L_WQ = 114688;
constexpr int L_MODW = 143360;
constexpr int L_SILU = 157696;
constexpr int L_STAT = 143360;
constexpr int L_GB = 145408;
constexpr int L_KMX = 158592;
constexpr int L_LOW = 0;
constexpr int L_M2 = 57344;
constexpr int L_GA = L_M2, L_G2 = L_M2 + 1792, L_GBV = L_M2 + 3584, L_PK = L_M2 + 5376;
constexpr int L_SM = L_M2 + 7168;
constexpr int L_HI = 65536;
constexpr int LDS_BYTES = 160 * 1024;

#define ASTEPH(SC, SN, KADDR, V1ADDR, V0ADDR, H1, H2, H3, H4) do { \
    SN = MFMA32(kf0, qf0, zero16); SBAR(); \
    pw1[2] = pack2(e12, e13); pw1[3] = pack2(e14, e15); \
    kf0 = ldsfrag(KADDR); SBAR(); \
    SC[0] = EX2(SC[0]); SC[1] = EX2(SC[1]); SBAR(); H1; SBAR(); SC[2] = EX2(SC[2]); SC[3] = EX2(SC[3]); PIN(SC); PIN(pw1); SBAR(); \
    o = MFMA32(vf1, BC8(pw1), o); SBAR(); \
    vf1 = ldsfrag(V1ADDR); \
    SC[4] = EX2(SC[4]); SC[5] = EX2(SC[5]); SBAR(); pw0[0] = pack2(SC[0], SC[1]); pw0[1] = pack2(SC[2], SC[3]); SBAR(); H2; SBAR(); SC[6] = EX2(SC[6]); SC[7] = EX2(SC[7]); PIN(SC); PIN(pw0); SBAR(); \
    SN = MFMA32(kf1, qf1, SN); SBAR(); \
    kf1 = ldsfrag((KADDR) + 1024); \
    SC[8] = EX2(SC[8]); SC[9] = EX2(SC[9]); SBAR(); pw0[2] = pack2(SC[4], SC[5]); pw0[3] = pack2(SC[6], SC[7]); SBAR(); H3; SBAR(); SC[10] = EX2(SC[10]); SC[11] = EX2(SC[11]); PIN(SC); PIN(pw0); SBAR(); \
    o = MFMA32(vf0, BC8(pw0), o); SBAR(); \
    vf0 = ldsfrag(V0ADDR); \
    e12 = EX2(SC[12]); e13 = EX2(SC[13]); SBAR(); pw1[0] = pack2(SC[8], SC[9]); pw1[1] = pack2(SC[10], SC[11]); SBAR(); H4; SBAR(); e14 = EX2(SC[14]); e15 = EX2(SC[15]); PIN(e12); PIN(e13); PIN(e14); PIN(e15); PIN(pw1); SBAR(); \
  } while (0)
#define NOH ((void)0)
#define ASTEP(SC, SN, KADDR, V1ADDR, V0ADDR) ASTEPH(SC, SN, KADDR, V1ADDR, V0ADDR, NOH, NOH, NOH, NOH)

#define ATTN_PROLOGUE() \
    kf0 = ldsfrag(abase); kf1 = ldsfrag(abase + 1024); \
    vf0 = ldsfrag(abase + 2048); vf1 = vf0; \
    e12 = 0.f; e13 = 0.f; e14 = 0.f; e15 = 0.f; pw1[0] = 0; pw1[1] = 0; o = zero16; \
    sA = MFMA32(kf0, qf0, zero16); sA = MFMA32(kf1, qf1, sA); \
    kf0 = ldsfrag(abase + 4096); kf1 = ldsfrag(abase + 4096 + 1024);

__global__ void __launch_bounds__(512) fused_block(const u16* __restrict__ x, const u16* __restrict__ wqkv, const u16* __restrict__ wo,
                                                    const u16* __restrict__ w1, const u16* __restrict__ w2, const u16* __restrict__ cvec,
                                                    const u16* __restrict__ wmod, u16* __restrict__ out, char* __restrict__ ws) {
  __shared__ __attribute__((aligned(16))) char lds_arr[LDS_BYTES];
  const lds_ptr L = (lds_ptr)lds_arr;
  const unsigned lds0 = (unsigned)(uintptr_t)lds_arr;
  const int t = threadIdx.x, lane = t & 63, wv = __builtin_amdgcn_readfirstlane(t >> 6), r = lane & 31, h = lane >> 5;
  __builtin_assume(wv < 8);
  const int g16 = (lane >> 4) & 1, tq = (lane & 15) >> 2, tp = lane & 3;
  const int b = blockIdx.x, xg = b & 7, rk = b >> 3;
  const __amdgpu_buffer_rsrc_t rs = __builtin_amdgcn_make_buffer_rsrc(ws, 0, WS_USED, 0x00020000);
  asm volatile("" :: "s"(x), "s"(wqkv), "s"(wo), "s"(w1), "s"(w2), "s"(cvec), "s"(wmod), "s"(out), "s"(ws));

  const int rt = xg * 2 + (rk >> 4), hq = rk & 15;
  const bool modwave = (rk < 21) && (wv == 0);
  const int msl = xg * 21 + rk;
  u32x4 craw = {0, 0, 0, 0};
  if (modwave) {
    asm volatile("global_load_dwordx4 %0, %1, %2" : "=v"(craw) : "v"((lane < 56 ? lane : 55) * 16), "s"(cvec) : "memory");
    const unsigned vo = (unsigned)(lane >> 1) * (6 * DM * 2) + (lane & 1) * 16;
#pragma unroll
    for (int i = 0; i < 14; ++i) dma1(wmod + msl * 16 + i * 32 * 6 * DM, vo, lds0 + L_MODW + i * 1024);
  }
  const int a_tt = wv & 3, a_kh = wv >> 2;
  if (wv > 0) {
    const u16* xb = x + (size_t)(rt * 128) * DM;
    const int g0 = (2 * (wv - 1) + hq) & 15, g1 = (g0 + 1) & 15, g2 = (14 + hq) & 15, g3 = (15 + hq) & 15;
    const unsigned p0 = (g0 & 1) << 6, p1 = p0 ^ 64;
#pragma unroll
    for (int i = 0; i < 7; ++i) {
      const int q = i * 64 + lane, rl = (q * 1171) >> 16, c = q - rl * 56;
      const unsigned vo = rl * (DM * 2) + ((c ^ (rl >> 1)) * 16);
      dma1(xb + g0 * 8 * DM, vo ^ p0, lds0 + L_XS + g0 * 7168 + i * 1024);
      dma1(xb + g1 * 8 * DM, vo ^ p1, lds0 + L_XS + g1 * 7168 + i * 1024);
    }
    const int q = (wv - 1) * 64 + lane, rl = (q * 1171) >> 16, c = q - rl * 56;
    const unsigned vo = rl * (DM * 2) + ((c ^ (rl >> 1)) * 16);
    dma1(xb + g2 * 8 * DM, vo ^ p0, lds0 + L_XS + g2 * 7168 + (wv - 1) * 1024);
    dma1(xb + g3 * 8 * DM, vo ^ p1, lds0 + L_XS + g3 * 7168 + (wv - 1) * 1024);
  }
  u64 epoch;
  asm volatile("s_load_dwordx2 %0, %1, 0x0" : "=s"(epoch) : "s"(ws) : "memory");
  const unsigned wvo = (lane >> 2) * (3 * DM * 2) + ((tp == 3) ? 40 : tp * 16);
  if (wv > 0) {
#pragma unroll
    for (int i = 0; i < 4; ++i) { const int s = (wv - 1) * 4 + i; dma1(wqkv + hq * DH + s * 16 * 3 * DM, wvo, lds0 + L_WQ + s * 1024); }
  }
  if (t == 0) *(LAS unsigned*)(L + L_KMX) = 0;
  asm volatile("s_waitcnt lgkmcnt(0)" : "+s"(epoch) :: "memory");
  const unsigned tag = (unsigned)epoch + 1u;
  if (modwave) {
    asm volatile("s_waitcnt vmcnt(14)" : "+v"(craw) :: "memory");
    u32x4 sv;
#pragma unroll
    for (int j = 0; j < 4; ++j) {
      const float c0_ = bflo(craw[j]), c1_ = bfhi(craw[j]);
      sv[j] = pack2(c0_ * __builtin_amdgcn_rcpf(1.f + EX2(-1.4426950408889634f * c0_)), c1_ * __builtin_amdgcn_rcpf(1.f + EX2(-1.4426950408889634f * c1_)));
    }
    if (lane < 56) *(LAS u32x4*)(L + L_SILU + lane * 16) = sv;
    const int kg = lane >> 4;
    const lds_cptr bp = L + L_MODW + (8 * kg + tq) * 32 + tp * 8;
    const lds_cptr ap = L + L_SILU + kg * 16;
    bf16x8 af[14], bfr[14];
    asm volatile("s_waitcnt lgkmcnt(0)" ::: "memory");
#pragma unroll
    for (int s = 0; s < 14; ++s) af[s] = ldsfrag(ap + s * 64);
    asm volatile("s_waitcnt vmcnt(0)" ::: "memory");
#pragma unroll
    for (int s = 0; s < 14; ++s) bfr[s] = trfrag(bp + s * 1024, bp + s * 1024 + 128);
    f32x4 acc0 = {0.f, 0.f, 0.f, 0.f}, acc1 = acc0;
#pragma unroll
    for (int s = 0; s < 14; s += 2) { acc0 = MFMA16(af[s], bfr[s], acc0); acc1 = MFMA16(af[s + 1], bfr[s + 1], acc1); }
    if (lane < 16) put_word(ws + OFF_MODG + (msl * 16 + lane) * 8, tag, __float_as_uint(rbf(acc0[0] + acc1[0])));
  }
  BAR_VM(4);
  u32x4 xr[14];
  {
    const int row = a_tt * 32 + r, gsw = (row >> 1) & 7, c0 = a_kh * 28 + h;
#pragma unroll
    for (int i = 0; i < 4; ++i) {
      const int c = c0 + 2 * i;
      const lds_cptr xa = L + L_XS + row * (DM * 2) + (c & ~7) * 16 + (((c & 7) ^ gsw) * 16);
#pragma unroll
      for (int s = i; s < 14; s += 4) xr[s] = *(const LAS u32x4*)(xa + (s >> 2) * 128);
    }
  }
  u32x4 mg;
  asm volatile("global_load_dwordx4 %0, %1, %2 offset:0 sc1" : "=v"(mg) : "v"((t < DM ? t : DM - 1) * 16), "s"(ws + OFF_MODG) : "memory");
  BAR_LGKM();
  f32x16 zero16;
#pragma unroll
  for (int i = 0; i < 16; ++i) zero16[i] = 0.f;
  f32x16 aq = zero16, ak = zero16, av = zero16;
  const int wro = a_kh * 14 * 1024 + (8 * h + tq) * 64 + g16 * 32 + tp * 8;
  const lds_cptr wqp = L + L_WQ + wro, wkp = L + L_WKV + wro, wvp = L + L_WKV + 28672 + wro;
  {
    const u16* wsrc = wqkv + hq * DH + (1 + (wv >> 2)) * DM + (wv & 3) * 7 * 16 * 3 * DM;
    float s1a = 0.f, s2a = 0.f, s1b = 0.f, s2b = 0.f;
#pragma unroll
    for (int s = 0; s < 14; ++s) {
      if (s == 0 || s == 5 || s == 10) dma1(wsrc + (s / 5) * 16 * 3 * DM, wvo, lds0 + L_WKV + (wv * 7 + (s / 5)) * 1024);
      s1a = dot2bf(xr[s][0], 0x3f803f80u, s1a); s2a = dot2bf(xr[s][0], xr[s][0], s2a);
      s1b = dot2bf(xr[s][1], 0x3f803f80u, s1b); s2b = dot2bf(xr[s][1], xr[s][1], s2b);
      s1a = dot2bf(xr[s][2], 0x3f803f80u, s1a); s2a = dot2bf(xr[s][2], xr[s][2], s2a);
      s1b = dot2bf(xr[s][3], 0x3f803f80u, s1b); s2b = dot2bf(xr[s][3], xr[s][3], s2b);
      PIN(s1a); PIN(s2a); PIN(s1b); PIN(s2b); SBAR();
    }
    const float s1 = xhalf_add(s1a + s1b), s2 = xhalf_add(s2a + s2b);
    if (h == 0) { f32x2 v = {s1, s2}; *(LAS f32x2*)(L + L_STAT + (wv * 32 + r) * 8) = v; }
    asm volatile("s_waitcnt vmcnt(3)" : "+v"(mg) :: "memory");
    if (wv < 7) {
      if (!__all(mg[1] == tag && mg[3] == tag)) {
        const u64 t0 = __builtin_amdgcn_s_memrealtime();
        for (;;) {
          mg = __builtin_amdgcn_raw_buffer_load_b128(rs, OFF_MODG + t * 16, 0, 16);
          if (__all(mg[1] == tag && mg[3] == tag)) break;
          if (__builtin_amdgcn_s_memrealtime() - t0 > 2000000ull) break;
          __builtin_amdgcn_s_sleep(1);
        }
      }
      f32x2 o2 = {__uint_as_float(mg[0]), __uint_as_float(mg[2])};
      const bool isg = t >= 224;
      if (isg) { o2[0] = rbf(1.f + o2[0]); o2[1] = rbf(1.f + o2[1]); }
      *(LAS f32x2*)(L + L_GB + (isg ? 2 * t - DM : DM + 2 * t) * 4) = o2;
    }
    BAR_LGKM();
    {
      const unsigned to = (unsigned)(((rk >> 3) * 32 + wv * 4 + ((lane >> 2) & 3)) * (FF * 2) + (((rk & 7) * 448) & ~127) + (lane & 3) * 128);
      touch1(w1, to, lds0 + L_KMX + 64);
    }
    const lds_cptr gp = L + L_GB + (a_kh * 224 + 8 * h) * 4;
    f32x4 gq[3][4];
#define LOADGB(slot, s) do { gq[slot][0] = *(const LAS f32x4*)(gp + (s) * 64); gq[slot][1] = *(const LAS f32x4*)(gp + (s) * 64 + 16); \
      gq[slot][2] = *(const LAS f32x4*)(gp + DM * 4 + (s) * 64); gq[slot][3] = *(const LAS f32x4*)(gp + DM * 4 + (s) * 64 + 16); } while (0)
    const f32x2 o2 = *(const LAS f32x2*)(L + L_STAT + ((wv ^ 4) * 32 + r) * 8);
    LOADGB(0, 0); LOADGB(1, 1);
    const float mu = (s1 + o2[0]) * (1.f / DM);
    const float var = fmaxf((s2 + o2[1]) * (1.f / DM) - mu * mu, 0.f);
    const float rstd = __builtin_amdgcn_rsqf(var + 1e-5f);
    const unsigned rhi = __float_as_uint(rbf(rstd)), rlo = rhi >> 16;
    const float nbr = -mu * __uint_as_float(rhi);
    bf16x8 wa2[2];
    wa2[0] = trfrag(wqp, wqp + 256);
    SBAR();
#pragma unroll
    for (int s = 0; s < 14; ++s) {
      if (s + 2 < 14) LOADGB((s + 2) % 3, s + 2);
      if (s < 8 && (s & 1) == 0) dma1(wsrc + (3 + s / 2) * 16 * 3 * DM, wvo, lds0 + L_WKV + (wv * 7 + 3 + s / 2) * 1024);
      if (s >= 1) { aq = MFMA32(wa2[(s - 1) & 1], BC8(xr[s - 1]), aq); PIN(aq); }
      SBAR();
      if (s >= 1) wa2[s & 1] = trfrag(wqp + s * 1024, wqp + s * 1024 + 256);
      float tt_[8];
#pragma unroll
      for (int j = 0; j < 4; ++j) {
        asm("v_dot2_f32_bf16 %0, %1, %2, %3" : "=v"(tt_[2 * j]) : "v"(xr[s][j]), "v"(rlo), "v"(nbr));
        asm("v_dot2_f32_bf16 %0, %1, %2, %3" : "=v"(tt_[2 * j + 1]) : "v"(xr[s][j]), "v"(rhi), "v"(nbr));
      }
      SBAR();
#pragma unroll
      for (int j = 0; j < 8; ++j) asm("v_fma_f32 %0, %1, %2, %3" : "=v"(tt_[j]) : "v"(tt_[j]), "v"(gq[s % 3][j >> 2][j & 3]), "v"(gq[s % 3][2 + (j >> 2)][j & 3]));
      SBAR();
#pragma unroll
      for (int j = 0; j < 4; ++j) xr[s][j] = pack2(tt_[2 * j], tt_[2 * j + 1]);
      PIN(xr[s]); SBAR();
    }
    aq = MFMA32(wa2[1], BC8(xr[13]), aq); PIN(aq);
  }
  {
    bf16x8 wa[14], wb[14];
    BAR_VM(0);
#pragma unroll
    for (int s = 0; s < 22; ++s) {
      if (s >= 4 && s < 18) { ak = MFMA32(wb[s - 4], BC8(xr[s - 4]), ak); PIN(ak); SBAR(); }
      if (s < 14) wb[s] = trfrag(wkp + s * 1024, wkp + s * 1024 + 256);
      if (s >= 4 && s < 18) wa[s - 4] = trfrag(wvp + (s - 4) * 1024, wvp + (s - 4) * 1024 + 256);
      SBAR();
      if (s >= 8) { av = MFMA32(BC8(xr[s - 8]), wa[s - 8], av); PIN(av); SBAR(); }
    }
  }
  {
    const lds_ptr xw = L + L_XCH + wv * 6144 + lane * 16;
    const lds_cptr xrd = L + L_XCH + (wv ^ 4) * 6144 + lane * 16;
    const int tt = rt * 4 + a_tt;
    const bool vpad = (r >= 24 && r < 28);
    const unsigned vconst = (r == 24) ? 0x3f803f80u : 0u;
#define W4(off, A, i0) do { f32x4 v_ = {A[i0], A[(i0) + 1], A[(i0) + 2], A[(i0) + 3]}; *(LAS f32x4*)(xw + (off)) = v_; } while (0)
#define R4(off, A, i0) do { const f32x4 v_ = *(const LAS f32x4*)(xrd + (off)); A[i0] += v_[0]; A[(i0) + 1] += v_[1]; A[(i0) + 2] += v_[2]; A[(i0) + 3] += v_[3]; } while (0)
    if (a_kh == 0) {
      W4(0, ak, 0); W4(1024, ak, 4); W4(2048, ak, 8); W4(3072, ak, 12); W4(4096, av, 8); W4(5120, av, 12);
      BAR_LGKM();
      R4(0, aq, 0); R4(1024, aq, 4); R4(2048, aq, 8); R4(3072, aq, 12); R4(4096, av, 0); R4(5120, av, 4);
      const float qs = 0.18898223650461362f * 1.4426950408889634f;
#pragma unroll
      for (int i = 0; i < 16; ++i) aq[i] *= qs;
      u32x4 f0 = pack8(aq, 0), f1 = pack8(aq, 1), vf = pack8(av, 0);
      if (h == 0) { f1[2] = 0; f1[3] = 0; }
#pragma unroll
      for (int i = 0; i < 4; ++i) vf[i] = vpad ? vconst : vf[i];
      const unsigned mo = OFF_Q + (hq * NTT + tt) * 2048 + lane * 16;
      __builtin_amdgcn_raw_buffer_store_b128(f0, rs, mo, 0, 16);
      __builtin_amdgcn_raw_buffer_store_b128(f1, rs, mo + 1024, 0, 16);
      __builtin_amdgcn_raw_buffer_store_b128(vf, rs, OFF_KV + ((hq * NTT + tt) * 4 + 2) * 1024 + lane * 16, 0, 16);
    } else {
      W4(0, aq, 0); W4(1024, aq, 4); W4(2048, aq, 8); W4(3072, aq, 12); W4(4096, av, 0); W4(5120, av, 4);
      BAR_LGKM();
      R4(0, ak, 0); R4(1024, ak, 4); R4(2048, ak, 8); R4(3072, ak, 12); R4(4096, av, 8); R4(5120, av, 12);
      u32x4 f0 = pack8(ak, 0), f1 = pack8(ak, 1), vf = pack8(av, 1);
      u32x4 f1z = f1;
      if (h == 0) { f1z[2] = 0; f1z[3] = 0; f1[2] = 0x3f803f80u; f1[3] = 0x00003f80u; }
#pragma unroll
      for (int i = 0; i < 4; ++i) vf[i] = vpad ? vconst : vf[i];
      const unsigned mo = OFF_KV + (hq * NTT + tt) * 4096 + lane * 16;
      __builtin_amdgcn_raw_buffer_store_b128(f0, rs, mo, 0, 16);
      __builtin_amdgcn_raw_buffer_store_b128(f1, rs, mo + 1024, 0, 16);
      __builtin_amdgcn_raw_buffer_store_b128(vf, rs, mo + 3072, 0, 16);
      float kn = 0.f;
#pragma unroll
      for (int i = 0; i < 4; ++i) { kn = dot2bf(f0[i], f0[i], kn); kn = dot2bf(f1z[i], f1z[i], kn); }
      kn = xhalf_add(kn);
      asm volatile("ds_max_u32 %0, %1" :: "v"(lds0 + L_KMX), "v"(__float_as_uint(kn)) : "memory");
    }
    BAR_ALL();
    if (t == 0) put_word(ws + OFF_FKV + (hq * 16 + rt) * 8, tag, *(const LAS unsigned*)(L + L_KMX));
  }
  const int head = xg * 2 + (rk >> 4), qt = rk & 15;
  const int tg = xg * 4 + (rk >> 3), sl = rk & 7;
  const int qb = wv & 3, kh = wv >> 2;
  const int tb = wv >> 2, kq = wv & 3;
  const int tt_own = tg * 2 + tb;
  const unsigned l16 = lane * 16;
  const unsigned o_off = OFF_O + ((qt * 4 + qb) * 32 + head * 2 + kh) * 1024 + l16;
  const unsigned y_off = OFF_Y + (tt_own * 28 + sl * 4 + kq) * 1024 + l16;
  const unsigned st_off = OFF_ST + ((tt_own * 28 + sl * 4 + kq) * 32 + r) * 8;
  const unsigned h_off = OFF_H + ((tg * 2) * 112 + (sl * 7 + wv) * 2) * 1024 + l16;
  unsigned w1vo[4];
#pragma unroll
  for (int i = 0; i < 4; ++i) { const int pc = (wv + 8 * i < 28) ? wv + 8 * i : wv; const int q = pc * 64 + lane, row = q / 28, c = q - row * 28; w1vo[i] = row * (FF * 2) + c * 16; }
  const u16* w1b = w1 + sl * 224;
  const lds_cptr w1rd = L + L_LOW + (4 * h + tq) * 448 + wv * 64 + g16 * 32 + tp * 8;
  const unsigned w1d3 = (wv < 4) ? 24576 : 0;
#define W1D1(c, j) do { if ((c) >= 0) dma1(w1b + (size_t)(c) * 64 * FF, w1vo[j], lds0 + L_LOW + ((c) & 1) * 28672 + wv * 1024 + ((j) < 3 ? (j) * 8192 : w1d3)); } while (0)
#define W1_DMA(c) do { W1D1(c, 0); W1D1(c, 1); W1D1(c, 2); W1D1(c, 3); } while (0)
  if (wv == 7) {
    float km = __uint_as_float(wait_words(ws + OFF_FKV + head * 128, 16, tag, lane));
#pragma unroll
    for (int m = 8; m >= 1; m >>= 1) km = fmaxf(km, __shfl_xor(km, m));
    if (lane == 0) *(LAS float*)(L + L_SM) = km;
  } else {
    const u64 t0 = __builtin_amdgcn_s_memrealtime();
    u32x4 v0, v1;
    for (;;) {
      v0 = __builtin_amdgcn_raw_buffer_load_b128(rs, OFF_MODG + 7168 + t * 16, 0, 16);
      v1 = __builtin_amdgcn_raw_buffer_load_b128(rs, OFF_MODG + 14336 + t * 16, 0, 16);
      if (__all(v0[1] == tag && v0[3] == tag && v1[1] == tag && v1[3] == tag)) break;
      if (__builtin_amdgcn_s_memrealtime() - t0 > 2000000ull) break;
      __builtin_amdgcn_s_sleep(1);
    }
    W1_DMA(0); W1_DMA(1);
    const int e = (t < 224) ? 2 * t : 2 * t - DM;
    const int pk = (e >> 4) * 32 + ((e >> 2) & 1) * 16 + ((e >> 3) & 1) * 8 + (e & 3) * 2;
    if (t < 224) {
      f32x2 a = {__uint_as_float(v0[0]), __uint_as_float(v0[2])};
      f32x2 g = {rbf(1.f + __uint_as_float(v1[0])), rbf(1.f + __uint_as_float(v1[2]))};
      *(LAS f32x2*)(L + L_GA + e * 4) = a;
      *(LAS f32x2*)(L + L_G2 + e * 4) = g;
      *(LAS unsigned*)(L + L_PK + pk) = pack2(g[0], g[1]);
    } else {
      f32x2 g = {__uint_as_float(v1[0]), __uint_as_float(v1[2])};
      *(LAS f32x2*)(L + L_GBV + e * 4) = g;
      *(LAS unsigned*)(L + L_PK + 896 + pk) = pack2(__uint_as_float(v0[0]), __uint_as_float(v0[2]));
    }
  }
  BAR_LGKM();
  const char* kvsrc = ws + OFF_KV + (size_t)(head * NTT + kh * 32 + qb) * 4096;
  const unsigned kvdst = lds0 + L_HI + kh * 16384 + qb * 4096;
#define DMA_KV(i, buf) dma4c(kvsrc + (size_t)(i) * 16384, l16, kvdst + (buf))
  bf16x8 qf0, qf1;
  {
    u32x4 q0, q1;
    asm volatile("global_load_dwordx4 %0, %2, %3 sc1\n\tglobal_load_dwordx4 %1, %2, %3 offset:1024 sc1" : "=&v"(q0), "=&v"(q1)
                 : "v"(l16), "s"(ws + OFF_Q + (head * NTT + qt * 4 + qb) * 2048) : "memory");
    DMA_KV(0, 0);
    if (wv == 7) { W1_DMA(0); W1_DMA(1); }
    DMA_KV(1, 32768);
    const float km = *(const LAS float*)(L + L_SM);
    asm volatile("s_waitcnt vmcnt(8)" : "+v"(q0), "+v"(q1) :: "memory");
    float qn = 0.f;
#pragma unroll
    for (int i = 0; i < 4; ++i) { qn = dot2bf(q0[i], q0[i], qn); qn = dot2bf(q1[i], q1[i], qn); }
    qn = xhalf_add(qn);
    const float bound = __builtin_amdgcn_sqrtf(qn * km) * 1.004f + 1e-30f;
    if (h == 0) q1[2] = ((__float_as_uint(bound) + 0xffffu) >> 16) | 0x8000u;
    qf0 = BC8(q0); qf1 = BC8(q1);
  }
  bf16x8 wf[28];
  f32x16 o, sA, sB;
  u32x4 pw0, pw1;
  bf16x8 kf0, kf1, vf0, vf1;
  float e12, e13, e14, e15;
  lds_cptr abase = L + L_HI + kh * 16384 + l16;
  lds_cptr aslot[3] = {abase, abase + 32768, abase + 65536};
  PIN(aslot[0]); PIN(aslot[1]); PIN(aslot[2]);
  BAR_VM(4);
  ATTN_PROLOGUE();
  const unsigned rvo = (lane >> 3) * (DM * 2) + (((lane & 7) ^ (((lane >> 4) & 1) << 2)) * 16);
  const int rsw = ((tq >> 1) & 1) * 64;
  const u16* wob = wo + (sl < 7 ? sl : 0) * 64;
  const int wo3 = (wv < 4) ? wv + 24 : wv;
#define W1_RD1(c, i_) (wf[4 * (c) + (i_)] = trfrag(w1rd + ((c) & 1) * 28672 + (i_) * 16 * 448, w1rd + ((c) & 1) * 28672 + ((i_) * 16 + 8) * 448))
#define W1_READ(c) do { W1_RD1(c, 0); W1_RD1(c, 1); W1_RD1(c, 2); W1_RD1(c, 3); } while (0)
#define KV1(st, j) do { if ((st) < 8) dma1c(kvsrc + (size_t)(st) * 16384 + (j) * 1024, l16, kvdst + ((st) % 3) * 32768 + (j) * 1024); } while (0)
#define W1R1(c, j) do { if ((c) >= 0) W1_RD1(((c) >= 0 ? (c) : 0), j); } while (0)
#define WO1(hf, j) do { if ((hf) >= 0) { const int pc_ = (j) < 3 ? wv + 8 * (j) : wo3; \
      dma1(wob + ((hf) * 28 + pc_) * 8 * DM, rvo, lds0 + L_HI + ((hf) ? 0 : 65536) + pc_ * 1024); } } while (0)
#define STAGE(st, NW, RA, RB, RC, DC, WH, TCH) do { \
    const lds_cptr bc = aslot[(st) % 3], bn = aslot[((st) + 1) % 3]; \
    ASTEPH(sA, sB, bc + 2 * 4096, bc + 0 * 4096 + 3072, bc + 1 * 4096 + 2048, W1R1(RA, 0), W1R1(RA, 1), W1R1(RA, 2), W1R1(RA, 3)); \
    ASTEPH(sB, sA, bc + 3 * 4096, bc + 1 * 4096 + 3072, bc + 2 * 4096 + 2048, W1R1(RB, 0), W1R1(RB, 1), W1R1(RB, 2), W1R1(RB, 3)); \
    BAR_VM(NW); \
    ASTEPH(sA, sB, bn + 0 * 4096, bc + 2 * 4096 + 3072, bc + 3 * 4096 + 2048, KV1((st) + 2, 0); WO1(WH, 0); W1R1(RC, 0), KV1((st) + 2, 1); WO1(WH, 1); W1R1(RC, 1), \
           KV1((st) + 2, 2); WO1(WH, 2); W1R1(RC, 2), KV1((st) + 2, 3); WO1(WH, 3); W1R1(RC, 3)); \
    ASTEPH(sB, sA, bn + 1 * 4096, bc + 3 * 4096 + 3072, bn + 0 * 4096 + 2048, W1D1(DC, 0), W1D1(DC, 1), W1D1(DC, 2), W1D1(DC, 3); TCH); \
  } while (0)
  const int tl = wv * 9 + (lane < 9 ? lane : 8);
#define TOUCH_WO(i) touch1(wo, (rk * 98 + ((i) * 72 + tl < 98 ? (i) * 72 + tl : 97)) * 128, lds0 + L_SM + 256)
#define TOUCH_W2(i) do { const int li_ = (rk & 7) * 168 + ((i) * 72 + tl < 168 ? (i) * 72 + tl : 167); touch1(w2, ((rk >> 3) * 3136 + li_) * 128, lds0 + L_SM + 256); } while (0)
  STAGE(0, 0,  0,  1, -1,  2, -1, TOUCH_WO(0));
  STAGE(1, 5, -1, -1, -1,  3, -1, TOUCH_WO(1));
  STAGE(2, 5, -1, -1,  2, -1, -1, TOUCH_W2(0));
  STAGE(3, 1, -1, -1,  3,  4, -1, NOH);
  STAGE(4, 4, -1, -1, -1,  5, -1, NOH);
  STAGE(5, 4, -1, -1,  4, -1, -1, TOUCH_W2(1); TOUCH_W2(2));
  STAGE(6, 2, -1, -1,  5,  6,  0, NOH);
  STAGE(7, 8, -1, -1, -1, -1,  1, NOH);
  float fin[8];
  float lsum;
  constexpr int L_X = L_LOW + 28672;
#pragma unroll 1
  for (int pass = 0; pass < 2; ++pass) {
    pw1[2] = pack2(e12, e13); pw1[3] = pack2(e14, e15);
    o = MFMA32(vf1, BC8(pw1), o);
    {
      f32x4 a, b2;
      if (kh) {
#pragma unroll
        for (int i = 0; i < 4; ++i) { a[i] = o[i]; b2[i] = o[4 + i]; fin[i] = o[8 + i]; fin[4 + i] = o[12 + i]; }
      } else {
#pragma unroll
        for (int i = 0; i < 4; ++i) { a[i] = o[8 + i]; b2[i] = o[12 + i]; fin[i] = o[i]; fin[4 + i] = o[4 + i]; }
      }
      *(LAS f32x4*)(L + L_X + (wv * 2 + 0) * 1024 + l16) = a;
      *(LAS f32x4*)(L + L_X + (wv * 2 + 1) * 1024 + l16) = b2;
      if (h == 0) *(LAS float*)(L + L_X + 16384 + (wv * 32 + r) * 4) = o[12];
    }
    BAR_LGKM();
    {
      const int pw = wv ^ 4;
      const f32x4 a = *(const LAS f32x4*)(L + L_X + (pw * 2 + 0) * 1024 + l16);
      const f32x4 b2 = *(const LAS f32x4*)(L + L_X + (pw * 2 + 1) * 1024 + l16);
      const float c0 = *(const LAS float*)(L + L_X + 16384 + lane * 4) + *(const LAS float*)(L + L_X + 16384 + (128 + lane) * 4);
      const float c1 = *(const LAS float*)(L + L_X + 16384 + (64 + lane) * 4) + *(const LAS float*)(L + L_X + 16384 + (192 + lane) * 4);
#pragma unroll
      for (int i = 0; i < 4; ++i) { fin[i] += a[i]; fin[4 + i] += b2[i]; }
      lsum = *(const LAS float*)(L + L_X + 16384 + (qb * 32 + r) * 4) + *(const LAS float*)(L + L_X + 16384 + ((qb + 4) * 32 + r) * 4);
      const bool ok = (c0 >= 8.271806e-25f) && (c1 >= 8.271806e-25f);
      if (pass == 1 || __all(ok)) break;
    }
    {
      u32x4 q1 = __builtin_bit_cast(u32x4, qf1);
      if (h == 0) { q1[2] = 0; q1[3] = 0; }
      qf1 = BC8(q1);
      float mx = -INFINITY;
      const unsigned ko = OFF_KV + (head * NTT + kh * 32) * 4096 + l16;
#pragma unroll 1
      for (int kt = 0; kt < 32; ++kt) {
        const u32x4 k0 = __builtin_amdgcn_raw_buffer_load_b128(rs, ko + kt * 4096, 0, 16);
        const u32x4 k1 = __builtin_amdgcn_raw_buffer_load_b128(rs, ko + kt * 4096 + 1024, 0, 16);
        f32x16 s = MFMA32(BC8(k0), qf0, zero16);
        s = MFMA32(BC8(k1), qf1, s);
#pragma unroll
        for (int i = 0; i < 16; ++i) mx = fmaxf(mx, s[i]);
      }
      mx = xhalf_max(mx);
      if (h == 0) *(LAS float*)(L + L_X + 20480 + (wv * 32 + r) * 4) = mx;
      BAR_ALL();
      mx = fmaxf(*(const LAS float*)(L + L_X + 20480 + (qb * 32 + r) * 4), *(const LAS float*)(L + L_X + 20480 + ((qb + 4) * 32 + r) * 4));
      const float m0 = rbf(mx), m1 = rbf(mx - m0), m2 = rbf(mx - m0 - m1);
      if (h == 0) { q1[2] = pack2(-m0, -m1); q1[3] = pack2(-m2, 0.f); }
      qf1 = BC8(q1);
      DMA_KV(0, 0);
      DMA_KV(1, 32768);
      asm volatile("s_waitcnt vmcnt(4) lgkmcnt(0)\n\ts_barrier" ::: "memory");
      ATTN_PROLOGUE();
      int cur = 0, nxt = 32768, nn = 65536;
#pragma unroll 1
      for (int st = 0; st < 8; ++st) {
        const lds_cptr bc = abase + cur, bn = abase + nxt;
        ASTEP(sA, sB, bc + 2 * 4096, bc + 0 * 4096 + 3072, bc + 1 * 4096 + 2048);
        ASTEP(sB, sA, bc + 3 * 4096, bc + 1 * 4096 + 3072, bc + 2 * 4096 + 2048);
        BAR_VM(0);
        if (st + 2 < 8) DMA_KV(st + 2, nn);
        if (st == 7) { WO1(0, 0); WO1(0, 1); WO1(0, 2); WO1(0, 3); WO1(1, 0); WO1(1, 1); WO1(1, 2); WO1(1, 3); }
        ASTEP(sA, sB, bn + 0 * 4096, bc + 2 * 4096 + 3072, bc + 3 * 4096 + 2048);
        ASTEP(sB, sA, bn + 1 * 4096, bc + 3 * 4096 + 3072, bn + 0 * 4096 + 2048);
        const int tmp = cur; cur = nxt; nxt = nn; nn = tmp;
      }
    }
  }
  {
    const float inv = __builtin_amdgcn_rcpf(lsum);
    u32x4 ov;
#pragma unroll
    for (int i = 0; i < 4; ++i) ov[i] = pack2(fin[2 * i] * inv, fin[2 * i + 1] * inv);
    if (kh == 1 && h == 0) { ov[2] = 0; ov[3] = 0; }
    __builtin_amdgcn_raw_buffer_store_b128(ov, rs, o_off, 0, 16);
    BAR_ALL();
    if (t == 0) put_word(ws + OFF_FO + (qt * 16 + head) * 8, tag, 0);
  }
  const int n0 = (sl * 2 + (kq >> 1)) * 32 + (kq & 1) * 16 + 4 * h;
  u32x2 xa2, xb2;
  unsigned xc0;
  asm volatile("global_load_dwordx2 %0, %3, %5\n\tglobal_load_dwordx2 %1, %3, %5 offset:16\n\tglobal_load_ushort %2, %4, %5"
               : "=&v"(xa2), "=&v"(xb2), "=&v"(xc0) : "v"((r * DM + (sl < 7 ? n0 : 0)) * 2), "v"(r * DM * 2), "s"(x + (size_t)tt_own * 32 * DM) : "memory");
  W1_READ(6);
  f32x16 uv = zero16;
  if (wv < 7) {
    const lds_cptr pp = L + L_PK + g16 * 896 + h * 16;
    bf16x8 pf[14];
#pragma unroll
    for (int hf2 = 0; hf2 < 2; ++hf2) {
#pragma unroll
      for (int i = 0; i < 14; ++i) pf[i] = ldsfrag(pp + (hf2 * 14 + i) * 32);
      SBAR();
#pragma unroll
      for (int i = 0; i < 14; ++i) uv = MFMA32(wf[hf2 * 14 + i], pf[i], uv);
      PIN(uv); SBAR();
    }
  }
  if (wv == 7 && sl < 7) wait_words(ws + OFF_FO + (tg >> 1) * 128, 16, tag, lane);
  asm volatile("s_waitcnt vmcnt(0) lgkmcnt(0)\n\ts_barrier" : "+v"(xa2), "+v"(xb2), "+v"(xc0) :: "memory");
  const lds_ptr redb = L + L_LOW + tb * 24576 + l16;
#define RW1(A, o8, p, KQ) do { f32x4 v0 = {A[o8], A[(o8) + 1], A[(o8) + 2], A[(o8) + 3]}, v1 = {A[(o8) + 4], A[(o8) + 5], A[(o8) + 6], A[(o8) + 7]}; \
        *(LAS f32x4*)(redb + (((p) * 3 + (((KQ) - (p) - 1) & 3)) * 2) * 1024) = v0; *(LAS f32x4*)(redb + (((p) * 3 + (((KQ) - (p) - 1) & 3)) * 2 + 1) * 1024) = v1; } while (0)
#define RKEEP(AT, A, o8) do { _Pragma("unroll") for (int i = 0; i < 8; ++i) AT[i] = A[(o8) + i]; } while (0)
#define RED_WRITE(AT, A0, A1) do { \
      if (kq == 0)      { RW1(A0, 8, 1, 0); RW1(A1, 0, 2, 0); RW1(A1, 8, 3, 0); RKEEP(AT, A0, 0); } \
      else if (kq == 1) { RW1(A0, 0, 0, 1); RW1(A1, 0, 2, 1); RW1(A1, 8, 3, 1); RKEEP(AT, A0, 8); } \
      else if (kq == 2) { RW1(A0, 0, 0, 2); RW1(A0, 8, 1, 2); RW1(A1, 8, 3, 2); RKEEP(AT, A1, 0); } \
      else              { RW1(A0, 0, 0, 3); RW1(A0, 8, 1, 3); RW1(A1, 0, 2, 3); RKEEP(AT, A1, 8); } } while (0)
#define RED_READ(AT) do { \
      _Pragma("unroll") for (int d = 0; d < 3; ++d) { \
        const f32x4 v0 = *(const LAS f32x4*)(redb + kq * 6144 + (d * 2) * 1024); \
        const f32x4 v1 = *(const LAS f32x4*)(redb + kq * 6144 + (d * 2 + 1) * 1024); \
        _Pragma("unroll") for (int i = 0; i < 4; ++i) { AT[i] += v0[i]; AT[4 + i] += v1[i]; } } } while (0)
  const u16* w2b = w2 + (sl < 7 ? sl : 0) * 64;
#define W2A(i) dma1(w2b + (size_t)((wv >> 1) * 448 + ((wv & 1) * 12 + (i)) * 8) * DM, rvo, lds0 + L_HI + (wv * 12 + (i)) * 1024)
  u32x4 x1p;
  if (sl < 7) {
    float x1[8];
    f32x16 acc0 = zero16, acc1 = zero16;
    {
      const lds_cptr wop = L + L_HI + ((kq >> 1) ? 0 : 65536) + ((kq & 1) * 4 * DH + 4 * h + tq) * 128 + g16 * 32 + tp * 8;
      const lds_cptr p0 = wop + rsw, p1 = wop + (64 ^ rsw);
      bf16x8 wa0[4], wa1[4];
#define WOROW0(i) (((i) >> 1) * DH + 16 * ((i) & 1))
#define WOROW1(i) (((i) >> 1) * DH + (((i) & 1) ? 20 : 8))
      u32x4 ob[8];
      const unsigned oo = OFF_O + (tt_own * 32 + kq * 8) * 1024 + l16;
#pragma unroll
      for (int i = 0; i < 8; ++i) ob[i] = __builtin_amdgcn_raw_buffer_load_b128(rs, oo + i * 1024, 0, 16);
      SBAR();
#pragma unroll
      for (int i = 0; i < 4; ++i) {
        wa0[i] = trfrag(p0 + WOROW0(i) * 128, p0 + WOROW1(i) * 128);
        wa1[i] = trfrag(p1 + WOROW0(i) * 128, p1 + WOROW1(i) * 128);
      }
      SBAR();
#pragma unroll
      for (int i = 0; i < 8; ++i) {
        acc0 = MFMA32(wa0[i & 3], BC8(ob[i]), acc0); acc1 = MFMA32(wa1[i & 3], BC8(ob[i]), acc1); PIN(acc0); PIN(acc1); SBAR();
        if (i < 4) {
          wa0[i] = trfrag(p0 + WOROW0(i + 4) * 128, p0 + WOROW1(i + 4) * 128);
          wa1[i] = trfrag(p1 + WOROW0(i + 4) * 128, p1 + WOROW1(i + 4) * 128);
        }
        SBAR();
      }
    }
    float at[8];
    RED_WRITE(at, acc0, acc1);
    const f32x4 ga0 = *(const LAS f32x4*)(L + L_GA + n0 * 4), ga1 = *(const LAS f32x4*)(L + L_GA + (n0 + 8) * 4);
    const f32x4 g20 = *(const LAS f32x4*)(L + L_G2 + n0 * 4), g21 = *(const LAS f32x4*)(L + L_G2 + (n0 + 8) * 4);
    BAR_LGKM();
    RED_READ(at);
    x1[0] = bflo(xa2[0]) + ga0[0] * at[0]; x1[1] = bfhi(xa2[0]) + ga0[1] * at[1];
    x1[2] = bflo(xa2[1]) + ga0[2] * at[2]; x1[3] = bfhi(xa2[1]) + ga0[3] * at[3];
    x1[4] = bflo(xb2[0]) + ga1[0] * at[4]; x1[5] = bfhi(xb2[0]) + ga1[1] * at[5];
    x1[6] = bflo(xb2[1]) + ga1[2] * at[6]; x1[7] = bfhi(xb2[1]) + ga1[3] * at[7];
#pragma unroll
    for (int i = 0; i < 4; ++i) x1p[i] = pack2(x1[2 * i], x1[2 * i + 1]);
    PIN(x1p);
    const float cshift = __uint_as_float(xc0 << 16);
    float p1 = 0.f, p2 = 0.f, y[8];
#pragma unroll
    for (int i = 0; i < 8; ++i) {
      const float d = x1[i] - cshift;
      p1 += d; p2 += d * d;
      y[i] = d * (i < 4 ? g20[i & 3] : g21[i & 3]);
    }
    u32x4 yv;
#pragma unroll
    for (int i = 0; i < 4; ++i) yv[i] = pack2(y[2 * i], y[2 * i + 1]);
    __builtin_amdgcn_raw_buffer_store_b128(yv, rs, y_off, 0, 16);
    p1 = xhalf_add(p1); p2 = xhalf_add(p2);
    {
      u32x2 sv = {__float_as_uint(p1), __float_as_uint(p2)};
      __builtin_amdgcn_raw_buffer_store_b64(sv, rs, st_off, 0, 16);
    }
    BAR_ALL();
    if (t == 0) put_word(ws + OFF_FY + tg * 128 + sl * 8, tag, 0);
    if (b == 0 && t == 64) __hip_atomic_store((gu64*)ws, epoch + 1ull, RLX_AGENT);
    if (wv < 7) { W2A(0); W2A(1); W2A(2); W2A(3); W2A(4); W2A(5); W2A(6); W2A(7); W2A(8); W2A(9); W2A(10); W2A(11); }
  }
  f32x16 u2, v2;
#pragma unroll
  for (int i = 0; i < 16; ++i) {
    auto rr = __builtin_amdgcn_permlane16_swap(__float_as_uint(uv[i]), __float_as_uint(uv[i]), false, false);
    u2[i] = __uint_as_float(rr[0]); v2[i] = __uint_as_float(rr[1]) * -1.4426950408889634f;
  }
  PIN(u2); PIN(v2);
  if (wv == 7) wait_words(ws + OFF_FY + tg * 128, 7, tag, lane);
  BAR_LGKM();
  {
    if (wv < 7) {
      const char* ysrc = ws + OFF_Y + (size_t)tg * 57344 + wv * 8192;
      dma4c(ysrc, l16, lds0 + L_LOW + wv * 8192); dma4c(ysrc + 4096, l16, lds0 + L_LOW + wv * 8192 + 4096);
    } else {
      u32x2 sv[28];
#pragma unroll
      for (int i = 0; i < 28; ++i) sv[i] = __builtin_amdgcn_raw_buffer_load_b64(rs, OFF_ST + (((tg * 2 + h) * 28 + i) * 32 + r) * 8, 0, 16);
      float a1 = 0.f, a2 = 0.f;
#pragma unroll
      for (int i = 0; i < 28; ++i) { a1 += __uint_as_float(sv[i][0]); a2 += __uint_as_float(sv[i][1]); }
      const float m = a1 * (1.f / DM);
      const float var = fmaxf(a2 * (1.f / DM) - m * m, 0.f);
      f32x2 o2 = {__builtin_amdgcn_rsqf(var + 1e-5f) * -1.4426950408889634f, m};
      *(LAS f32x2*)(L + L_SM + 512 + lane * 8) = o2;
    }
  }
  BAR_ALL();
  f32x16 c0 = zero16, c1 = zero16;
  f32x2 st0, st1;
  u32x4 gbp;
#define SILU1(C, ST, i) do { const float t_ = __builtin_fmaf(-ST[1], u2[i], C[i]); const float q_ = __builtin_fmaf(ST[0], t_, v2[i]); \
      C[i] = q_ * __builtin_amdgcn_rcpf(1.f + EX2(q_)); } while (0)
  {
    const f32x4 gb0 = *(const LAS f32x4*)(L + L_GBV + (sl < 7 ? n0 : 0) * 4), gb1 = *(const LAS f32x4*)(L + L_GBV + ((sl < 7 ? n0 : 0) + 8) * 4);
    gbp[0] = pack2(gb0[0], gb0[1]); gbp[1] = pack2(gb0[2], gb0[3]); gbp[2] = pack2(gb1[0], gb1[1]); gbp[3] = pack2(gb1[2], gb1[3]); PIN(gbp);
  }
  const int rowoff = (4 * h + tq) * 128 + g16 * 32 + tp * 8;
  const lds_cptr hi0 = L + L_HI + kq * 24576 + rowoff + rsw, hi1 = L + L_HI + kq * 24576 + rowoff + (64 ^ rsw);
  bf16x8 r0[12], r1[12];
  {
    const lds_cptr yp = L + L_LOW + l16;
    st0 = *(const LAS f32x2*)(L + L_SM + 512 + r * 8); st1 = *(const LAS f32x2*)(L + L_SM + 512 + (32 + r) * 8);
    bf16x8 yb[4];
    if (wv < 7) {
#pragma unroll
      for (int i = 0; i < 4; ++i) yb[i] = ldsfrag(yp + i * 1024);
      SBAR();
#pragma unroll
      for (int ks = 0; ks < 28; ++ks) {
        c0 = MFMA32(wf[ks], yb[ks & 3], c0); PIN(c0); SBAR();
        yb[ks & 3] = ldsfrag(yp + (ks + 4) * 1024);
        SBAR();
      }
    } else {
      W2A(0); W2A(1); W2A(2); W2A(3); W2A(4); W2A(5); W2A(6); W2A(7); W2A(8); W2A(9); W2A(10); W2A(11);
      const unsigned to = (192 + (rk >> 3) * 64 + lane) * (DM * 2);
#pragma unroll
      for (int q4 = 0; q4 < 4; ++q4) touch1(w2b + q4 * 448 * DM, to, lds0 + L_SM + 256);
    }
    if (wv < 7) {
#pragma unroll
      for (int ks = 0; ks < 28; ++ks) {
        c1 = MFMA32(wf[ks], yb[ks & 3], c1); PIN(c1); SBAR();
        if (ks + 4 < 28) yb[ks & 3] = ldsfrag(yp + (28 + ks + 4) * 1024);
        if (ks >= 1 && ks < 17) { SILU1(c0, st0, ks - 1); PIN(c0); }
        if (ks == 18) __builtin_amdgcn_raw_buffer_store_b128(pack8(c0, 0), rs, h_off, 0, 16);
        if (ks == 19) __builtin_amdgcn_raw_buffer_store_b128(pack8(c0, 1), rs, h_off + 1024, 0, 16);
        SBAR();
      }
    }
  }
  BAR_ALL();
  if (t == 0) put_word(ws + OFF_FH + tg * 128 + sl * 8, tag, 0);
#pragma unroll
  for (int j = 0; j < 12; ++j) { r0[j] = trfrag(hi0 + j * 2048, hi0 + j * 2048 + 1024); r1[j] = trfrag(hi1 + j * 2048, hi1 + j * 2048 + 1024); }
  SBAR();
  if (wv < 7) {
#pragma unroll
    for (int i = 0; i < 8; ++i) SILU1(c1, st1, i);
    __builtin_amdgcn_raw_buffer_store_b128(pack8(c1, 0), rs, h_off + 112 * 1024, 0, 16);
#pragma unroll
    for (int i = 8; i < 16; ++i) SILU1(c1, st1, i);
    __builtin_amdgcn_raw_buffer_store_b128(pack8(c1, 1), rs, h_off + 113 * 1024, 0, 16);
  }
  BAR_ALL();
  if (t == 0) put_word(ws + OFF_FH + tg * 128 + 64 + sl * 8, tag, 0);
  if (sl == 7) return;
  {
#pragma unroll
    for (int i = 0; i < 16; ++i)
      dma1(w2b + (size_t)((wv >> 1) * 448 + 192 + ((wv & 1) * 16 + i) * 8) * DM, rvo, lds0 + (wv * 16 + i) * 1024);
    const unsigned ho = OFF_H + (tt_own * 112 + kq * 28) * 1024;
    f32x16 d0, d1;
    const lds_cptr lo0 = L + kq * 32768 + rowoff + rsw, lo1 = L + kq * 32768 + rowoff + (64 ^ rsw);
    if (tb) BAR_VM(0);
    wait_words(ws + OFF_FH + tg * 128 + tb * 64, 8, tag, lane);
    u32x4 hb[28];
#pragma unroll
    for (int i = 0; i < 18; ++i) hb[i] = __builtin_amdgcn_raw_buffer_load_b128(rs, ho + l16 + i * 1024, 0, 16);
    SBAR();
    d0 = zero16; d1 = zero16;
#pragma unroll
    for (int i = 0; i < 12; ++i) {
      d0 = MFMA32(r0[i], BC8(hb[i]), d0); PIN(d0); SBAR();
      d1 = MFMA32(r1[i], BC8(hb[i]), d1); PIN(d1); SBAR();
      if (i >= 1 && i < 11) hb[17 + i] = __builtin_amdgcn_raw_buffer_load_b128(rs, ho + l16 + (17 + i) * 1024, 0, 16);
      SBAR();
    }
    if (!tb) asm volatile("s_barrier" ::: "memory");
    {
      bf16x8 a0[4], a1[4];
#pragma unroll
      for (int i = 0; i < 4; ++i) { a0[i] = trfrag(lo0 + i * 2048, lo0 + i * 2048 + 1024); a1[i] = trfrag(lo1 + i * 2048, lo1 + i * 2048 + 1024); }
      SBAR();
#pragma unroll
      for (int i = 0; i < 16; ++i) {
        d0 = MFMA32(a0[i & 3], BC8(hb[12 + i]), d0); PIN(d0); SBAR();
        if (i + 4 < 16) a0[i & 3] = trfrag(lo0 + (i + 4) * 2048, lo0 + (i + 4) * 2048 + 1024);
        SBAR();
        d1 = MFMA32(a1[i & 3], BC8(hb[12 + i]), d1); PIN(d1); SBAR();
        if (i + 4 < 16) a1[i & 3] = trfrag(lo1 + (i + 4) * 2048, lo1 + (i + 4) * 2048 + 1024);
        SBAR();
      }
    }
    BAR_LGKM();
    float f[8];
    RED_WRITE(f, d0, d1);
    BAR_LGKM();
    RED_READ(f);
#pragma unroll
    for (int i = 0; i < 8; ++i) f[i] *= -0.6931471805599453f;
    u32x2 oa, ob2;
    oa[0] = pack2(bflo(x1p[0]) + bflo(gbp[0]) * f[0], bfhi(x1p[0]) + bfhi(gbp[0]) * f[1]);
    oa[1] = pack2(bflo(x1p[1]) + bflo(gbp[1]) * f[2], bfhi(x1p[1]) + bfhi(gbp[1]) * f[3]);
    ob2[0] = pack2(bflo(x1p[2]) + bflo(gbp[2]) * f[4], bfhi(x1p[2]) + bfhi(gbp[2]) * f[5]);
    ob2[1] = pack2(bflo(x1p[3]) + bflo(gbp[3]) * f[6], bfhi(x1p[3]) + bfhi(gbp[3]) * f[7]);
    u16* orow = out + (size_t)(tt_own * 32 + r) * DM;
    *(u32x2*)(orow + n0) = oa;
    *(u32x2*)(orow + n0 + 8) = ob2;
  }
}

extern "C" void kernel_launch(void* const* d_in, const int* in_sizes, int n_in, void* d_out, int out_size, void* d_ws,
                              size_t ws_size, hipStream_t stream) {
  fused_block<<<256, 512, 0, stream>>>((const u16*)d_in[0], (const u16*)d_in[1], (const u16*)d_in[2], (const u16*)d_in[4],
                                       (const u16*)d_in[5], (const u16*)d_in[6], (const u16*)d_in[7], (u16*)d_out, (char*)d_ws);
}
```
